```python
import torch

def _softmax(x, axis=-1):
    return torch.softmax(x, dim=axis)
def _gelu(x):
    return torch.nn.functional.gelu(x, approximate="tanh")
def _silu(x):
    return torch.nn.functional.silu(x)
def _sigmoid(x):
    return torch.sigmoid(x)
def _layernorm(x, axis=-1, eps=1e-5):
    m = x.mean(dim=axis, keepdim=True)
    v = x.var(dim=axis, keepdim=True, unbiased=False)
    return (x - m) / torch.sqrt(v + eps)
def _scatter_add(tgt, idx, src, axis=0):
    return tgt.clone().index_add_(axis, idx, src)
def _segment_sum(src, seg, n):
    out = torch.zeros((n,) + src.shape[1:], dtype=src.dtype)
    return out.index_add_(0, seg, src)
def _pad(x, pw):
    flat = tuple(v for pair in reversed(pw) for v in pair)
    return torch.nn.functional.pad(x, flat)

kernel_name = 'standalone__adm_resblock__diffusion'

def setup_inputs():

    _w = (lambda a: (lambda t: t.to(torch.bfloat16) if t.is_floating_point() else t)(torch.as_tensor(a)))
    B, C, HW, E = 2, 128, 128, 768
    sc = (9.0 * C) ** -0.5
    return {
        "x": _w((lambda *s: torch.randn(*s, dtype=torch.bfloat16) * 0.05)(B, C, HW, HW)),
        "t_emb": _w((lambda *s: torch.randn(*s, dtype=torch.bfloat16) * 0.4)(B, E)),
        "g1": _w(1.0 + 0.1 * (lambda *s: torch.randn(*s, dtype=torch.bfloat16) * 0.4)(C)),
        "be1": _w(0.1 * (lambda *s: torch.randn(*s, dtype=torch.bfloat16) * 0.4)(C)),
        "w1": _w((lambda *s: torch.randn(*s, dtype=torch.bfloat16) * 0.4)(C, C, 3, 3) * sc),
        "w_emb": _w((lambda *s: torch.randn(*s, dtype=torch.bfloat16) * 0.4)(E, C) * (E ** -0.5)),
        "g2": _w(1.0 + 0.1 * (lambda *s: torch.randn(*s, dtype=torch.bfloat16) * 0.4)(C)),
        "be2": _w(0.1 * (lambda *s: torch.randn(*s, dtype=torch.bfloat16) * 0.4)(C)),
        "w2": _w((lambda *s: torch.randn(*s, dtype=torch.bfloat16) * 0.4)(C, C, 3, 3) * sc),
    }

def reference(x, t_emb, g1, be1, w1, w_emb, g2, be2, w2):
    B, C, H, W = x.shape
    G, eps = 4, 1e-5
    def gn(z, gamma, beta):
        zr = z.reshape(B, G, (C // G) * H * W)
        mu = zr.mean(-1).reshape(B, G, 1)
        var = ((zr - mu) ** 2).mean(-1).reshape(B, G, 1)
        zn = ((zr - mu) / torch.sqrt(var + eps)).reshape(B, C, H, W)
        return zn * gamma.reshape(1, C, 1, 1) + beta.reshape(1, C, 1, 1)
    def conv3(z, w):
        zp = _pad(z, ((0, 0), (0, 0), (1, 1), (1, 1)))
        taps = [torch.einsum("bchw,oc->bohw", zp[:, :, i:i+H, j:j+W], w[:, :, i, j]) for i in range(3) for j in range(3)]
        out = taps[0]
        for t in taps[1:]:
            out = out + t
        return out
    h = conv3(_silu(gn(x, g1, be1)), w1)
    emb = _silu(t_emb) @ w_emb
    h = h + emb.reshape(B, C, 1, 1)
    h = conv3(_silu(gn(h, g2, be2)), w2)
    return x + h
```

```cpp
#include <hip/hip_runtime.h>
#include <hip/hip_bf16.h>
#include <stdint.h>

typedef float f32x4 __attribute__((ext_vector_type(4)));
typedef float f32x2 __attribute__((ext_vector_type(2)));
typedef __bf16 bf16x8 __attribute__((ext_vector_type(8)));
typedef __bf16 bf16x2 __attribute__((ext_vector_type(2)));
typedef unsigned u32x4 __attribute__((ext_vector_type(4)));
typedef unsigned u32x2 __attribute__((ext_vector_type(2)));
typedef unsigned short u16;
#define GAS __attribute__((address_space(1)))
#define DI __device__ __forceinline__
#define SB() __builtin_amdgcn_sched_barrier(0)

constexpr int RP = 4608;
constexpr int TILE_BYTES = 10 * RP;
constexpr int WD = 18;
constexpr float LOG2E = 1.4426950408889634f;
constexpr int SC1 = 16;
constexpr int SPIN_MAX = 1 << 16;

constexpr unsigned WB_SIZE = 1u << 20;
constexpr unsigned WB_WF = 0, WB_EPOCH = 589824, WB_FLAG = WB_EPOCH + 256  , WB_G1 = WB_FLAG + 1024, WB_G2 = WB_G1 + 8192,
                   WB_EG = WB_G2 + 8192  , WB_CF = WB_EG + 4096  , WB_XC = WB_CF + 512  ;
constexpr unsigned WS_HB = 2u << 20;
constexpr int RS = 8224;
constexpr int L_T = 0, L_RAW = TILE_BYTES, L_GB = L_RAW + 10 * RS  , L_PAR = L_GB + 2048  , L_EMB = L_PAR + 1024,
              L_STAT = L_EMB + 512  , L_DUMMY = (L_STAT + 64 + 255) / 256 * 256  , L_END = L_DUMMY + 1024;
#define LAS __attribute__((address_space(3)))

DI int swz(int c) { return c ^ ((c >> 1) & 4); }
DI unsigned cvtpk(float lo, float hi) { f32x2 v = {lo, hi}; bf16x2 b = __builtin_convertvector(v, bf16x2); return __builtin_bit_cast(unsigned, b); }
DI unsigned cvtpk_s(float lo, float hi) { unsigned r; asm("v_cvt_pk_bf16_f32 %0, %1, %2" : "=v"(r) : "v"(lo), "v"(hi)); return r; }
DI float bflo(unsigned d) { return __uint_as_float(d << 16); }
DI float bfhi(unsigned d) { return __uint_as_float(d & 0xffff0000u); }
DI float dot2(unsigned a, unsigned b, float c) { return __builtin_amdgcn_fdot2_f32_bf16(__builtin_bit_cast(bf16x2, a), __builtin_bit_cast(bf16x2, b), c, false); }
template <int CTRL> DI float dppadd(float v) { return v + __int_as_float(__builtin_amdgcn_update_dpp(0, __float_as_int(v), CTRL, 0xf, 0xf, true)); }
DI float wave_sum(float v) {
  v = dppadd<0xB1>(v); v = dppadd<0x4E>(v); v = dppadd<0x141>(v); v = dppadd<0x140>(v);
  auto p = __builtin_amdgcn_permlane16_swap(__float_as_uint(v), __float_as_uint(v), false, false);
  v = __uint_as_float(p[0]) + __uint_as_float(p[1]);
  auto q = __builtin_amdgcn_permlane32_swap(__float_as_uint(v), __float_as_uint(v), false, false);
  return __uint_as_float(q[0]) + __uint_as_float(q[1]);
}
DI void bar() { asm volatile("s_waitcnt lgkmcnt(0)\n\ts_barrier" ::: "memory"); }
DI void bar_nowait() { asm volatile("s_barrier" ::: "memory"); }
DI void cbar() { asm volatile("" ::: "memory"); }
typedef __amdgpu_buffer_rsrc_t rsrc_t;
DI rsrc_t mk_rsrc(const void* p, unsigned bytes) { return __builtin_amdgcn_make_buffer_rsrc((void*)p, 0, bytes, 0x00020000); }
DI u32x4 bld(rsrc_t r, unsigned voff, unsigned soff) { return __builtin_amdgcn_raw_buffer_load_b128(r, voff, soff, 0); }
DI u32x4 bld_sc1(rsrc_t r, unsigned voff, unsigned soff) { return __builtin_amdgcn_raw_buffer_load_b128(r, voff, soff, SC1); }
DI void bst_sc1(u32x4 v, rsrc_t r, unsigned voff, unsigned soff) { __builtin_amdgcn_raw_buffer_store_b128(v, r, voff, soff, SC1); }

struct Ctx {
  rsrc_t rx, rw;
  char* smem; char* ws;
  int b, ty, tx, jb, y0, x0, w, lane, t;
  unsigned wb;
  unsigned tag;
};

#define STAMP_DECL
#define STAMP(idx) do {} while (0)
#define STAMP_FLUSH do {} while (0)

DI void stat_par(const char* smem, int gbsel, int grp, int ch, f32x4& a, f32x4& b) {
  const f32x4 g = *(const f32x4*)(smem + L_GB + gbsel * 1024 + ch * 4), be = *(const f32x4*)(smem + L_GB + gbsel * 1024 + 512 + ch * 4);
  const f32x2 st = *(const f32x2*)(smem + L_STAT + grp * 8);
  a = g * (st[1] * LOG2E); b = be * LOG2E - a * st[0];
}
DI u32x2 act4_packed(u32x2 v, f32x4 a, f32x4 b) {
  u32x2 o;
#pragma unroll
  for (int m = 0; m < 2; ++m) {
    f32x2 xx = {bflo(v[m]), bfhi(v[m])}, aa = {a[2 * m], a[2 * m + 1]}, bb = {b[2 * m], b[2 * m + 1]};
    f32x2 u = xx * aa + bb;
    f32x2 e = {__builtin_amdgcn_exp2f(-u[0]), __builtin_amdgcn_exp2f(-u[1])};
    f32x2 d = e * LOG2E + LOG2E;
    f32x2 r = {__builtin_amdgcn_rcpf(d[0]), __builtin_amdgcn_rcpf(d[1])};
    f32x2 y = u * r;
    o[m] = cvtpk(y[0], y[1]);
  }
  return o;
}
DI float act1(float x, float a, float b) { float u = __builtin_fmaf(x, a, b); float e = __builtin_amdgcn_exp2f(-u); return u * __builtin_amdgcn_rcpf(__builtin_fmaf(e, LOG2E, LOG2E)); }
DI u32x2 act4_scalar(u32x2 v, f32x4 a, f32x4 b) {
  u32x2 o;
  o[0] = cvtpk_s(act1(bflo(v[0]), a[0], b[0]), act1(bfhi(v[0]), a[1], b[1]));
  o[1] = cvtpk_s(act1(bflo(v[1]), a[2], b[2]), act1(bfhi(v[1]), a[3], b[3]));
  return o;
}
DI unsigned ntask(const Ctx& c, unsigned px, int c4) {
  const unsigned row = px / 18, col = px - row * 18;
  const unsigned off = L_T + row * RP + col * 256 + (((c4 >> 1) ^ swz(col & 15)) << 4) + (c4 & 1) * 8;
  const bool valid = px < 180 && (unsigned)(c.y0 - 1 + (int)row) < 128u && (unsigned)(c.x0 - 1 + (int)col) < 128u;
  unsigned r = valid ? off : (unsigned)(L_DUMMY + c.lane * 16 + (c4 & 1) * 8);
  asm volatile("" : "+v"(r));
  return r;
}
struct Task0 { unsigned off[3]; };
DI void task0_init(const Ctx& c, Task0& t0) {
#pragma unroll
  for (int r = 0; r < 3; ++r) t0.off[r] = ntask(c, (c.t >> 3) + 64 * r, c.t & 7);
}
DI void norm_group0(const Ctx& c, const Task0& t0, int gbsel) {
  u32x2 v[3];
#pragma unroll
  for (int r = 0; r < 3; ++r) v[r] = *(const u32x2*)(c.smem + t0.off[r]);
  f32x4 a, b; stat_par(c.smem, gbsel, 0, 4 * (c.t & 7), a, b);
#pragma unroll
  for (int r = 0; r < 2; ++r) *(u32x2*)(c.smem + t0.off[r]) = act4_packed(v[r], a, b);
  if (c.w < 7) *(u32x2*)(c.smem + t0.off[2]) = act4_packed(v[2], a, b);
  else {
    if (c.lane < 48) {
      const int ch = 32 + 2 * c.lane;
      const f32x2 g = *(const f32x2*)(c.smem + L_GB + gbsel * 1024 + ch * 4), be = *(const f32x2*)(c.smem + L_GB + gbsel * 1024 + 512 + ch * 4);
      const f32x2 st = *(const f32x2*)(c.smem + L_STAT + (ch >> 5) * 8);
      const f32x2 aa = g * (st[1] * LOG2E);
      *(f32x2*)(c.smem + L_PAR + ch * 4) = aa; *(f32x2*)(c.smem + L_PAR + 512 + ch * 4) = be * LOG2E - aa * st[0];
    }
  }
}
struct HTask { unsigned off[3]; };
DI void htask_init(const Ctx& c, HTask& ht) {
#pragma unroll
  for (int r = 0; r < 3; ++r) ht.off[r] = ntask(c, ((c.t - 256) >> 2) + 64 * r, 2 * (c.t & 3));
}
DI void norm_group_h(const Ctx& c, const HTask& ht, int k) {
  const int ch = 32 * k + 8 * (c.t & 3);
  u32x4 v[3];
#pragma unroll
  for (int r = 0; r < 3; ++r) v[r] = *(const u32x4*)(c.smem + (ht.off[r] ^ (k << 6)));
  const f32x4 a0 = *(const f32x4*)(c.smem + L_PAR + ch * 4), a1 = *(const f32x4*)(c.smem + L_PAR + ch * 4 + 16);
  const f32x4 b0 = *(const f32x4*)(c.smem + L_PAR + 512 + ch * 4), b1 = *(const f32x4*)(c.smem + L_PAR + 512 + ch * 4 + 16);
#pragma unroll
  for (int r = 0; r < 3; ++r) {
    const u32x2 lo = act4_scalar(u32x2{v[r][0], v[r][1]}, a0, b0), hi = act4_scalar(u32x2{v[r][2], v[r][3]}, a1, b1);
    *(u32x4*)(c.smem + (ht.off[r] ^ (k << 6))) = u32x4{lo[0], lo[1], hi[0], hi[1]};
  }
}

DI void xraw_issue(const Ctx& c) {
  int xs = c.x0 - 8 + 8 * (c.lane & 3);
  { const int lo = c.x0 & 64; xs = xs < lo ? lo : (xs > lo + 56 ? lo + 56 : xs); }
  const unsigned vo = (unsigned)(((c.b * 128 + 32 * c.w + (c.lane >> 2)) * 128) * 128 + xs) * 2;
#pragma unroll
  for (int row = 0; row < 10; ++row) {
    const int y = row == 0 ? (c.ty == 0 ? 0 : c.y0 - 1) : row == 9 ? (c.ty == 15 ? 127 : c.y0 + 8) : c.y0 - 1 + row;
#pragma unroll
    for (int cbl = 0; cbl < 2; ++cbl)
      __builtin_amdgcn_raw_ptr_buffer_load_lds(c.rx, (LAS void*)(c.smem + L_RAW + row * RS + (2 * c.w + cbl) * 1024), 16, vo, y * 256 + cbl * 16 * 32768, 0, 0);
  }
}
template <int RPAIR>
DI void xraw_step(const Ctx& c, unsigned ra, unsigned rh, const unsigned (&wa)[4], const unsigned (&wh)[2], bool rowok, bool hok) {
  u32x2 v[6];
  constexpr int RO = (RPAIR & 1) * 2 * RS;
  const unsigned ra2 = ra + (RPAIR >> 1) * 4 * RS, rh2 = rh + (RPAIR >> 1) * 4 * RS;
  asm volatile("s_waitcnt vmcnt(%8)\n\t"
               "ds_read_b64_tr_b16 %0, %6 offset:%9\n\tds_read_b64_tr_b16 %1, %6 offset:%10\n\tds_read_b64_tr_b16 %2, %6 offset:%11\n\tds_read_b64_tr_b16 %3, %6 offset:%12\n\t"
               "ds_read_b64_tr_b16 %4, %7 offset:%9\n\tds_read_b64_tr_b16 %5, %7 offset:%13\n\ts_waitcnt lgkmcnt(0)"
               : "=&v"(v[0]), "=&v"(v[1]), "=&v"(v[2]), "=&v"(v[3]), "=&v"(v[4]), "=&v"(v[5])
               : "v"(ra2), "v"(rh2), "i"(16 - 4 * RPAIR), "i"(RO), "i"(RO + 512), "i"(RO + 1024), "i"(RO + 1536), "i"(RO + 1024) : "memory");
  if (rowok) {
#pragma unroll
    for (int a = 0; a < 4; ++a) asm volatile("ds_write_b64 %0, %1 offset:%2" :: "v"(wa[a]), "v"(v[a]), "i"(RPAIR * 2 * RP) : "memory");
  }
  if (hok) {
#pragma unroll
    for (int hb = 0; hb < 2; ++hb) asm volatile("ds_write_b64 %0, %1 offset:%2" :: "v"(wh[hb]), "v"(v[4 + hb]), "i"(RPAIR * 2 * RP) : "memory");
  }
}
DI void xraw_transpose(const Ctx& c) {
  const unsigned lb = (unsigned)(size_t)(LAS char*)c.smem;
  const int gsel = c.lane >> 4, i = c.lane & 15, qp = i >> 2, p = i & 3;
  const unsigned ra = lb + L_RAW + (gsel & 1) * RS + (4 * (8 * c.w + (gsel >> 1)) + qp) * 64 + 16 + p * 8;
  unsigned wa[4];
#pragma unroll
  for (int a = 0; a < 4; ++a) wa[a] = lb + L_T + (gsel & 1) * RP + (i + 1) * 256 + (((4 * c.w + a) ^ swz((i + 1) & 15)) << 4) + (gsel >> 1) * 8;
  const unsigned rh = lb + L_RAW + (p >> 1) * RS + (4 * (8 * c.w + gsel) + qp) * 64 + ((p & 1) ? 48 : 8);
  const int side = qp & 1, hrow = qp >> 1, hcol = side ? 17 : 0;
  unsigned wh[2];
#pragma unroll
  for (int hb = 0; hb < 2; ++hb) wh[hb] = lb + L_T + hrow * RP + hcol * 256 + (((4 * c.w + 2 * hb + (gsel >> 1)) ^ swz(hcol & 15)) << 4) + (gsel & 1) * 8;
  const bool huse = (side ? p == 0 : p == 3) && (side ? (c.tx != 7 && c.tx != 3) : (c.tx != 0 && c.tx != 4));
  const bool top = c.ty == 0, bot = c.ty == 15;
  xraw_step<0>(c, ra, rh, wa, wh, !(top && (gsel & 1) == 0), huse && !(top && hrow == 0));
  xraw_step<1>(c, ra, rh, wa, wh, true, huse);
  xraw_step<2>(c, ra, rh, wa, wh, true, huse);
  xraw_step<3>(c, ra, rh, wa, wh, true, huse);
  xraw_step<4>(c, ra, rh, wa, wh, !(bot && (gsel & 1) == 1), huse && !(bot && hrow == 1));
}
struct Halo { unsigned go[4], lo[4]; bool inr[4], valid[4]; };
DI void halo_init(const Ctx& c, Halo& h) {
  const int tid = c.t - 256, cg = tid & 15;
#pragma unroll
  for (int r = 0; r < 4; ++r) {
    const int hp = (tid >> 4) + 16 * r;
    int row, col;
    if (hp < 18) { row = 0; col = hp; } else if (hp < 36) { row = 9; col = hp - 18; } else { const int j = hp - 36; row = 1 + (j >> 1); col = (j & 1) ? 17 : 0; }
    h.inr[r] = hp < 52;
    h.valid[r] = h.inr[r] && (unsigned)(c.y0 - 1 + row) < 128u && (unsigned)(c.x0 - 1 + col) < 128u;
    h.go[r] = WS_HB + (unsigned)(((c.b * 128 + c.y0 - 1 + row) * 128 + c.x0 - 1 + col) * 256 + cg * 16);
    h.lo[r] = L_T + row * RP + col * 256 + ((cg ^ swz(col & 15)) << 4);
  }
}
struct Clr { unsigned o[3]; };
DI void clear_init(const Ctx& c, Clr& cl) {
#pragma unroll
  for (int r = 0; r < 3; ++r) {
    int id = c.t - 256 + 256 * r; id = id < 704 ? id : id - 256;
    const int i = id >> 4;
    int ry, rx;
    if (i < 16) { ry = 0; rx = i; } else if (i < 32) { ry = 7; rx = i - 16; } else { const int j = i - 32; ry = 1 + (j >> 1); rx = (j & 1) ? 15 : 0; }
    cl.o[r] = WS_HB + (unsigned)(((c.b * 128 + c.y0 + ry) * 128 + c.x0 + rx) * 256 + (id & 15) * 16);
    asm volatile("" : "+v"(cl.o[r]));
  }
}
DI void clear_borders(const Ctx& c, const Clr& cl) {
  const u32x4 s = {~0u, ~0u, ~0u, ~0u};
#pragma unroll
  for (int r = 0; r < 3; ++r) bst_sc1(s, c.rw, cl.o[r], 0);
}
DI void stats_to_lds(const Ctx& c, int g, const u32x4& v0, const u32x4& v1) {
  const float s = wave_sum(__uint_as_float(v0[0]) + __uint_as_float(v1[0])), q = wave_sum(__uint_as_float(v0[2]) + __uint_as_float(v1[2]));
  const float mean = s * (1.f / 524288.f); float var = q * (1.f / 524288.f) - mean * mean; var = var < 0.f ? 0.f : var;
  if (c.lane == 0) *(f32x2*)(c.smem + L_STAT + g * 8) = f32x2{mean, __builtin_amdgcn_rsqf(var + 1e-5f)};
}

#define WLD bld_sc1
template <int CONV>
DI void mainloop(const Ctx& c, f32x4 (&acc)[8][2], bf16x8 (&wr)[WD], unsigned wvoff, const f32x4 (&ini)[2]) {
  const char* T = c.smem + L_T;
  const int px = c.lane & 15, q = c.lane >> 4;
  const int colP = px + (q & 1), colD = px + 2;
  const unsigned baseP = colP * 256 + (((q >> 1) ^ swz(colP & 15)) << 4), baseD = colD * 256 + ((q ^ swz(colD & 15)) << 4);
  auto rd = [&](int B, int r) -> bf16x8 {
    const int sp = B / 3, ty = B % 3;
    const unsigned a = ty == 2 ? (baseD ^ (sp << 6)) : (baseP ^ ((2 * sp + ty) << 5));
    return *(const bf16x8*)(T + a + r * RP);
  };
  bf16x8 xf[4];
#pragma unroll
  for (int r = 0; r < 3; ++r) xf[r] = rd(0, r);
#pragma unroll
  for (int B = 0; B < 12; ++B) {
#pragma unroll
    for (int r = 0; r < 10; ++r) {
      const int G = B * 10 + r;
      if (r == 7 && (B % 3) == 2 && B < 11) { bar_nowait(); SB(); }
      { const int Gn = G + 3; if (Gn < 120) xf[Gn & 3] = rd(Gn / 10, Gn % 10); }
#pragma unroll
      for (int dy = 0; dy < 3; ++dy) {
        const int row = r - dy;
        if (row >= 0 && row < 8) {
#pragma unroll
          for (int nt = 0; nt < 2; ++nt) {
            const int ts = 6 * B + 2 * dy + nt;
            if (CONV == 1) asm volatile("v_mfma_f32_16x16x32_bf16 %0, %1, %2, %0" : "+v"(acc[row][nt]) : "v"(xf[G & 3]), "v"(wr[ts % WD]));
            else if (B == 0 && dy == 0) asm volatile("v_mfma_f32_16x16x32_bf16 %0, %1, %2, %3" : "=&v"(acc[row][nt]) : "v"(wr[ts % WD]), "v"(xf[G & 3]), "v"(ini[nt]));
            else           asm volatile("v_mfma_f32_16x16x32_bf16 %0, %1, %2, %0" : "+v"(acc[row][nt]) : "v"(wr[ts % WD]), "v"(xf[G & 3]));
            if (row == 7) { const int tn = CONV * 72 + ts + WD; if (tn < 144) wr[ts % WD] = __builtin_bit_cast(bf16x8, WLD(c.rw, wvoff, (unsigned)tn * 1024 + (tn >= 72 ? 294912u - 72u * 1024u : 0u))); }
            SB();
          }
        }
      }
    }
  }
  asm volatile("s_nop 15" ::: "memory");
  SB();
}

DI void pin_acc(f32x4 (&acc)[8][2]) {
#pragma unroll
  for (int r = 0; r < 8; ++r) asm volatile("" : "+v"(acc[r][0]), "+v"(acc[r][1]));
}
__global__ __launch_bounds__(512, 2) void k_resblock(const u16* __restrict__ x, const u16* __restrict__ t_emb, const u16* __restrict__ g1, const u16* __restrict__ be1,
                                                     const u16* __restrict__ w1, const u16* __restrict__ w_emb, const u16* __restrict__ g2, const u16* __restrict__ be2,
                                                     const u16* __restrict__ w2, u16* __restrict__ out, char* __restrict__ ws) {
  extern __shared__ __attribute__((aligned(256))) char smem[];
  Ctx c;
  c.smem = smem; c.ws = ws;
  c.t = threadIdx.x; c.lane = c.t & 63; c.w = __builtin_amdgcn_readfirstlane(c.t >> 6);
  const int xcd = blockIdx.x & 7, jx = blockIdx.x >> 3;
  c.b = xcd >> 2; c.ty = (xcd & 3) * 4 + (jx >> 3); c.tx = jx & 7;
  c.jb = c.ty * 8 + c.tx; c.y0 = c.ty * 8; c.x0 = c.tx * 16;
  c.rx = mk_rsrc(x, 8u << 20); c.rw = mk_rsrc(ws, 16u << 20);
  c.wb = (unsigned)c.b * WB_SIZE;
  const unsigned epoch = __hip_atomic_load((GAS unsigned*)(ws + c.wb + WB_EPOCH), __ATOMIC_RELAXED, __HIP_MEMORY_SCOPE_AGENT);

  if (c.w < 4) {
    STAMP_DECL STAMP(0);
    const int ch = c.t & 127;
    const unsigned gbA = (c.t < 128 ? g1 : be1)[ch], gbB = (c.t < 128 ? g2 : be2)[ch];
    SB();
    bar_nowait(); SB();
    STAMP(14);
    xraw_issue(c); SB();
    STAMP(15);
    Task0 t0; task0_init(c, t0);
    xraw_transpose(c); SB();
    c.tag = __builtin_amdgcn_readfirstlane(epoch) + 1u;
    { float* gbp = (float*)(smem + L_GB) + (c.t < 128 ? 0 : 128) + ch; gbp[0] = bflo(gbA); gbp[256] = bflo(gbB); }
    STAMP(3);
    {
      const unsigned fo = c.wb + WB_FLAG + (unsigned)((((c.lane >> 3) & 1) * 4 + c.w) * 128 + (c.lane & 7) * 4);
      for (int spin = 0; spin < SPIN_MAX; ++spin) {
        cbar();
        const unsigned f = __builtin_amdgcn_raw_buffer_load_b32(c.rw, fo, 0, SC1);
        if (!__any(f != c.tag)) break;
        __builtin_amdgcn_s_sleep(2);
      }
    }
    STAMP(4);
    const unsigned wvoff = c.wb + WB_WF + (unsigned)c.w * 73728u + (unsigned)c.lane * 16u;
    bf16x8 wr[WD];
#pragma unroll
    for (int k = 0; k < WD; ++k) wr[k] = __builtin_bit_cast(bf16x8, bld_sc1(c.rw, wvoff, (unsigned)k * 1024));
    const int px = c.lane & 15, q = c.lane >> 4;
    bar();
    STAMP(5);
    f32x4 acc[8][2], ini[2];
    ini[0] = *(const f32x4*)(smem + L_EMB + (32 * c.w + 4 * q) * 4); ini[1] = *(const f32x4*)(smem + L_EMB + (32 * c.w + 16 + 4 * q) * 4);
    norm_group0(c, t0, 0);
    bar();
    STAMP(6);
    __builtin_amdgcn_s_setprio(1);
    mainloop<0>(c, acc, wr, wvoff, ini);
    __builtin_amdgcn_s_setprio(0);
    STAMP(7);
    {
      f32x2 s2 = {0.f, 0.f}, q2 = {0.f, 0.f};
#pragma unroll
      for (int r = 0; r < 8; ++r)
#pragma unroll
        for (int nt = 0; nt < 2; ++nt)
#pragma unroll
          for (int k = 0; k < 2; ++k) { const f32x2 v = {acc[r][nt][2 * k], acc[r][nt][2 * k + 1]}; s2 += v; q2 += v * v; }
      const float s = wave_sum(s2[0] + s2[1]), qs = wave_sum(q2[0] + q2[1]);
      if (c.lane == 0) bst_sc1(u32x4{__float_as_uint(s), c.tag, __float_as_uint(qs), c.tag}, c.rw, c.wb + WB_G2 + (unsigned)(c.w * 128 + c.jb) * 16, 0);
    }
    STAMP(1);
    bar_nowait();
    {
      const int chk = 4 * c.w + (((q & 1) << 1) | (q >> 1));
      const unsigned go = WS_HB + (unsigned)(((c.b * 128 + c.y0) * 128 + c.x0 + px) * 256 + chk * 16);
      char* lp = smem + L_T + RP + (px + 1) * 256 + ((chk ^ swz((px + 1) & 15)) << 4);
      const bool edge = px == 0 || px == 15;
#pragma unroll
      for (int rr = 0; rr < 8; ++rr) {
        const int r = rr == 0 ? 0 : (rr == 1 ? 7 : rr - 1);
        const unsigned d0 = cvtpk(acc[r][0][0], acc[r][0][1]), d1 = cvtpk(acc[r][0][2], acc[r][0][3]);
        const unsigned d2 = cvtpk(acc[r][1][0], acc[r][1][1]), d3 = cvtpk(acc[r][1][2], acc[r][1][3]);
        auto a = __builtin_amdgcn_permlane16_swap(d0, d2, false, false);
        auto e = __builtin_amdgcn_permlane16_swap(d1, d3, false, false);
        const u32x4 v = {a[0], e[0], a[1], e[1]};
        if (r == 0 || r == 7 || edge) bst_sc1(u32x4{min(v[0], 0xfffffffeu), min(v[1], 0xfffffffeu), min(v[2], 0xfffffffeu), min(v[3], 0xfffffffeu)}, c.rw, go, r * 32768);
        *(u32x4*)(lp + r * RP) = v;
      }
    }
    bar();
    STAMP(8);
#pragma unroll
    for (int r = 0; r < 8; ++r) {
      const u32x4 v = *(const u32x4*)(smem + L_RAW + (r + 1) * RS + (32 * c.w + 16 * (q & 1) + px) * 64 + 16 + (q >> 1) * 16);
      auto a = __builtin_amdgcn_permlane16_swap(v[0], v[2], false, false);
      auto e = __builtin_amdgcn_permlane16_swap(v[1], v[3], false, false);
      acc[r][0] = f32x4{bflo(a[0]), bfhi(a[0]), bflo(e[0]), bfhi(e[0])};
      acc[r][1] = f32x4{bflo(a[1]), bfhi(a[1]), bflo(e[1]), bfhi(e[1])};
    }
    pin_acc(acc);
    STAMP(9);
    bar();
    STAMP(10);
    norm_group0(c, t0, 1);
    bar();
    STAMP(11);
    __builtin_amdgcn_s_setprio(1);
    mainloop<1>(c, acc, wr, wvoff, ini);
    __builtin_amdgcn_s_setprio(0);
    STAMP(12);
    u16* op = out + ((size_t)(c.b * 128 + 32 * c.w + 16 * (q & 1) + px) * 128 + c.y0) * 128 + c.x0 + 8 * (q >> 1);
#pragma unroll
    for (int r = 0; r < 8; ++r) {
      const unsigned d0 = cvtpk(acc[r][0][0], acc[r][0][1]), d1 = cvtpk(acc[r][0][2], acc[r][0][3]);
      const unsigned d2 = cvtpk(acc[r][1][0], acc[r][1][1]), d3 = cvtpk(acc[r][1][2], acc[r][1][3]);
      auto a = __builtin_amdgcn_permlane16_swap(d0, d2, false, false);
      auto e = __builtin_amdgcn_permlane16_swap(d1, d3, false, false);
      *(u32x4*)(op + r * 128) = u32x4{a[0], e[0], a[1], e[1]};
    }
    STAMP(13); STAMP_FLUSH;
  } else {
    STAMP_DECL STAMP(0);
    const int g = c.w - 4;
    const int yrow = (xcd & 3) * 32 + jx;
    __builtin_amdgcn_s_setprio(2);
    const int ksvc = jx * 4 + (xcd & 3);
    const bool svc_w = c.w == 7 && (ksvc < 32 || (ksvc >= 64 && ksvc < 96)), svc_e = c.w == 7 && ksvc >= 32 && ksvc < 64;
    const int wu = ksvc < 32 ? ksvc : ksvc - 32;
    u32x4 sv[9]; unsigned te[6];
    if (svc_w) {
      const u32x4* src = (const u32x4*)(((wu >> 5) ? w2 : w1) + (size_t)((32 * ((wu >> 3) & 3) + (c.lane & 31)) * 128 + 16 * (wu & 7) + 8 * (c.lane >> 5)) * 9);
#pragma unroll
      for (int k = 0; k < 9; ++k) sv[k] = src[k];
    }
    if (svc_e) {
      const int j = ksvc - 32, e0 = 192 * (j >> 3) + (c.lane >> 1);
#pragma unroll
      for (int k = 0; k < 6; ++k) { sv[k] = *(const u32x4*)(w_emb + (size_t)(e0 + 32 * k) * 128 + 16 * (j & 7) + 8 * (c.lane & 1)); te[k] = t_emb[c.b * 768 + e0 + 32 * k]; }
    }
    SB();
    u32x4 xr[8];
    { const unsigned o = (unsigned)(((c.b * 128 + 32 * g + 8 * (c.lane >> 4)) * 128 + yrow) * 128) * 2 + (c.lane & 15) * 16;
#pragma unroll
      for (int jj = 0; jj < 8; ++jj) xr[jj] = bld(c.rx, o, jj * 32768); }
    SB();
    bar_nowait(); SB();
    STAMP(4);
    c.tag = __builtin_amdgcn_readfirstlane(epoch) + 1u;
    {
      float s = 0.f, qs = 0.f;
#pragma unroll
      for (int jj = 0; jj < 8; ++jj)
#pragma unroll
        for (int k = 0; k < 4; ++k) { s = dot2(xr[jj][k], 0x3f803f80u, s); qs = dot2(xr[jj][k], xr[jj][k], qs); }
      s = wave_sum(s); qs = wave_sum(qs);
      if (c.lane == 0) bst_sc1(u32x4{__float_as_uint(s), c.tag, __float_as_uint(qs), c.tag}, c.rw, c.wb + WB_G1 + (unsigned)(g * 128 + c.jb) * 16, 0);
    }
    {
      const int k16 = c.lane & 15;
      if (k16 == 7 || k16 == 8) {
        unsigned v[4];
#pragma unroll
        for (int m = 0; m < 4; ++m) {
          const unsigned a = __builtin_amdgcn_perm(xr[2 * m + 1][3], xr[2 * m][3], 0x07060302u), b2 = __builtin_amdgcn_perm(xr[2 * m + 1][0], xr[2 * m][0], 0x05040100u);
          v[m] = k16 == 7 ? a : b2;
        }
        const unsigned o = c.wb + WB_XC + (unsigned)((yrow * 2 + (k16 - 7)) * 64 + 16 * g + 4 * (c.lane >> 4)) * 8;
        bst_sc1(u32x4{v[0], c.tag, v[1], c.tag}, c.rw, o, 0); bst_sc1(u32x4{v[2], c.tag, v[3], c.tag}, c.rw, o, 16);
      }
    }
    STAMP(1);
    if (svc_w) {
      const int cs = wu & 7, o = c.lane & 31, h = c.lane >> 5;
      const unsigned dst = c.wb + WB_WF + (unsigned)(wu >> 5) * 294912u + (unsigned)((wu >> 3) & 3) * 73728u + (unsigned)(o >> 4) * 1024u + (unsigned)(o & 15) * 16u;
      const unsigned dP = dst + (unsigned)(3 * (cs >> 1) + (cs & 1)) * 6144u + (unsigned)h * 512u;
      const unsigned dD = dst + (unsigned)(3 * (cs >> 1) + 2) * 6144u + (unsigned)(2 * (cs & 1) + h) * 256u;
#pragma unroll
      for (int k = 0; k < 9; ++k) {
        const int dy = k / 3, dx = k % 3;
        u32x4 o4;
#pragma unroll
        for (int m = 0; m < 4; ++m) {
          const int e0 = 18 * m + k, e1 = e0 + 9;
          const unsigned sel = ((e0 & 1) ? 0x0302u : 0x0100u) | ((e1 & 1) ? 0x07060000u : 0x05040000u);
          o4[m] = __builtin_amdgcn_perm(sv[e1 >> 3][(e1 >> 1) & 3], sv[e0 >> 3][(e0 >> 1) & 3], sel);
        }
        bst_sc1(o4, c.rw, dx < 2 ? dP : dD, dy * 2048 + (dx < 2 ? dx * 256 : 0));
      }
      asm volatile("s_waitcnt vmcnt(0)" ::: "memory");
      if (c.lane == 0) __hip_atomic_store((GAS unsigned*)(ws + c.wb + WB_FLAG + (wu >> 3) * 128 + (wu & 7) * 4), c.tag, __ATOMIC_RELAXED, __HIP_MEMORY_SCOPE_AGENT);
    }
    if (svc_e) {
      f32x2 a[4];
#pragma unroll
      for (int m = 0; m < 4; ++m) a[m] = f32x2{0.f, 0.f};
#pragma unroll
      for (int k = 0; k < 6; ++k) {
        const float tv = bflo(te[k]);
        const float sl = tv * __builtin_amdgcn_rcpf(1.f + __builtin_amdgcn_exp2f(-tv * LOG2E));
#pragma unroll
        for (int m = 0; m < 4; ++m) a[m] += f32x2{bflo(sv[k][m]), bfhi(sv[k][m])} * sl;
      }
      float r[8];
#pragma unroll
      for (int m = 0; m < 8; ++m) {
        float v = a[m >> 1][m & 1];
        v = dppadd<0x4E>(v); v = dppadd<0x124>(v); v = dppadd<0x128>(v);
        auto p = __builtin_amdgcn_permlane16_swap(__float_as_uint(v), __float_as_uint(v), false, false);
        v = __uint_as_float(p[0]) + __uint_as_float(p[1]);
        auto p2 = __builtin_amdgcn_permlane32_swap(__float_as_uint(v), __float_as_uint(v), false, false);
        r[m] = __uint_as_float(p2[0]) + __uint_as_float(p2[1]);
      }
      const int j = ksvc - 32;
      if (c.lane < 2) {
        const unsigned o = c.wb + WB_EG + (unsigned)((j >> 3) * 128 + 16 * (j & 7) + 8 * c.lane) * 8;
#pragma unroll
        for (int m = 0; m < 4; ++m) bst_sc1(u32x4{__float_as_uint(r[2 * m]), c.tag, __float_as_uint(r[2 * m + 1]), c.tag}, c.rw, o, m * 16);
      }
    }
    __builtin_amdgcn_s_setprio(0);
    STAMP(2);
    Halo hl; halo_init(c, hl);
#pragma unroll
    for (int r = 0; r < 4; ++r) if (hl.inr[r] && !hl.valid[r]) *(u32x4*)(smem + hl.lo[r]) = u32x4{0, 0, 0, 0};
    Task0 t0; task0_init(c, t0);
    HTask ht; htask_init(c, ht);
    Clr cl; clear_init(c, cl);
    STAMP(3);
    {
      u32x4 v0, v1, ve[4], vc[2]; bool n0 = true, n1 = true, ne = c.w == 4, nc[2];
      const unsigned o = c.wb + WB_G1 + (unsigned)(g * 128 + c.lane) * 16;
      const bool xc = c.tx == 3 || c.tx == 4;
      unsigned co[2], cl[2]; bool cin[2];
#pragma unroll
      for (int r = 0; r < 2; ++r) {
        const int id = c.t - 256 + 256 * r, row = id >> 5, i = id & 31, y = c.y0 - 1 + row, col = c.tx == 3 ? 17 : 0;
        cin[r] = xc && id < 320;
        nc[r] = cin[r] && (unsigned)y < 128u;
        co[r] = c.wb + WB_XC + (unsigned)((y * 2 + (c.tx == 3 ? 1 : 0)) * 64 + 2 * i) * 8;
        cl[r] = L_T + row * RP + col * 256 + (((i >> 1) ^ swz(col & 15)) << 4) + (i & 1) * 8;
        vc[r] = u32x4{0, 0, 0, 0};
      }
      for (int spin = 0; spin < SPIN_MAX; ++spin) {
        cbar();
        if (n0) v0 = bld_sc1(c.rw, o, 0);
        if (n1) v1 = bld_sc1(c.rw, o, 1024);
        if (ne) {
#pragma unroll
          for (int k = 0; k < 4; ++k) ve[k] = bld_sc1(c.rw, c.wb + WB_EG + (unsigned)c.lane * 16, k * 1024);
        }
#pragma unroll
        for (int r = 0; r < 2; ++r) if (nc[r]) vc[r] = bld_sc1(c.rw, co[r], 0);
        n0 = !(v0[1] == c.tag && v0[3] == c.tag); n1 = !(v1[1] == c.tag && v1[3] == c.tag);
        if (ne) { bool ok = true;
#pragma unroll
          for (int k = 0; k < 4; ++k) ok = ok && ve[k][1] == c.tag && ve[k][3] == c.tag;
          ne = !ok; }
#pragma unroll
        for (int r = 0; r < 2; ++r) nc[r] = nc[r] && !(vc[r][1] == c.tag && vc[r][3] == c.tag);
        if (!__any(n0 || n1 || ne || nc[0] || nc[1])) break;
        __builtin_amdgcn_s_sleep(2);
      }
      stats_to_lds(c, g, v0, v1);
      if (c.w == 4) *(f32x2*)(smem + L_EMB + c.lane * 8) = f32x2{(__uint_as_float(ve[0][0]) + __uint_as_float(ve[1][0])) + (__uint_as_float(ve[2][0]) + __uint_as_float(ve[3][0])),
                                                               (__uint_as_float(ve[0][2]) + __uint_as_float(ve[1][2])) + (__uint_as_float(ve[2][2]) + __uint_as_float(ve[3][2]))};
#pragma unroll
      for (int r = 0; r < 2; ++r) if (cin[r]) *(u32x2*)(smem + cl[r]) = u32x2{vc[r][0], vc[r][2]};
    }
    STAMP(6);
    bar();
    if (c.t == 256 && c.jb == 0) __hip_atomic_store((GAS unsigned*)(ws + c.wb + WB_EPOCH), c.tag, __ATOMIC_RELAXED, __HIP_MEMORY_SCOPE_AGENT);
    norm_group0(c, t0, 0);
    bar();
#pragma unroll 1
    for (int k = 1; k < 4; ++k) {
      if (k == 2) clear_borders(c, cl);
      norm_group_h(c, ht, k);
      if (k == 2) asm volatile("s_waitcnt vmcnt(0)" ::: "memory");
      bar();
      if (k == 2 && c.t == 256) __hip_atomic_store((GAS unsigned*)(ws + c.wb + WB_CF + c.jb * 4), c.tag, __ATOMIC_RELAXED, __HIP_MEMORY_SCOPE_AGENT);
    }
    STAMP(7);
    {
      if (c.w == 5) {
        const int dy = (c.lane & 15) / 3 - 1, dx = (c.lane & 15) % 3 - 1;
        const int ny = c.ty + dy, nx = c.tx + dx;
        const bool chk = c.lane < 9 && (unsigned)ny < 16u && (unsigned)nx < 8u;
        for (int spin = 0; spin < SPIN_MAX; ++spin) {
          cbar();
          unsigned f = c.tag;
          if (chk) f = __builtin_amdgcn_raw_buffer_load_b32(c.rw, c.wb + WB_CF + (unsigned)(ny * 8 + nx) * 4, 0, SC1);
          if (!__any(f != c.tag)) break;
          __builtin_amdgcn_s_sleep(2);
        }
      }
      u32x4 hv[4]; bool need[4];
#pragma unroll
      for (int r = 0; r < 4; ++r) { need[r] = hl.valid[r]; hv[r] = u32x4{0, 0, 0, 0}; }
      u32x4 v0, v1; bool n0 = true, n1 = true;
      const unsigned o = c.wb + WB_G2 + (unsigned)(g * 128 + c.lane) * 16;
      bar_nowait();
      bar();
      STAMP(13);
      for (int spin = 0; spin < SPIN_MAX; ++spin) {
        cbar();
#pragma unroll
        for (int r = 0; r < 4; ++r) if (need[r]) hv[r] = bld_sc1(c.rw, hl.go[r], 0);
        if (n0) v0 = bld_sc1(c.rw, o, 0);
        if (n1) v1 = bld_sc1(c.rw, o, 1024);
        bool any = false;
#pragma unroll
        for (int r = 0; r < 4; ++r) { need[r] = need[r] && (hv[r][0] == ~0u || hv[r][1] == ~0u || hv[r][2] == ~0u || hv[r][3] == ~0u); any |= need[r]; }
        n0 = !(v0[1] == c.tag && v0[3] == c.tag); n1 = !(v1[1] == c.tag && v1[3] == c.tag);
        if (!__any(any || n0 || n1)) break;
        __builtin_amdgcn_s_sleep(1);
      }
      STAMP(8);
#pragma unroll
      for (int r = 0; r < 4; ++r) if (hl.valid[r]) *(u32x4*)(smem + hl.lo[r]) = hv[r];
      stats_to_lds(c, g, v0, v1);
    }
    bar();
    norm_group0(c, t0, 1);
    bar();
#pragma unroll 1
    for (int k = 1; k < 4; ++k) { if (k == 1) STAMP(9); norm_group_h(c, ht, k); if (k == 1) STAMP(10); if (k == 2) STAMP(11); bar(); }
    STAMP(12); STAMP_FLUSH;
  }
}

extern "C" void kernel_launch(void* const* d_in, const int* in_sizes, int n_in, void* d_out, int out_size, void* d_ws, size_t ws_size, hipStream_t stream) {
  (void)hipFuncSetAttribute((const void*)k_resblock, hipFuncAttributeMaxDynamicSharedMemorySize, L_END);
  k_resblock<<<256, 512, L_END, stream>>>((const u16*)d_in[0], (const u16*)d_in[1], (const u16*)d_in[2], (const u16*)d_in[3], (const u16*)d_in[4],
                                          (const u16*)d_in[5], (const u16*)d_in[6], (const u16*)d_in[7], (const u16*)d_in[8], (u16*)d_out, (char*)d_ws);
}
```

```cpp
#define DIAG 1
#define DIAG_WG 100
#define DIAG_A 0
#define DIAG_B 5
#define DIAG_SCALE 0
#include <hip/hip_runtime.h>
#include <hip/hip_bf16.h>
#include <stdint.h>

typedef float f32x4 __attribute__((ext_vector_type(4)));
typedef float f32x2 __attribute__((ext_vector_type(2)));
typedef __bf16 bf16x8 __attribute__((ext_vector_type(8)));
typedef __bf16 bf16x2 __attribute__((ext_vector_type(2)));
typedef unsigned u32x4 __attribute__((ext_vector_type(4)));
typedef unsigned u32x2 __attribute__((ext_vector_type(2)));
typedef unsigned short u16;
#define GAS __attribute__((address_space(1)))
#define DI __device__ __forceinline__
#define SB() __builtin_amdgcn_sched_barrier(0)

constexpr int RP = 4608;
constexpr int TILE_BYTES = 10 * RP;
constexpr int WD = 18;
constexpr float LOG2E = 1.4426950408889634f;
constexpr int SC1 = 16;
constexpr int SPIN_MAX = 1 << 16;

constexpr unsigned WB_SIZE = 1u << 20;
constexpr unsigned WB_WF = 0, WB_EPOCH = 589824, WB_FLAG = WB_EPOCH + 256  , WB_G1 = WB_FLAG + 1024, WB_G2 = WB_G1 + 8192,
                   WB_EG = WB_G2 + 8192  , WB_CF = WB_EG + 4096  , WB_XC = WB_CF + 512  ;
constexpr unsigned WS_HB = 2u << 20;
constexpr int RS = 8224;
constexpr int L_T = 0, L_RAW = TILE_BYTES, L_GB = L_RAW + 10 * RS  , L_PAR = L_GB + 2048  , L_EMB = L_PAR + 1024,
              L_STAT = L_EMB + 512  , L_DUMMY = (L_STAT + 64 + 255) / 256 * 256  , L_END = L_DUMMY + 1024;
#define LAS __attribute__((address_space(3)))

DI int swz(int c) { return c ^ ((c >> 1) & 4); }
DI unsigned cvtpk(float lo, float hi) { f32x2 v = {lo, hi}; bf16x2 b = __builtin_convertvector(v, bf16x2); return __builtin_bit_cast(unsigned, b); }
DI unsigned cvtpk_s(float lo, float hi) { unsigned r; asm("v_cvt_pk_bf16_f32 %0, %1, %2" : "=v"(r) : "v"(lo), "v"(hi)); return r; }
DI float bflo(unsigned d) { return __uint_as_float(d << 16); }
DI float bfhi(unsigned d) { return __uint_as_float(d & 0xffff0000u); }
DI float dot2(unsigned a, unsigned b, float c) { return __builtin_amdgcn_fdot2_f32_bf16(__builtin_bit_cast(bf16x2, a), __builtin_bit_cast(bf16x2, b), c, false); }
template <int CTRL> DI float dppadd(float v) { return v + __int_as_float(__builtin_amdgcn_update_dpp(0, __float_as_int(v), CTRL, 0xf, 0xf, true)); }
DI float wave_sum(float v) {
  v = dppadd<0xB1>(v); v = dppadd<0x4E>(v); v = dppadd<0x141>(v); v = dppadd<0x140>(v);
  auto p = __builtin_amdgcn_permlane16_swap(__float_as_uint(v), __float_as_uint(v), false, false);
  v = __uint_as_float(p[0]) + __uint_as_float(p[1]);
  auto q = __builtin_amdgcn_permlane32_swap(__float_as_uint(v), __float_as_uint(v), false, false);
  return __uint_as_float(q[0]) + __uint_as_float(q[1]);
}
DI void bar() { asm volatile("s_waitcnt lgkmcnt(0)\n\ts_barrier" ::: "memory"); }
DI void bar_nowait() { asm volatile("s_barrier" ::: "memory"); }
DI void cbar() { asm volatile("" ::: "memory"); }
typedef __amdgpu_buffer_rsrc_t rsrc_t;
DI rsrc_t mk_rsrc(const void* p, unsigned bytes) { return __builtin_amdgcn_make_buffer_rsrc((void*)p, 0, bytes, 0x00020000); }
DI u32x4 bld(rsrc_t r, unsigned voff, unsigned soff) { return __builtin_amdgcn_raw_buffer_load_b128(r, voff, soff, 0); }
DI u32x4 bld_sc1(rsrc_t r, unsigned voff, unsigned soff) { return __builtin_amdgcn_raw_buffer_load_b128(r, voff, soff, SC1); }
DI void bst_sc1(u32x4 v, rsrc_t r, unsigned voff, unsigned soff) { __builtin_amdgcn_raw_buffer_store_b128(v, r, voff, soff, SC1); }

struct Ctx {
  rsrc_t rx, rw;
  char* smem; char* ws;
  int b, ty, tx, jb, y0, x0, w, lane, t;
  unsigned wb;
  unsigned tag;
};

#define STAMP_DECL
#define STAMP(idx) do {} while (0)
#define STAMP_FLUSH do {} while (0)

DI void stat_par(const char* smem, int gbsel, int grp, int ch, f32x4& a, f32x4& b) {
  const f32x4 g = *(const f32x4*)(smem + L_GB + gbsel * 1024 + ch * 4), be = *(const f32x4*)(smem + L_GB + gbsel * 1024 + 512 + ch * 4);
  const f32x2 st = *(const f32x2*)(smem + L_STAT + grp * 8);
  a = g * (st[1] * LOG2E); b = be * LOG2E - a * st[0];
}
DI u32x2 act4_packed(u32x2 v, f32x4 a, f32x4 b) {
  u32x2 o;
#pragma unroll
  for (int m = 0; m < 2; ++m) {
    f32x2 xx = {bflo(v[m]), bfhi(v[m])}, aa = {a[2 * m], a[2 * m + 1]}, bb = {b[2 * m], b[2 * m + 1]};
    f32x2 u = xx * aa + bb;
    f32x2 e = {__builtin_amdgcn_exp2f(-u[0]), __builtin_amdgcn_exp2f(-u[1])};
    f32x2 d = e * LOG2E + LOG2E;
    f32x2 r = {__builtin_amdgcn_rcpf(d[0]), __builtin_amdgcn_rcpf(d[1])};
    f32x2 y = u * r;
    o[m] = cvtpk(y[0], y[1]);
  }
  return o;
}
DI float act1(float x, float a, float b) { float u = __builtin_fmaf(x, a, b); float e = __builtin_amdgcn_exp2f(-u); return u * __builtin_amdgcn_rcpf(__builtin_fmaf(e, LOG2E, LOG2E)); }
DI u32x2 act4_scalar(u32x2 v, f32x4 a, f32x4 b) {
  u32x2 o;
  o[0] = cvtpk_s(act1(bflo(v[0]), a[0], b[0]), act1(bfhi(v[0]), a[1], b[1]));
  o[1] = cvtpk_s(act1(bflo(v[1]), a[2], b[2]), act1(bfhi(v[1]), a[3], b[3]));
  return o;
}
DI unsigned ntask(const Ctx& c, unsigned px, int c4) {
  const unsigned row = px / 18, col = px - row * 18;
  const unsigned off = L_T + row * RP + col * 256 + (((c4 >> 1) ^ swz(col & 15)) << 4) + (c4 & 1) * 8;
  const bool valid = px < 180 && (unsigned)(c.y0 - 1 + (int)row) < 128u && (unsigned)(c.x0 - 1 + (int)col) < 128u;
  unsigned r = valid ? off : (unsigned)(L_DUMMY + c.lane * 16 + (c4 & 1) * 8);
  asm volatile("" : "+v"(r));
  return r;
}
struct Task0 { unsigned off[3]; };
DI void task0_init(const Ctx& c, Task0& t0) {
#pragma unroll
  for (int r = 0; r < 3; ++r) t0.off[r] = ntask(c, (c.t >> 3) + 64 * r, c.t & 7);
}
DI void norm_group0(const Ctx& c, const Task0& t0, int gbsel) {
  u32x2 v[3];
#pragma unroll
  for (int r = 0; r < 3; ++r) v[r] = *(const u32x2*)(c.smem + t0.off[r]);
  f32x4 a, b; stat_par(c.smem, gbsel, 0, 4 * (c.t & 7), a, b);
#pragma unroll
  for (int r = 0; r < 2; ++r) *(u32x2*)(c.smem + t0.off[r]) = act4_packed(v[r], a, b);
  if (c.w < 7) *(u32x2*)(c.smem + t0.off[2]) = act4_packed(v[2], a, b);
  else {
    if (c.lane < 48) {
      const int ch = 32 + 2 * c.lane;
      const f32x2 g = *(const f32x2*)(c.smem + L_GB + gbsel * 1024 + ch * 4), be = *(const f32x2*)(c.smem + L_GB + gbsel * 1024 + 512 + ch * 4);
      const f32x2 st = *(const f32x2*)(c.smem + L_STAT + (ch >> 5) * 8);
      const f32x2 aa = g * (st[1] * LOG2E);
      *(f32x2*)(c.smem + L_PAR + ch * 4) = aa; *(f32x2*)(c.smem + L_PAR + 512 + ch * 4) = be * LOG2E - aa * st[0];
    }
  }
}
struct HTask { unsigned off[3]; };
DI void htask_init(const Ctx& c, HTask& ht) {
#pragma unroll
  for (int r = 0; r < 3; ++r) ht.off[r] = ntask(c, ((c.t - 256) >> 2) + 64 * r, 2 * (c.t & 3));
}
DI void norm_group_h(const Ctx& c, const HTask& ht, int k) {
  const int ch = 32 * k + 8 * (c.t & 3);
  u32x4 v[3];
#pragma unroll
  for (int r = 0; r < 3; ++r) v[r] = *(const u32x4*)(c.smem + (ht.off[r] ^ (k << 6)));
  const f32x4 a0 = *(const f32x4*)(c.smem + L_PAR + ch * 4), a1 = *(const f32x4*)(c.smem + L_PAR + ch * 4 + 16);
  const f32x4 b0 = *(const f32x4*)(c.smem + L_PAR + 512 + ch * 4), b1 = *(const f32x4*)(c.smem + L_PAR + 512 + ch * 4 + 16);
#pragma unroll
  for (int r = 0; r < 3; ++r) {
    const u32x2 lo = act4_scalar(u32x2{v[r][0], v[r][1]}, a0, b0), hi = act4_scalar(u32x2{v[r][2], v[r][3]}, a1, b1);
    *(u32x4*)(c.smem + (ht.off[r] ^ (k << 6))) = u32x4{lo[0], lo[1], hi[0], hi[1]};
  }
}

DI void xraw_issue(const Ctx& c) {
  int xs = c.x0 - 8 + 8 * (c.lane & 3);
  { const int lo = c.x0 & 64; xs = xs < lo ? lo : (xs > lo + 56 ? lo + 56 : xs); }
  const unsigned vo = (unsigned)(((c.b * 128 + 32 * c.w + (c.lane >> 2)) * 128) * 128 + xs) * 2;
#pragma unroll
  for (int row = 0; row < 10; ++row) {
    const int y = row == 0 ? (c.ty == 0 ? 0 : c.y0 - 1) : row == 9 ? (c.ty == 15 ? 127 : c.y0 + 8) : c.y0 - 1 + row;
#pragma unroll
    for (int cbl = 0; cbl < 2; ++cbl)
      __builtin_amdgcn_raw_ptr_buffer_load_lds(c.rx, (LAS void*)(c.smem + L_RAW + row * RS + (2 * c.w + cbl) * 1024), 16, vo, y * 256 + cbl * 16 * 32768, 0, 0);
  }
}
template <int RPAIR>
DI void xraw_step(const Ctx& c, unsigned ra, unsigned rh, const unsigned (&wa)[4], const unsigned (&wh)[2], bool rowok, bool hok) {
  u32x2 v[6];
  constexpr int RO = (RPAIR & 1) * 2 * RS;
  const unsigned ra2 = ra + (RPAIR >> 1) * 4 * RS, rh2 = rh + (RPAIR >> 1) * 4 * RS;
  asm volatile("s_waitcnt vmcnt(%8)\n\t"
               "ds_read_b64_tr_b16 %0, %6 offset:%9\n\tds_read_b64_tr_b16 %1, %6 offset:%10\n\tds_read_b64_tr_b16 %2, %6 offset:%11\n\tds_read_b64_tr_b16 %3, %6 offset:%12\n\t"
               "ds_read_b64_tr_b16 %4, %7 offset:%9\n\tds_read_b64_tr_b16 %5, %7 offset:%13\n\ts_waitcnt lgkmcnt(0)"
               : "=&v"(v[0]), "=&v"(v[1]), "=&v"(v[2]), "=&v"(v[3]), "=&v"(v[4]), "=&v"(v[5])
               : "v"(ra2), "v"(rh2), "i"(16 - 4 * RPAIR), "i"(RO), "i"(RO + 512), "i"(RO + 1024), "i"(RO + 1536), "i"(RO + 1024) : "memory");
  if (rowok) {
#pragma unroll
    for (int a = 0; a < 4; ++a) asm volatile("ds_write_b64 %0, %1 offset:%2" :: "v"(wa[a]), "v"(v[a]), "i"(RPAIR * 2 * RP) : "memory");
  }
  if (hok) {
#pragma unroll
    for (int hb = 0; hb < 2; ++hb) asm volatile("ds_write_b64 %0, %1 offset:%2" :: "v"(wh[hb]), "v"(v[4 + hb]), "i"(RPAIR * 2 * RP) : "memory");
  }
}
DI void xraw_transpose(const Ctx& c) {
  const unsigned lb = (unsigned)(size_t)(LAS char*)c.smem;
  const int gsel = c.lane >> 4, i = c.lane & 15, qp = i >> 2, p = i & 3;
  const unsigned ra = lb + L_RAW + (gsel & 1) * RS + (4 * (8 * c.w + (gsel >> 1)) + qp) * 64 + 16 + p * 8;
  unsigned wa[4];
#pragma unroll
  for (int a = 0; a < 4; ++a) wa[a] = lb + L_T + (gsel & 1) * RP + (i + 1) * 256 + (((4 * c.w + a) ^ swz((i + 1) & 15)) << 4) + (gsel >> 1) * 8;
  const unsigned rh = lb + L_RAW + (p >> 1) * RS + (4 * (8 * c.w + gsel) + qp) * 64 + ((p & 1) ? 48 : 8);
  const int side = qp & 1, hrow = qp >> 1, hcol = side ? 17 : 0;
  unsigned wh[2];
#pragma unroll
  for (int hb = 0; hb < 2; ++hb) wh[hb] = lb + L_T + hrow * RP + hcol * 256 + (((4 * c.w + 2 * hb + (gsel >> 1)) ^ swz(hcol & 15)) << 4) + (gsel & 1) * 8;
  const bool huse = (side ? p == 0 : p == 3) && (side ? (c.tx != 7 && c.tx != 3) : (c.tx != 0 && c.tx != 4));
  const bool top = c.ty == 0, bot = c.ty == 15;
  xraw_step<0>(c, ra, rh, wa, wh, !(top && (gsel & 1) == 0), huse && !(top && hrow == 0));
  xraw_step<1>(c, ra, rh, wa, wh, true, huse);
  xraw_step<2>(c, ra, rh, wa, wh, true, huse);
  xraw_step<3>(c, ra, rh, wa, wh, true, huse);
  xraw_step<4>(c, ra, rh, wa, wh, !(bot && (gsel & 1) == 1), huse && !(bot && hrow == 1));
}
struct Halo { unsigned go[4], lo[4]; bool inr[4], valid[4]; };
DI void halo_init(const Ctx& c, Halo& h) {
  const int tid = c.t - 256, cg = tid & 15;
#pragma unroll
  for (int r = 0; r < 4; ++r) {
    const int hp = (tid >> 4) + 16 * r;
    int row, col;
    if (hp < 18) { row = 0; col = hp; } else if (hp < 36) { row = 9; col = hp - 18; } else { const int j = hp - 36; row = 1 + (j >> 1); col = (j & 1) ? 17 : 0; }
    h.inr[r] = hp < 52;
    h.valid[r] = h.inr[r] && (unsigned)(c.y0 - 1 + row) < 128u && (unsigned)(c.x0 - 1 + col) < 128u;
    h.go[r] = WS_HB + (unsigned)(((c.b * 128 + c.y0 - 1 + row) * 128 + c.x0 - 1 + col) * 256 + cg * 16);
    h.lo[r] = L_T + row * RP + col * 256 + ((cg ^ swz(col & 15)) << 4);
  }
}
struct Clr { unsigned o[3]; };
DI void clear_init(const Ctx& c, Clr& cl) {
#pragma unroll
  for (int r = 0; r < 3; ++r) {
    int id = c.t - 256 + 256 * r; id = id < 704 ? id : id - 256;
    const int i = id >> 4;
    int ry, rx;
    if (i < 16) { ry = 0; rx = i; } else if (i < 32) { ry = 7; rx = i - 16; } else { const int j = i - 32; ry = 1 + (j >> 1); rx = (j & 1) ? 15 : 0; }
    cl.o[r] = WS_HB + (unsigned)(((c.b * 128 + c.y0 + ry) * 128 + c.x0 + rx) * 256 + (id & 15) * 16);
    asm volatile("" : "+v"(cl.o[r]));
  }
}
DI void clear_borders(const Ctx& c, const Clr& cl) {
  const u32x4 s = {~0u, ~0u, ~0u, ~0u};
#pragma unroll
  for (int r = 0; r < 3; ++r) bst_sc1(s, c.rw, cl.o[r], 0);
}
DI void stats_to_lds(const Ctx& c, int g, const u32x4& v0, const u32x4& v1) {
  const float s = wave_sum(__uint_as_float(v0[0]) + __uint_as_float(v1[0])), q = wave_sum(__uint_as_float(v0[2]) + __uint_as_float(v1[2]));
  const float mean = s * (1.f / 524288.f); float var = q * (1.f / 524288.f) - mean * mean; var = var < 0.f ? 0.f : var;
  if (c.lane == 0) *(f32x2*)(c.smem + L_STAT + g * 8) = f32x2{mean, __builtin_amdgcn_rsqf(var + 1e-5f)};
}

#define WLD bld_sc1
template <int CONV>
DI void mainloop(const Ctx& c, f32x4 (&acc)[8][2], bf16x8 (&wr)[WD], unsigned wvoff, const f32x4 (&ini)[2]) {
  const char* T = c.smem + L_T;
  const int px = c.lane & 15, q = c.lane >> 4;
  const int colP = px + (q & 1), colD = px + 2;
  const unsigned baseP = colP * 256 + (((q >> 1) ^ swz(colP & 15)) << 4), baseD = colD * 256 + ((q ^ swz(colD & 15)) << 4);
  auto rd = [&](int B, int r) -> bf16x8 {
    const int sp = B / 3, ty = B % 3;
    const unsigned a = ty == 2 ? (baseD ^ (sp << 6)) : (baseP ^ ((2 * sp + ty) << 5));
    return *(const bf16x8*)(T + a + r * RP);
  };
  bf16x8 xf[4];
#pragma unroll
  for (int r = 0; r < 3; ++r) xf[r] = rd(0, r);
#pragma unroll
  for (int B = 0; B < 12; ++B) {
#pragma unroll
    for (int r = 0; r < 10; ++r) {
      const int G = B * 10 + r;
      if (r == 7 && (B % 3) == 2 && B < 11) { bar_nowait(); SB(); }
      { const int Gn = G + 3; if (Gn < 120) xf[Gn & 3] = rd(Gn / 10, Gn % 10); }
#pragma unroll
      for (int dy = 0; dy < 3; ++dy) {
        const int row = r - dy;
        if (row >= 0 && row < 8) {
#pragma unroll
          for (int nt = 0; nt < 2; ++nt) {
            const int ts = 6 * B + 2 * dy + nt;
            if (CONV == 1) asm volatile("v_mfma_f32_16x16x32_bf16 %0, %1, %2, %0" : "+v"(acc[row][nt]) : "v"(xf[G & 3]), "v"(wr[ts % WD]));
            else if (B == 0 && dy == 0) asm volatile("v_mfma_f32_16x16x32_bf16 %0, %1, %2, %3" : "=&v"(acc[row][nt]) : "v"(wr[ts % WD]), "v"(xf[G & 3]), "v"(ini[nt]));
            else           asm volatile("v_mfma_f32_16x16x32_bf16 %0, %1, %2, %0" : "+v"(acc[row][nt]) : "v"(wr[ts % WD]), "v"(xf[G & 3]));
            if (row == 7) { const int tn = CONV * 72 + ts + WD; if (tn < 144) wr[ts % WD] = __builtin_bit_cast(bf16x8, WLD(c.rw, wvoff, (unsigned)tn * 1024 + (tn >= 72 ? 294912u - 72u * 1024u : 0u))); }
            SB();
          }
        }
      }
    }
  }
  asm volatile("s_nop 15" ::: "memory");
  SB();
}

DI void pin_acc(f32x4 (&acc)[8][2]) {
#pragma unroll
  for (int r = 0; r < 8; ++r) asm volatile("" : "+v"(acc[r][0]), "+v"(acc[r][1]));
}
__global__ __launch_bounds__(512, 2) void k_resblock(const u16* __restrict__ x, const u16* __restrict__ t_emb, const u16* __restrict__ g1, const u16* __restrict__ be1,
                                                     const u16* __restrict__ w1, const u16* __restrict__ w_emb, const u16* __restrict__ g2, const u16* __restrict__ be2,
                                                     const u16* __restrict__ w2, u16* __restrict__ out, char* __restrict__ ws) {
  extern __shared__ __attribute__((aligned(256))) char smem[];
  Ctx c;
  c.smem = smem; c.ws = ws;
  c.t = threadIdx.x; c.lane = c.t & 63; c.w = __builtin_amdgcn_readfirstlane(c.t >> 6);
  const int xcd = blockIdx.x & 7, jx = blockIdx.x >> 3;
  c.b = xcd >> 2; c.ty = (xcd & 3) * 4 + (jx >> 3); c.tx = jx & 7;
  c.jb = c.ty * 8 + c.tx; c.y0 = c.ty * 8; c.x0 = c.tx * 16;
  c.rx = mk_rsrc(x, 8u << 20); c.rw = mk_rsrc(ws, 16u << 20);
  c.wb = (unsigned)c.b * WB_SIZE;
  const unsigned epoch = __hip_atomic_load((GAS unsigned*)(ws + c.wb + WB_EPOCH), __ATOMIC_RELAXED, __HIP_MEMORY_SCOPE_AGENT);

  unsigned long long dg[8];
#define DG(i) do { dg[i] = __builtin_amdgcn_s_memrealtime(); asm volatile("" : "+s"(dg[i])); } while (0)
  if (c.w < 4) {
    DG(0);
    STAMP_DECL STAMP(0);
    const int ch = c.t & 127;
    const unsigned gbA = (c.t < 128 ? g1 : be1)[ch], gbB = (c.t < 128 ? g2 : be2)[ch];
    SB();
    bar_nowait(); SB();
    STAMP(14);
    xraw_issue(c); SB();
    STAMP(15);
    Task0 t0; task0_init(c, t0);
    xraw_transpose(c); SB();
    c.tag = __builtin_amdgcn_readfirstlane(epoch) + 1u;
    { float* gbp = (float*)(smem + L_GB) + (c.t < 128 ? 0 : 128) + ch; gbp[0] = bflo(gbA); gbp[256] = bflo(gbB); }
    STAMP(3);
    {
      const unsigned fo = c.wb + WB_FLAG + (unsigned)((((c.lane >> 3) & 1) * 4 + c.w) * 128 + (c.lane & 7) * 4);
      for (int spin = 0; spin < SPIN_MAX; ++spin) {
        cbar();
        const unsigned f = __builtin_amdgcn_raw_buffer_load_b32(c.rw, fo, 0, SC1);
        if (!__any(f != c.tag)) break;
        __builtin_amdgcn_s_sleep(2);
      }
    }
    STAMP(4);
    const unsigned wvoff = c.wb + WB_WF + (unsigned)c.w * 73728u + (unsigned)c.lane * 16u;
    bf16x8 wr[WD];
#pragma unroll
    for (int k = 0; k < WD; ++k) wr[k] = __builtin_bit_cast(bf16x8, bld_sc1(c.rw, wvoff, (unsigned)k * 1024));
    const int px = c.lane & 15, q = c.lane >> 4;
    bar();
    STAMP(5);
    f32x4 acc[8][2], ini[2];
    ini[0] = *(const f32x4*)(smem + L_EMB + (32 * c.w + 4 * q) * 4); ini[1] = *(const f32x4*)(smem + L_EMB + (32 * c.w + 16 + 4 * q) * 4);
    norm_group0(c, t0, 0);
    bar();
    STAMP(6); DG(1);
    __builtin_amdgcn_s_setprio(1);
    mainloop<0>(c, acc, wr, wvoff, ini);
    __builtin_amdgcn_s_setprio(0);
    STAMP(7); DG(2);
    {
      f32x2 s2 = {0.f, 0.f}, q2 = {0.f, 0.f};
#pragma unroll
      for (int r = 0; r < 8; ++r)
#pragma unroll
        for (int nt = 0; nt < 2; ++nt)
#pragma unroll
          for (int k = 0; k < 2; ++k) { const f32x2 v = {acc[r][nt][2 * k], acc[r][nt][2 * k + 1]}; s2 += v; q2 += v * v; }
      const float s = wave_sum(s2[0] + s2[1]), qs = wave_sum(q2[0] + q2[1]);
      if (c.lane == 0) bst_sc1(u32x4{__float_as_uint(s), c.tag, __float_as_uint(qs), c.tag}, c.rw, c.wb + WB_G2 + (unsigned)(c.w * 128 + c.jb) * 16, 0);
    }
    STAMP(1);
    bar_nowait();
    {
      const int chk = 4 * c.w + (((q & 1) << 1) | (q >> 1));
      const unsigned go = WS_HB + (unsigned)(((c.b * 128 + c.y0) * 128 + c.x0 + px) * 256 + chk * 16);
      char* lp = smem + L_T + RP + (px + 1) * 256 + ((chk ^ swz((px + 1) & 15)) << 4);
      const bool edge = px == 0 || px == 15;
#pragma unroll
      for (int rr = 0; rr < 8; ++rr) {
        const int r = rr == 0 ? 0 : (rr == 1 ? 7 : rr - 1);
        const unsigned d0 = cvtpk(acc[r][0][0], acc[r][0][1]), d1 = cvtpk(acc[r][0][2], acc[r][0][3]);
        const unsigned d2 = cvtpk(acc[r][1][0], acc[r][1][1]), d3 = cvtpk(acc[r][1][2], acc[r][1][3]);
        auto a = __builtin_amdgcn_permlane16_swap(d0, d2, false, false);
        auto e = __builtin_amdgcn_permlane16_swap(d1, d3, false, false);
        const u32x4 v = {a[0], e[0], a[1], e[1]};
        if (r == 0 || r == 7 || edge) bst_sc1(u32x4{min(v[0], 0xfffffffeu), min(v[1], 0xfffffffeu), min(v[2], 0xfffffffeu), min(v[3], 0xfffffffeu)}, c.rw, go, r * 32768);
        *(u32x4*)(lp + r * RP) = v;
      }
    }
    bar();
    STAMP(8);
#pragma unroll
    for (int r = 0; r < 8; ++r) {
      const u32x4 v = *(const u32x4*)(smem + L_RAW + (r + 1) * RS + (32 * c.w + 16 * (q & 1) + px) * 64 + 16 + (q >> 1) * 16);
      auto a = __builtin_amdgcn_permlane16_swap(v[0], v[2], false, false);
      auto e = __builtin_amdgcn_permlane16_swap(v[1], v[3], false, false);
      acc[r][0] = f32x4{bflo(a[0]), bfhi(a[0]), bflo(e[0]), bfhi(e[0])};
      acc[r][1] = f32x4{bflo(a[1]), bfhi(a[1]), bflo(e[1]), bfhi(e[1])};
    }
    pin_acc(acc);
    STAMP(9);
    bar();
    STAMP(10);
    norm_group0(c, t0, 1);
    bar();
    STAMP(11); DG(3);
    __builtin_amdgcn_s_setprio(1);
    mainloop<1>(c, acc, wr, wvoff, ini);
    __builtin_amdgcn_s_setprio(0);
    STAMP(12); DG(4);
    u16* op = out + ((size_t)(c.b * 128 + 32 * c.w + 16 * (q & 1) + px) * 128 + c.y0) * 128 + c.x0 + 8 * (q >> 1);
#pragma unroll
    for (int r = 0; r < 8; ++r) {
      const unsigned d0 = cvtpk(acc[r][0][0], acc[r][0][1]), d1 = cvtpk(acc[r][0][2], acc[r][0][3]);
      const unsigned d2 = cvtpk(acc[r][1][0], acc[r][1][1]), d3 = cvtpk(acc[r][1][2], acc[r][1][3]);
      auto a = __builtin_amdgcn_permlane16_swap(d0, d2, false, false);
      auto e = __builtin_amdgcn_permlane16_swap(d1, d3, false, false);
      *(u32x4*)(op + r * 128) = u32x4{a[0], e[0], a[1], e[1]};
    }
    STAMP(13); STAMP_FLUSH;
    DG(5);
    if (blockIdx.x == DIAG_WG && c.w == 0) {
      const long long val = (long long)(dg[DIAG_B] - dg[DIAG_A]) * DIAG_SCALE;
      const unsigned long long t0_ = __builtin_amdgcn_s_memrealtime();
      while ((long long)(__builtin_amdgcn_s_memrealtime() - t0_) < val) __builtin_amdgcn_s_sleep(1);
    }
  } else {
    STAMP_DECL STAMP(0);
    const int g = c.w - 4;
    const int yrow = (xcd & 3) * 32 + jx;
    __builtin_amdgcn_s_setprio(2);
    const int ksvc = jx * 4 + (xcd & 3);
    const bool svc_w = c.w == 7 && (ksvc < 32 || (ksvc >= 64 && ksvc < 96)), svc_e = c.w == 7 && ksvc >= 32 && ksvc < 64;
    const int wu = ksvc < 32 ? ksvc : ksvc - 32;
    u32x4 sv[9]; unsigned te[6];
    if (svc_w) {
      const u32x4* src = (const u32x4*)(((wu >> 5) ? w2 : w1) + (size_t)((32 * ((wu >> 3) & 3) + (c.lane & 31)) * 128 + 16 * (wu & 7) + 8 * (c.lane >> 5)) * 9);
#pragma unroll
      for (int k = 0; k < 9; ++k) sv[k] = src[k];
    }
    if (svc_e) {
      const int j = ksvc - 32, e0 = 192 * (j >> 3) + (c.lane >> 1);
#pragma unroll
      for (int k = 0; k < 6; ++k) { sv[k] = *(const u32x4*)(w_emb + (size_t)(e0 + 32 * k) * 128 + 16 * (j & 7) + 8 * (c.lane & 1)); te[k] = t_emb[c.b * 768 + e0 + 32 * k]; }
    }
    SB();
    u32x4 xr[8];
    { const unsigned o = (unsigned)(((c.b * 128 + 32 * g + 8 * (c.lane >> 4)) * 128 + yrow) * 128) * 2 + (c.lane & 15) * 16;
#pragma unroll
      for (int jj = 0; jj < 8; ++jj) xr[jj] = bld(c.rx, o, jj * 32768); }
    SB();
    bar_nowait(); SB();
    STAMP(4);
    c.tag = __builtin_amdgcn_readfirstlane(epoch) + 1u;
    {
      float s = 0.f, qs = 0.f;
#pragma unroll
      for (int jj = 0; jj < 8; ++jj)
#pragma unroll
        for (int k = 0; k < 4; ++k) { s = dot2(xr[jj][k], 0x3f803f80u, s); qs = dot2(xr[jj][k], xr[jj][k], qs); }
      s = wave_sum(s); qs = wave_sum(qs);
      if (c.lane == 0) bst_sc1(u32x4{__float_as_uint(s), c.tag, __float_as_uint(qs), c.tag}, c.rw, c.wb + WB_G1 + (unsigned)(g * 128 + c.jb) * 16, 0);
    }
    {
      const int k16 = c.lane & 15;
      if (k16 == 7 || k16 == 8) {
        unsigned v[4];
#pragma unroll
        for (int m = 0; m < 4; ++m) {
          const unsigned a = __builtin_amdgcn_perm(xr[2 * m + 1][3], xr[2 * m][3], 0x07060302u), b2 = __builtin_amdgcn_perm(xr[2 * m + 1][0], xr[2 * m][0], 0x05040100u);
          v[m] = k16 == 7 ? a : b2;
        }
        const unsigned o = c.wb + WB_XC + (unsigned)((yrow * 2 + (k16 - 7)) * 64 + 16 * g + 4 * (c.lane >> 4)) * 8;
        bst_sc1(u32x4{v[0], c.tag, v[1], c.tag}, c.rw, o, 0); bst_sc1(u32x4{v[2], c.tag, v[3], c.tag}, c.rw, o, 16);
      }
    }
    STAMP(1);
    if (svc_w) {
      const int cs = wu & 7, o = c.lane & 31, h = c.lane >> 5;
      const unsigned dst = c.wb + WB_WF + (unsigned)(wu >> 5) * 294912u + (unsigned)((wu >> 3) & 3) * 73728u + (unsigned)(o >> 4) * 1024u + (unsigned)(o & 15) * 16u;
      const unsigned dP = dst + (unsigned)(3 * (cs >> 1) + (cs & 1)) * 6144u + (unsigned)h * 512u;
      const unsigned dD = dst + (unsigned)(3 * (cs >> 1) + 2) * 6144u + (unsigned)(2 * (cs & 1) + h) * 256u;
#pragma unroll
      for (int k = 0; k < 9; ++k) {
        const int dy = k / 3, dx = k % 3;
        u32x4 o4;
#pragma unroll
        for (int m = 0; m < 4; ++m) {
          const int e0 = 18 * m + k, e1 = e0 + 9;
          const unsigned sel = ((e0 & 1) ? 0x0302u : 0x0100u) | ((e1 & 1) ? 0x07060000u : 0x05040000u);
          o4[m] = __builtin_amdgcn_perm(sv[e1 >> 3][(e1 >> 1) & 3], sv[e0 >> 3][(e0 >> 1) & 3], sel);
        }
        bst_sc1(o4, c.rw, dx < 2 ? dP : dD, dy * 2048 + (dx < 2 ? dx * 256 : 0));
      }
      asm volatile("s_waitcnt vmcnt(0)" ::: "memory");
      if (c.lane == 0) __hip_atomic_store((GAS unsigned*)(ws + c.wb + WB_FLAG + (wu >> 3) * 128 + (wu & 7) * 4), c.tag, __ATOMIC_RELAXED, __HIP_MEMORY_SCOPE_AGENT);
    }
    if (svc_e) {
      f32x2 a[4];
#pragma unroll
      for (int m = 0; m < 4; ++m) a[m] = f32x2{0.f, 0.f};
#pragma unroll
      for (int k = 0; k < 6; ++k) {
        const float tv = bflo(te[k]);
        const float sl = tv * __builtin_amdgcn_rcpf(1.f + __builtin_amdgcn_exp2f(-tv * LOG2E));
#pragma unroll
        for (int m = 0; m < 4; ++m) a[m] += f32x2{bflo(sv[k][m]), bfhi(sv[k][m])} * sl;
      }
      float r[8];
#pragma unroll
      for (int m = 0; m < 8; ++m) {
        float v = a[m >> 1][m & 1];
        v = dppadd<0x4E>(v); v = dppadd<0x124>(v); v = dppadd<0x128>(v);
        auto p = __builtin_amdgcn_permlane16_swap(__float_as_uint(v), __float_as_uint(v), false, false);
        v = __uint_as_float(p[0]) + __uint_as_float(p[1]);
        auto p2 = __builtin_amdgcn_permlane32_swap(__float_as_uint(v), __float_as_uint(v), false, false);
        r[m] = __uint_as_float(p2[0]) + __uint_as_float(p2[1]);
      }
      const int j = ksvc - 32;
      if (c.lane < 2) {
        const unsigned o = c.wb + WB_EG + (unsigned)((j >> 3) * 128 + 16 * (j & 7) + 8 * c.lane) * 8;
#pragma unroll
        for (int m = 0; m < 4; ++m) bst_sc1(u32x4{__float_as_uint(r[2 * m]), c.tag, __float_as_uint(r[2 * m + 1]), c.tag}, c.rw, o, m * 16);
      }
    }
    __builtin_amdgcn_s_setprio(0);
    STAMP(2);
    Halo hl; halo_init(c, hl);
#pragma unroll
    for (int r = 0; r < 4; ++r) if (hl.inr[r] && !hl.valid[r]) *(u32x4*)(smem + hl.lo[r]) = u32x4{0, 0, 0, 0};
    Task0 t0; task0_init(c, t0);
    HTask ht; htask_init(c, ht);
    Clr cl; clear_init(c, cl);
    STAMP(3);
    {
      u32x4 v0, v1, ve[4], vc[2]; bool n0 = true, n1 = true, ne = c.w == 4, nc[2];
      const unsigned o = c.wb + WB_G1 + (unsigned)(g * 128 + c.lane) * 16;
      const bool xc = c.tx == 3 || c.tx == 4;
      unsigned co[2], cl[2]; bool cin[2];
#pragma unroll
      for (int r = 0; r < 2; ++r) {
        const int id = c.t - 256 + 256 * r, row = id >> 5, i = id & 31, y = c.y0 - 1 + row, col = c.tx == 3 ? 17 : 0;
        cin[r] = xc && id < 320;
        nc[r] = cin[r] && (unsigned)y < 128u;
        co[r] = c.wb + WB_XC + (unsigned)((y * 2 + (c.tx == 3 ? 1 : 0)) * 64 + 2 * i) * 8;
        cl[r] = L_T + row * RP + col * 256 + (((i >> 1) ^ swz(col & 15)) << 4) + (i & 1) * 8;
        vc[r] = u32x4{0, 0, 0, 0};
      }
      for (int spin = 0; spin < SPIN_MAX; ++spin) {
        cbar();
        if (n0) v0 = bld_sc1(c.rw, o, 0);
        if (n1) v1 = bld_sc1(c.rw, o, 1024);
        if (ne) {
#pragma unroll
          for (int k = 0; k < 4; ++k) ve[k] = bld_sc1(c.rw, c.wb + WB_EG + (unsigned)c.lane * 16, k * 1024);
        }
#pragma unroll
        for (int r = 0; r < 2; ++r) if (nc[r]) vc[r] = bld_sc1(c.rw, co[r], 0);
        n0 = !(v0[1] == c.tag && v0[3] == c.tag); n1 = !(v1[1] == c.tag && v1[3] == c.tag);
        if (ne) { bool ok = true;
#pragma unroll
          for (int k = 0; k < 4; ++k) ok = ok && ve[k][1] == c.tag && ve[k][3] == c.tag;
          ne = !ok; }
#pragma unroll
        for (int r = 0; r < 2; ++r) nc[r] = nc[r] && !(vc[r][1] == c.tag && vc[r][3] == c.tag);
        if (!__any(n0 || n1 || ne || nc[0] || nc[1])) break;
        __builtin_amdgcn_s_sleep(2);
      }
      stats_to_lds(c, g, v0, v1);
      if (c.w == 4) *(f32x2*)(smem + L_EMB + c.lane * 8) = f32x2{(__uint_as_float(ve[0][0]) + __uint_as_float(ve[1][0])) + (__uint_as_float(ve[2][0]) + __uint_as_float(ve[3][0])),
                                                               (__uint_as_float(ve[0][2]) + __uint_as_float(ve[1][2])) + (__uint_as_float(ve[2][2]) + __uint_as_float(ve[3][2]))};
#pragma unroll
      for (int r = 0; r < 2; ++r) if (cin[r]) *(u32x2*)(smem + cl[r]) = u32x2{vc[r][0], vc[r][2]};
    }
    STAMP(6);
    bar();
    if (c.t == 256 && c.jb == 0) __hip_atomic_store((GAS unsigned*)(ws + c.wb + WB_EPOCH), c.tag, __ATOMIC_RELAXED, __HIP_MEMORY_SCOPE_AGENT);
    norm_group0(c, t0, 0);
    bar();
#pragma unroll 1
    for (int k = 1; k < 4; ++k) {
      if (k == 2) clear_borders(c, cl);
      norm_group_h(c, ht, k);
      if (k == 2) asm volatile("s_waitcnt vmcnt(0)" ::: "memory");
      bar();
      if (k == 2 && c.t == 256) __hip_atomic_store((GAS unsigned*)(ws + c.wb + WB_CF + c.jb * 4), c.tag, __ATOMIC_RELAXED, __HIP_MEMORY_SCOPE_AGENT);
    }
    STAMP(7);
    {
      if (c.w == 5) {
        const int dy = (c.lane & 15) / 3 - 1, dx = (c.lane & 15) % 3 - 1;
        const int ny = c.ty + dy, nx = c.tx + dx;
        const bool chk = c.lane < 9 && (unsigned)ny < 16u && (unsigned)nx < 8u;
        for (int spin = 0; spin < SPIN_MAX; ++spin) {
          cbar();
          unsigned f = c.tag;
          if (chk) f = __builtin_amdgcn_raw_buffer_load_b32(c.rw, c.wb + WB_CF + (unsigned)(ny * 8 + nx) * 4, 0, SC1);
          if (!__any(f != c.tag)) break;
          __builtin_amdgcn_s_sleep(2);
        }
      }
      u32x4 hv[4]; bool need[4];
#pragma unroll
      for (int r = 0; r < 4; ++r) { need[r] = hl.valid[r]; hv[r] = u32x4{0, 0, 0, 0}; }
      u32x4 v0, v1; bool n0 = true, n1 = true;
      const unsigned o = c.wb + WB_G2 + (unsigned)(g * 128 + c.lane) * 16;
      bar_nowait();
      bar();
      STAMP(13);
      for (int spin = 0; spin < SPIN_MAX; ++spin) {
        cbar();
#pragma unroll
        for (int r = 0; r < 4; ++r) if (need[r]) hv[r] = bld_sc1(c.rw, hl.go[r], 0);
        if (n0) v0 = bld_sc1(c.rw, o, 0);
        if (n1) v1 = bld_sc1(c.rw, o, 1024);
        bool any = false;
#pragma unroll
        for (int r = 0; r < 4; ++r) { need[r] = need[r] && (hv[r][0] == ~0u || hv[r][1] == ~0u || hv[r][2] == ~0u || hv[r][3] == ~0u); any |= need[r]; }
        n0 = !(v0[1] == c.tag && v0[3] == c.tag); n1 = !(v1[1] == c.tag && v1[3] == c.tag);
        if (!__any(any || n0 || n1)) break;
        __builtin_amdgcn_s_sleep(1);
      }
      STAMP(8);
#pragma unroll
      for (int r = 0; r < 4; ++r) if (hl.valid[r]) *(u32x4*)(smem + hl.lo[r]) = hv[r];
      stats_to_lds(c, g, v0, v1);
    }
    bar();
    norm_group0(c, t0, 1);
    bar();
#pragma unroll 1
    for (int k = 1; k < 4; ++k) { if (k == 1) STAMP(9); norm_group_h(c, ht, k); if (k == 1) STAMP(10); if (k == 2) STAMP(11); bar(); }
    STAMP(12); STAMP_FLUSH;
  }
}

extern "C" void kernel_launch(void* const* d_in, const int* in_sizes, int n_in, void* d_out, int out_size, void* d_ws, size_t ws_size, hipStream_t stream) {
  (void)hipFuncSetAttribute((const void*)k_resblock, hipFuncAttributeMaxDynamicSharedMemorySize, L_END);
  k_resblock<<<256, 512, L_END, stream>>>((const u16*)d_in[0], (const u16*)d_in[1], (const u16*)d_in[2], (const u16*)d_in[3], (const u16*)d_in[4],
                                          (const u16*)d_in[5], (const u16*)d_in[6], (const u16*)d_in[7], (const u16*)d_in[8], (u16*)d_out, (char*)d_ws);
}
```

```cpp
#define OUT_AUX 0
#include <hip/hip_runtime.h>
#include <hip/hip_bf16.h>
#include <stdint.h>

typedef float f32x4 __attribute__((ext_vector_type(4)));
typedef float f32x2 __attribute__((ext_vector_type(2)));
typedef __bf16 bf16x8 __attribute__((ext_vector_type(8)));
typedef __bf16 bf16x2 __attribute__((ext_vector_type(2)));
typedef unsigned u32x4 __attribute__((ext_vector_type(4)));
typedef unsigned u32x2 __attribute__((ext_vector_type(2)));
typedef unsigned short u16;
#define GAS __attribute__((address_space(1)))
#define DI __device__ __forceinline__
#define SB() __builtin_amdgcn_sched_barrier(0)

constexpr int RP = 4608;
constexpr int TILE_BYTES = 10 * RP;
constexpr int WD = 18;
constexpr float LOG2E = 1.4426950408889634f;
constexpr int SC1 = 16;
constexpr int SPIN_MAX = 1 << 16;

constexpr unsigned WB_SIZE = 1u << 20;
constexpr unsigned WB_WF = 0, WB_EPOCH = 589824, WB_FLAG = WB_EPOCH + 256  , WB_G1 = WB_FLAG + 1024, WB_G2 = WB_G1 + 8192,
                   WB_EG = WB_G2 + 8192  , WB_CF = WB_EG + 4096  , WB_XC = WB_CF + 512  ;
constexpr unsigned WS_HB = 2u << 20;
constexpr int RS = 8224;
constexpr int L_T = 0, L_RAW = TILE_BYTES, L_GB = L_RAW + 10 * RS  , L_PAR = L_GB + 2048  , L_EMB = L_PAR + 1024,
              L_STAT = L_EMB + 512  , L_DUMMY = (L_STAT + 64 + 255) / 256 * 256  , L_END = L_DUMMY + 1024;
#define LAS __attribute__((address_space(3)))

DI int swz(int c) { return c ^ ((c >> 1) & 4); }
DI unsigned cvtpk(float lo, float hi) { f32x2 v = {lo, hi}; bf16x2 b = __builtin_convertvector(v, bf16x2); return __builtin_bit_cast(unsigned, b); }
DI unsigned cvtpk_s(float lo, float hi) { unsigned r; asm("v_cvt_pk_bf16_f32 %0, %1, %2" : "=v"(r) : "v"(lo), "v"(hi)); return r; }
DI float bflo(unsigned d) { return __uint_as_float(d << 16); }
DI float bfhi(unsigned d) { return __uint_as_float(d & 0xffff0000u); }
DI float dot2(unsigned a, unsigned b, float c) { return __builtin_amdgcn_fdot2_f32_bf16(__builtin_bit_cast(bf16x2, a), __builtin_bit_cast(bf16x2, b), c, false); }
template <int CTRL> DI float dppadd(float v) { return v + __int_as_float(__builtin_amdgcn_update_dpp(0, __float_as_int(v), CTRL, 0xf, 0xf, true)); }
DI float wave_sum(float v) {
  v = dppadd<0xB1>(v); v = dppadd<0x4E>(v); v = dppadd<0x141>(v); v = dppadd<0x140>(v);
  auto p = __builtin_amdgcn_permlane16_swap(__float_as_uint(v), __float_as_uint(v), false, false);
  v = __uint_as_float(p[0]) + __uint_as_float(p[1]);
  auto q = __builtin_amdgcn_permlane32_swap(__float_as_uint(v), __float_as_uint(v), false, false);
  return __uint_as_float(q[0]) + __uint_as_float(q[1]);
}
DI void bar() { asm volatile("s_waitcnt lgkmcnt(0)\n\ts_barrier" ::: "memory"); }
DI void bar_nowait() { asm volatile("s_barrier" ::: "memory"); }
DI void cbar() { asm volatile("" ::: "memory"); }
typedef __amdgpu_buffer_rsrc_t rsrc_t;
DI rsrc_t mk_rsrc(const void* p, unsigned bytes) { return __builtin_amdgcn_make_buffer_rsrc((void*)p, 0, bytes, 0x00020000); }
DI u32x4 bld(rsrc_t r, unsigned voff, unsigned soff) { return __builtin_amdgcn_raw_buffer_load_b128(r, voff, soff, 0); }
DI u32x4 bld_sc1(rsrc_t r, unsigned voff, unsigned soff) { return __builtin_amdgcn_raw_buffer_load_b128(r, voff, soff, SC1); }
DI void bst_sc1(u32x4 v, rsrc_t r, unsigned voff, unsigned soff) { __builtin_amdgcn_raw_buffer_store_b128(v, r, voff, soff, SC1); }

struct Ctx {
  rsrc_t rx, rw;
  char* smem; char* ws;
  int b, ty, tx, jb, y0, x0, w, lane, t;
  unsigned wb;
  unsigned tag;
};

#define STAMP_DECL
#define STAMP(idx) do {} while (0)
#define STAMP_FLUSH do {} while (0)

DI void stat_par(const char* smem, int gbsel, int grp, int ch, f32x4& a, f32x4& b) {
  const f32x4 g = *(const f32x4*)(smem + L_GB + gbsel * 1024 + ch * 4), be = *(const f32x4*)(smem + L_GB + gbsel * 1024 + 512 + ch * 4);
  const f32x2 st = *(const f32x2*)(smem + L_STAT + grp * 8);
  a = g * (st[1] * LOG2E); b = be * LOG2E - a * st[0];
}
DI u32x2 act4_packed(u32x2 v, f32x4 a, f32x4 b) {
  u32x2 o;
#pragma unroll
  for (int m = 0; m < 2; ++m) {
    f32x2 xx = {bflo(v[m]), bfhi(v[m])}, aa = {a[2 * m], a[2 * m + 1]}, bb = {b[2 * m], b[2 * m + 1]};
    f32x2 u = xx * aa + bb;
    f32x2 e = {__builtin_amdgcn_exp2f(-u[0]), __builtin_amdgcn_exp2f(-u[1])};
    f32x2 d = e * LOG2E + LOG2E;
    f32x2 r = {__builtin_amdgcn_rcpf(d[0]), __builtin_amdgcn_rcpf(d[1])};
    f32x2 y = u * r;
    o[m] = cvtpk(y[0], y[1]);
  }
  return o;
}
DI float act1(float x, float a, float b) { float u = __builtin_fmaf(x, a, b); float e = __builtin_amdgcn_exp2f(-u); return u * __builtin_amdgcn_rcpf(__builtin_fmaf(e, LOG2E, LOG2E)); }
DI u32x2 act4_scalar(u32x2 v, f32x4 a, f32x4 b) {
  u32x2 o;
  o[0] = cvtpk_s(act1(bflo(v[0]), a[0], b[0]), act1(bfhi(v[0]), a[1], b[1]));
  o[1] = cvtpk_s(act1(bflo(v[1]), a[2], b[2]), act1(bfhi(v[1]), a[3], b[3]));
  return o;
}
DI unsigned ntask(const Ctx& c, unsigned px, int c4) {
  const unsigned row = px / 18, col = px - row * 18;
  const unsigned off = L_T + row * RP + col * 256 + (((c4 >> 1) ^ swz(col & 15)) << 4) + (c4 & 1) * 8;
  const bool valid = px < 180 && (unsigned)(c.y0 - 1 + (int)row) < 128u && (unsigned)(c.x0 - 1 + (int)col) < 128u;
  unsigned r = valid ? off : (unsigned)(L_DUMMY + c.lane * 16 + (c4 & 1) * 8);
  asm volatile("" : "+v"(r));
  return r;
}
struct Task0 { unsigned off[3]; };
DI void task0_init(const Ctx& c, Task0& t0) {
#pragma unroll
  for (int r = 0; r < 3; ++r) t0.off[r] = ntask(c, (c.t >> 3) + 64 * r, c.t & 7);
}
DI void norm_group0(const Ctx& c, const Task0& t0, int gbsel) {
  u32x2 v[3];
#pragma unroll
  for (int r = 0; r < 3; ++r) v[r] = *(const u32x2*)(c.smem + t0.off[r]);
  f32x4 a, b; stat_par(c.smem, gbsel, 0, 4 * (c.t & 7), a, b);
#pragma unroll
  for (int r = 0; r < 2; ++r) *(u32x2*)(c.smem + t0.off[r]) = act4_packed(v[r], a, b);
  if (c.w < 7) *(u32x2*)(c.smem + t0.off[2]) = act4_packed(v[2], a, b);
  else {
    if (c.lane < 48) {
      const int ch = 32 + 2 * c.lane;
      const f32x2 g = *(const f32x2*)(c.smem + L_GB + gbsel * 1024 + ch * 4), be = *(const f32x2*)(c.smem + L_GB + gbsel * 1024 + 512 + ch * 4);
      const f32x2 st = *(const f32x2*)(c.smem + L_STAT + (ch >> 5) * 8);
      const f32x2 aa = g * (st[1] * LOG2E);
      *(f32x2*)(c.smem + L_PAR + ch * 4) = aa; *(f32x2*)(c.smem + L_PAR + 512 + ch * 4) = be * LOG2E - aa * st[0];
    }
  }
}
struct HTask { unsigned off[3]; };
DI void htask_init(const Ctx& c, HTask& ht) {
#pragma unroll
  for (int r = 0; r < 3; ++r) ht.off[r] = ntask(c, ((c.t - 256) >> 2) + 64 * r, 2 * (c.t & 3));
}
DI void norm_group_h(const Ctx& c, const HTask& ht, int k) {
  const int ch = 32 * k + 8 * (c.t & 3);
  u32x4 v[3];
#pragma unroll
  for (int r = 0; r < 3; ++r) v[r] = *(const u32x4*)(c.smem + (ht.off[r] ^ (k << 6)));
  const f32x4 a0 = *(const f32x4*)(c.smem + L_PAR + ch * 4), a1 = *(const f32x4*)(c.smem + L_PAR + ch * 4 + 16);
  const f32x4 b0 = *(const f32x4*)(c.smem + L_PAR + 512 + ch * 4), b1 = *(const f32x4*)(c.smem + L_PAR + 512 + ch * 4 + 16);
#pragma unroll
  for (int r = 0; r < 3; ++r) {
    const u32x2 lo = act4_scalar(u32x2{v[r][0], v[r][1]}, a0, b0), hi = act4_scalar(u32x2{v[r][2], v[r][3]}, a1, b1);
    *(u32x4*)(c.smem + (ht.off[r] ^ (k << 6))) = u32x4{lo[0], lo[1], hi[0], hi[1]};
  }
}

DI void xraw_issue(const Ctx& c) {
  int xs = c.x0 - 8 + 8 * (c.lane & 3);
  { const int lo = c.x0 & 64; xs = xs < lo ? lo : (xs > lo + 56 ? lo + 56 : xs); }
  const unsigned vo = (unsigned)(((c.b * 128 + 32 * c.w + (c.lane >> 2)) * 128) * 128 + xs) * 2;
#pragma unroll
  for (int row = 0; row < 10; ++row) {
    const int y = row == 0 ? (c.ty == 0 ? 0 : c.y0 - 1) : row == 9 ? (c.ty == 15 ? 127 : c.y0 + 8) : c.y0 - 1 + row;
#pragma unroll
    for (int cbl = 0; cbl < 2; ++cbl)
      __builtin_amdgcn_raw_ptr_buffer_load_lds(c.rx, (LAS void*)(c.smem + L_RAW + row * RS + (2 * c.w + cbl) * 1024), 16, vo, y * 256 + cbl * 16 * 32768, 0, 0);
  }
}
template <int RPAIR>
DI void xraw_step(const Ctx& c, unsigned ra, unsigned rh, const unsigned (&wa)[4], const unsigned (&wh)[2], bool rowok, bool hok) {
  u32x2 v[6];
  constexpr int RO = (RPAIR & 1) * 2 * RS;
  const unsigned ra2 = ra + (RPAIR >> 1) * 4 * RS, rh2 = rh + (RPAIR >> 1) * 4 * RS;
  asm volatile("s_waitcnt vmcnt(%8)\n\t"
               "ds_read_b64_tr_b16 %0, %6 offset:%9\n\tds_read_b64_tr_b16 %1, %6 offset:%10\n\tds_read_b64_tr_b16 %2, %6 offset:%11\n\tds_read_b64_tr_b16 %3, %6 offset:%12\n\t"
               "ds_read_b64_tr_b16 %4, %7 offset:%9\n\tds_read_b64_tr_b16 %5, %7 offset:%13\n\ts_waitcnt lgkmcnt(0)"
               : "=&v"(v[0]), "=&v"(v[1]), "=&v"(v[2]), "=&v"(v[3]), "=&v"(v[4]), "=&v"(v[5])
               : "v"(ra2), "v"(rh2), "i"(16 - 4 * RPAIR), "i"(RO), "i"(RO + 512), "i"(RO + 1024), "i"(RO + 1536), "i"(RO + 1024) : "memory");
  if (rowok) {
#pragma unroll
    for (int a = 0; a < 4; ++a) asm volatile("ds_write_b64 %0, %1 offset:%2" :: "v"(wa[a]), "v"(v[a]), "i"(RPAIR * 2 * RP) : "memory");
  }
  if (hok) {
#pragma unroll
    for (int hb = 0; hb < 2; ++hb) asm volatile("ds_write_b64 %0, %1 offset:%2" :: "v"(wh[hb]), "v"(v[4 + hb]), "i"(RPAIR * 2 * RP) : "memory");
  }
}
DI void xraw_transpose(const Ctx& c) {
  const unsigned lb = (unsigned)(size_t)(LAS char*)c.smem;
  const int gsel = c.lane >> 4, i = c.lane & 15, qp = i >> 2, p = i & 3;
  const unsigned ra = lb + L_RAW + (gsel & 1) * RS + (4 * (8 * c.w + (gsel >> 1)) + qp) * 64 + 16 + p * 8;
  unsigned wa[4];
#pragma unroll
  for (int a = 0; a < 4; ++a) wa[a] = lb + L_T + (gsel & 1) * RP + (i + 1) * 256 + (((4 * c.w + a) ^ swz((i + 1) & 15)) << 4) + (gsel >> 1) * 8;
  const unsigned rh = lb + L_RAW + (p >> 1) * RS + (4 * (8 * c.w + gsel) + qp) * 64 + ((p & 1) ? 48 : 8);
  const int side = qp & 1, hrow = qp >> 1, hcol = side ? 17 : 0;
  unsigned wh[2];
#pragma unroll
  for (int hb = 0; hb < 2; ++hb) wh[hb] = lb + L_T + hrow * RP + hcol * 256 + (((4 * c.w + 2 * hb + (gsel >> 1)) ^ swz(hcol & 15)) << 4) + (gsel & 1) * 8;
  const bool huse = (side ? p == 0 : p == 3) && (side ? (c.tx != 7 && c.tx != 3) : (c.tx != 0 && c.tx != 4));
  const bool top = c.ty == 0, bot = c.ty == 15;
  xraw_step<0>(c, ra, rh, wa, wh, !(top && (gsel & 1) == 0), huse && !(top && hrow == 0));
  xraw_step<1>(c, ra, rh, wa, wh, true, huse);
  xraw_step<2>(c, ra, rh, wa, wh, true, huse);
  xraw_step<3>(c, ra, rh, wa, wh, true, huse);
  xraw_step<4>(c, ra, rh, wa, wh, !(bot && (gsel & 1) == 1), huse && !(bot && hrow == 1));
}
struct Halo { unsigned go[4], lo[4]; bool inr[4], valid[4]; };
DI void halo_init(const Ctx& c, Halo& h) {
  const int tid = c.t - 256, cg = tid & 15;
#pragma unroll
  for (int r = 0; r < 4; ++r) {
    const int hp = (tid >> 4) + 16 * r;
    int row, col;
    if (hp < 18) { row = 0; col = hp; } else if (hp < 36) { row = 9; col = hp - 18; } else { const int j = hp - 36; row = 1 + (j >> 1); col = (j & 1) ? 17 : 0; }
    h.inr[r] = hp < 52;
    h.valid[r] = h.inr[r] && (unsigned)(c.y0 - 1 + row) < 128u && (unsigned)(c.x0 - 1 + col) < 128u;
    h.go[r] = WS_HB + (unsigned)(((c.b * 128 + c.y0 - 1 + row) * 128 + c.x0 - 1 + col) * 256 + cg * 16);
    h.lo[r] = L_T + row * RP + col * 256 + ((cg ^ swz(col & 15)) << 4);
  }
}
struct Clr { unsigned o[3]; };
DI void clear_init(const Ctx& c, Clr& cl) {
#pragma unroll
  for (int r = 0; r < 3; ++r) {
    int id = c.t - 256 + 256 * r; id = id < 704 ? id : id - 256;
    const int i = id >> 4;
    int ry, rx;
    if (i < 16) { ry = 0; rx = i; } else if (i < 32) { ry = 7; rx = i - 16; } else { const int j = i - 32; ry = 1 + (j >> 1); rx = (j & 1) ? 15 : 0; }
    cl.o[r] = WS_HB + (unsigned)(((c.b * 128 + c.y0 + ry) * 128 + c.x0 + rx) * 256 + (id & 15) * 16);
    asm volatile("" : "+v"(cl.o[r]));
  }
}
DI void clear_borders(const Ctx& c, const Clr& cl) {
  const u32x4 s = {~0u, ~0u, ~0u, ~0u};
#pragma unroll
  for (int r = 0; r < 3; ++r) bst_sc1(s, c.rw, cl.o[r], 0);
}
DI void stats_to_lds(const Ctx& c, int g, const u32x4& v0, const u32x4& v1) {
  const float s = wave_sum(__uint_as_float(v0[0]) + __uint_as_float(v1[0])), q = wave_sum(__uint_as_float(v0[2]) + __uint_as_float(v1[2]));
  const float mean = s * (1.f / 524288.f); float var = q * (1.f / 524288.f) - mean * mean; var = var < 0.f ? 0.f : var;
  if (c.lane == 0) *(f32x2*)(c.smem + L_STAT + g * 8) = f32x2{mean, __builtin_amdgcn_rsqf(var + 1e-5f)};
}

#define WLD bld_sc1
template <int CONV>
DI void mainloop(const Ctx& c, f32x4 (&acc)[8][2], bf16x8 (&wr)[WD], unsigned wvoff, const f32x4 (&ini)[2]) {
  const char* T = c.smem + L_T;
  const int px = c.lane & 15, q = c.lane >> 4;
  const int colP = px + (q & 1), colD = px + 2;
  const unsigned baseP = colP * 256 + (((q >> 1) ^ swz(colP & 15)) << 4), baseD = colD * 256 + ((q ^ swz(colD & 15)) << 4);
  auto rd = [&](int B, int r) -> bf16x8 {
    const int sp = B / 3, ty = B % 3;
    const unsigned a = ty == 2 ? (baseD ^ (sp << 6)) : (baseP ^ ((2 * sp + ty) << 5));
    return *(const bf16x8*)(T + a + r * RP);
  };
  bf16x8 xf[4];
#pragma unroll
  for (int r = 0; r < 3; ++r) xf[r] = rd(0, r);
#pragma unroll
  for (int B = 0; B < 12; ++B) {
#pragma unroll
    for (int r = 0; r < 10; ++r) {
      const int G = B * 10 + r;
      if (r == 7 && (B % 3) == 2 && B < 11) { bar_nowait(); SB(); }
      { const int Gn = G + 3; if (Gn < 120) xf[Gn & 3] = rd(Gn / 10, Gn % 10); }
#pragma unroll
      for (int dy = 0; dy < 3; ++dy) {
        const int row = r - dy;
        if (row >= 0 && row < 8) {
#pragma unroll
          for (int nt = 0; nt < 2; ++nt) {
            const int ts = 6 * B + 2 * dy + nt;
            if (CONV == 1) asm volatile("v_mfma_f32_16x16x32_bf16 %0, %1, %2, %0" : "+v"(acc[row][nt]) : "v"(xf[G & 3]), "v"(wr[ts % WD]));
            else if (B == 0 && dy == 0) asm volatile("v_mfma_f32_16x16x32_bf16 %0, %1, %2, %3" : "=&v"(acc[row][nt]) : "v"(wr[ts % WD]), "v"(xf[G & 3]), "v"(ini[nt]));
            else           asm volatile("v_mfma_f32_16x16x32_bf16 %0, %1, %2, %0" : "+v"(acc[row][nt]) : "v"(wr[ts % WD]), "v"(xf[G & 3]));
            if (row == 7) { const int tn = CONV * 72 + ts + WD; if (tn < 144) wr[ts % WD] = __builtin_bit_cast(bf16x8, WLD(c.rw, wvoff, (unsigned)tn * 1024 + (tn >= 72 ? 294912u - 72u * 1024u : 0u))); }
            SB();
          }
        }
      }
    }
  }
  asm volatile("s_nop 15" ::: "memory");
  SB();
}

DI void pin_acc(f32x4 (&acc)[8][2]) {
#pragma unroll
  for (int r = 0; r < 8; ++r) asm volatile("" : "+v"(acc[r][0]), "+v"(acc[r][1]));
}
__global__ __launch_bounds__(512, 2) void k_resblock(const u16* __restrict__ x, const u16* __restrict__ t_emb, const u16* __restrict__ g1, const u16* __restrict__ be1,
                                                     const u16* __restrict__ w1, const u16* __restrict__ w_emb, const u16* __restrict__ g2, const u16* __restrict__ be2,
                                                     const u16* __restrict__ w2, u16* __restrict__ out, char* __restrict__ ws) {
  extern __shared__ __attribute__((aligned(256))) char smem[];
  Ctx c;
  c.smem = smem; c.ws = ws;
  c.t = threadIdx.x; c.lane = c.t & 63; c.w = __builtin_amdgcn_readfirstlane(c.t >> 6);
  const int xcd = blockIdx.x & 7, jx = blockIdx.x >> 3;
  c.b = xcd >> 2; c.ty = (xcd & 3) * 4 + (jx >> 3); c.tx = jx & 7;
  c.jb = c.ty * 8 + c.tx; c.y0 = c.ty * 8; c.x0 = c.tx * 16;
  c.rx = mk_rsrc(x, 8u << 20); c.rw = mk_rsrc(ws, 16u << 20);
  c.wb = (unsigned)c.b * WB_SIZE;
  const unsigned epoch = __hip_atomic_load((GAS unsigned*)(ws + c.wb + WB_EPOCH), __ATOMIC_RELAXED, __HIP_MEMORY_SCOPE_AGENT);

  if (c.w < 4) {
    STAMP_DECL STAMP(0);
    const int ch = c.t & 127;
    const unsigned gbA = (c.t < 128 ? g1 : be1)[ch], gbB = (c.t < 128 ? g2 : be2)[ch];
    SB();
    bar_nowait(); SB();
    STAMP(14);
    xraw_issue(c); SB();
    STAMP(15);
    Task0 t0; task0_init(c, t0);
    xraw_transpose(c); SB();
    c.tag = __builtin_amdgcn_readfirstlane(epoch) + 1u;
    { float* gbp = (float*)(smem + L_GB) + (c.t < 128 ? 0 : 128) + ch; gbp[0] = bflo(gbA); gbp[256] = bflo(gbB); }
    STAMP(3);
    {
      const unsigned fo = c.wb + WB_FLAG + (unsigned)((((c.lane >> 3) & 1) * 4 + c.w) * 128 + (c.lane & 7) * 4);
      for (int spin = 0; spin < SPIN_MAX; ++spin) {
        cbar();
        const unsigned f = __builtin_amdgcn_raw_buffer_load_b32(c.rw, fo, 0, SC1);
        if (!__any(f != c.tag)) break;
        __builtin_amdgcn_s_sleep(2);
      }
    }
    STAMP(4);
    const unsigned wvoff = c.wb + WB_WF + (unsigned)c.w * 73728u + (unsigned)c.lane * 16u;
    bf16x8 wr[WD];
#pragma unroll
    for (int k = 0; k < WD; ++k) wr[k] = __builtin_bit_cast(bf16x8, bld_sc1(c.rw, wvoff, (unsigned)k * 1024));
    const int px = c.lane & 15, q = c.lane >> 4;
    bar();
    STAMP(5);
    f32x4 acc[8][2], ini[2];
    ini[0] = *(const f32x4*)(smem + L_EMB + (32 * c.w + 4 * q) * 4); ini[1] = *(const f32x4*)(smem + L_EMB + (32 * c.w + 16 + 4 * q) * 4);
    norm_group0(c, t0, 0);
    bar();
    STAMP(6);
    __builtin_amdgcn_s_setprio(1);
    mainloop<0>(c, acc, wr, wvoff, ini);
    __builtin_amdgcn_s_setprio(0);
    STAMP(7);
    {
      f32x2 s2 = {0.f, 0.f}, q2 = {0.f, 0.f};
#pragma unroll
      for (int r = 0; r < 8; ++r)
#pragma unroll
        for (int nt = 0; nt < 2; ++nt)
#pragma unroll
          for (int k = 0; k < 2; ++k) { const f32x2 v = {acc[r][nt][2 * k], acc[r][nt][2 * k + 1]}; s2 += v; q2 += v * v; }
      const float s = wave_sum(s2[0] + s2[1]), qs = wave_sum(q2[0] + q2[1]);
      if (c.lane == 0) bst_sc1(u32x4{__float_as_uint(s), c.tag, __float_as_uint(qs), c.tag}, c.rw, c.wb + WB_G2 + (unsigned)(c.w * 128 + c.jb) * 16, 0);
    }
    STAMP(1);
    bar_nowait();
    {
      const int chk = 4 * c.w + (((q & 1) << 1) | (q >> 1));
      const unsigned go = WS_HB + (unsigned)(((c.b * 128 + c.y0) * 128 + c.x0 + px) * 256 + chk * 16);
      char* lp = smem + L_T + RP + (px + 1) * 256 + ((chk ^ swz((px + 1) & 15)) << 4);
      const bool edge = px == 0 || px == 15;
#pragma unroll
      for (int rr = 0; rr < 8; ++rr) {
        const int r = rr == 0 ? 0 : (rr == 1 ? 7 : rr - 1);
        const unsigned d0 = cvtpk(acc[r][0][0], acc[r][0][1]), d1 = cvtpk(acc[r][0][2], acc[r][0][3]);
        const unsigned d2 = cvtpk(acc[r][1][0], acc[r][1][1]), d3 = cvtpk(acc[r][1][2], acc[r][1][3]);
        auto a = __builtin_amdgcn_permlane16_swap(d0, d2, false, false);
        auto e = __builtin_amdgcn_permlane16_swap(d1, d3, false, false);
        const u32x4 v = {a[0], e[0], a[1], e[1]};
        if (r == 0 || r == 7 || edge) bst_sc1(u32x4{min(v[0], 0xfffffffeu), min(v[1], 0xfffffffeu), min(v[2], 0xfffffffeu), min(v[3], 0xfffffffeu)}, c.rw, go, r * 32768);
        *(u32x4*)(lp + r * RP) = v;
      }
    }
    bar();
    STAMP(8);
#pragma unroll
    for (int r = 0; r < 8; ++r) {
      const u32x4 v = *(const u32x4*)(smem + L_RAW + (r + 1) * RS + (32 * c.w + 16 * (q & 1) + px) * 64 + 16 + (q >> 1) * 16);
      auto a = __builtin_amdgcn_permlane16_swap(v[0], v[2], false, false);
      auto e = __builtin_amdgcn_permlane16_swap(v[1], v[3], false, false);
      acc[r][0] = f32x4{bflo(a[0]), bfhi(a[0]), bflo(e[0]), bfhi(e[0])};
      acc[r][1] = f32x4{bflo(a[1]), bfhi(a[1]), bflo(e[1]), bfhi(e[1])};
    }
    pin_acc(acc);
    STAMP(9);
    bar();
    STAMP(10);
    norm_group0(c, t0, 1);
    bar();
    STAMP(11);
    __builtin_amdgcn_s_setprio(1);
    mainloop<1>(c, acc, wr, wvoff, ini);
    __builtin_amdgcn_s_setprio(0);
    STAMP(12);
    const rsrc_t ro = mk_rsrc(out, 8u << 20);
    const unsigned oo = (unsigned)(((c.b * 128 + 32 * c.w + 16 * (q & 1) + px) * 128 + c.y0) * 128 + c.x0 + 8 * (q >> 1)) * 2;
#pragma unroll
    for (int r = 0; r < 8; ++r) {
      const unsigned d0 = cvtpk(acc[r][0][0], acc[r][0][1]), d1 = cvtpk(acc[r][0][2], acc[r][0][3]);
      const unsigned d2 = cvtpk(acc[r][1][0], acc[r][1][1]), d3 = cvtpk(acc[r][1][2], acc[r][1][3]);
      auto a = __builtin_amdgcn_permlane16_swap(d0, d2, false, false);
      auto e = __builtin_amdgcn_permlane16_swap(d1, d3, false, false);
      __builtin_amdgcn_raw_buffer_store_b128(u32x4{a[0], e[0], a[1], e[1]}, ro, oo, r * 256, OUT_AUX);
    }
    STAMP(13); STAMP_FLUSH;
  } else {
    STAMP_DECL STAMP(0);
    const int g = c.w - 4;
    const int yrow = (xcd & 3) * 32 + jx;
    __builtin_amdgcn_s_setprio(2);
    const int ksvc = jx * 4 + (xcd & 3);
    const bool svc_w = c.w == 7 && (ksvc < 32 || (ksvc >= 64 && ksvc < 96)), svc_e = c.w == 7 && ksvc >= 32 && ksvc < 64;
    const int wu = ksvc < 32 ? ksvc : ksvc - 32;
    u32x4 sv[9]; unsigned te[6];
    if (svc_w) {
      const u32x4* src = (const u32x4*)(((wu >> 5) ? w2 : w1) + (size_t)((32 * ((wu >> 3) & 3) + (c.lane & 31)) * 128 + 16 * (wu & 7) + 8 * (c.lane >> 5)) * 9);
#pragma unroll
      for (int k = 0; k < 9; ++k) sv[k] = src[k];
    }
    if (svc_e) {
      const int j = ksvc - 32, e0 = 192 * (j >> 3) + (c.lane >> 1);
#pragma unroll
      for (int k = 0; k < 6; ++k) { sv[k] = *(const u32x4*)(w_emb + (size_t)(e0 + 32 * k) * 128 + 16 * (j & 7) + 8 * (c.lane & 1)); te[k] = t_emb[c.b * 768 + e0 + 32 * k]; }
    }
    SB();
    u32x4 xr[8];
    { const unsigned o = (unsigned)(((c.b * 128 + 32 * g + 8 * (c.lane >> 4)) * 128 + yrow) * 128) * 2 + (c.lane & 15) * 16;
#pragma unroll
      for (int jj = 0; jj < 8; ++jj) xr[jj] = bld(c.rx, o, jj * 32768); }
    SB();
    bar_nowait(); SB();
    STAMP(4);
    c.tag = __builtin_amdgcn_readfirstlane(epoch) + 1u;
    {
      float s = 0.f, qs = 0.f;
#pragma unroll
      for (int jj = 0; jj < 8; ++jj)
#pragma unroll
        for (int k = 0; k < 4; ++k) { s = dot2(xr[jj][k], 0x3f803f80u, s); qs = dot2(xr[jj][k], xr[jj][k], qs); }
      s = wave_sum(s); qs = wave_sum(qs);
      if (c.lane == 0) bst_sc1(u32x4{__float_as_uint(s), c.tag, __float_as_uint(qs), c.tag}, c.rw, c.wb + WB_G1 + (unsigned)(g * 128 + c.jb) * 16, 0);
    }
    {
      const int k16 = c.lane & 15;
      if (k16 == 7 || k16 == 8) {
        unsigned v[4];
#pragma unroll
        for (int m = 0; m < 4; ++m) {
          const unsigned a = __builtin_amdgcn_perm(xr[2 * m + 1][3], xr[2 * m][3], 0x07060302u), b2 = __builtin_amdgcn_perm(xr[2 * m + 1][0], xr[2 * m][0], 0x05040100u);
          v[m] = k16 == 7 ? a : b2;
        }
        const unsigned o = c.wb + WB_XC + (unsigned)((yrow * 2 + (k16 - 7)) * 64 + 16 * g + 4 * (c.lane >> 4)) * 8;
        bst_sc1(u32x4{v[0], c.tag, v[1], c.tag}, c.rw, o, 0); bst_sc1(u32x4{v[2], c.tag, v[3], c.tag}, c.rw, o, 16);
      }
    }
    STAMP(1);
    if (svc_w) {
      const int cs = wu & 7, o = c.lane & 31, h = c.lane >> 5;
      const unsigned dst = c.wb + WB_WF + (unsigned)(wu >> 5) * 294912u + (unsigned)((wu >> 3) & 3) * 73728u + (unsigned)(o >> 4) * 1024u + (unsigned)(o & 15) * 16u;
      const unsigned dP = dst + (unsigned)(3 * (cs >> 1) + (cs & 1)) * 6144u + (unsigned)h * 512u;
      const unsigned dD = dst + (unsigned)(3 * (cs >> 1) + 2) * 6144u + (unsigned)(2 * (cs & 1) + h) * 256u;
#pragma unroll
      for (int k = 0; k < 9; ++k) {
        const int dy = k / 3, dx = k % 3;
        u32x4 o4;
#pragma unroll
        for (int m = 0; m < 4; ++m) {
          const int e0 = 18 * m + k, e1 = e0 + 9;
          const unsigned sel = ((e0 & 1) ? 0x0302u : 0x0100u) | ((e1 & 1) ? 0x07060000u : 0x05040000u);
          o4[m] = __builtin_amdgcn_perm(sv[e1 >> 3][(e1 >> 1) & 3], sv[e0 >> 3][(e0 >> 1) & 3], sel);
        }
        bst_sc1(o4, c.rw, dx < 2 ? dP : dD, dy * 2048 + (dx < 2 ? dx * 256 : 0));
      }
      asm volatile("s_waitcnt vmcnt(0)" ::: "memory");
      if (c.lane == 0) __hip_atomic_store((GAS unsigned*)(ws + c.wb + WB_FLAG + (wu >> 3) * 128 + (wu & 7) * 4), c.tag, __ATOMIC_RELAXED, __HIP_MEMORY_SCOPE_AGENT);
    }
    if (svc_e) {
      f32x2 a[4];
#pragma unroll
      for (int m = 0; m < 4; ++m) a[m] = f32x2{0.f, 0.f};
#pragma unroll
      for (int k = 0; k < 6; ++k) {
        const float tv = bflo(te[k]);
        const float sl = tv * __builtin_amdgcn_rcpf(1.f + __builtin_amdgcn_exp2f(-tv * LOG2E));
#pragma unroll
        for (int m = 0; m < 4; ++m) a[m] += f32x2{bflo(sv[k][m]), bfhi(sv[k][m])} * sl;
      }
      float r[8];
#pragma unroll
      for (int m = 0; m < 8; ++m) {
        float v = a[m >> 1][m & 1];
        v = dppadd<0x4E>(v); v = dppadd<0x124>(v); v = dppadd<0x128>(v);
        auto p = __builtin_amdgcn_permlane16_swap(__float_as_uint(v), __float_as_uint(v), false, false);
        v = __uint_as_float(p[0]) + __uint_as_float(p[1]);
        auto p2 = __builtin_amdgcn_permlane32_swap(__float_as_uint(v), __float_as_uint(v), false, false);
        r[m] = __uint_as_float(p2[0]) + __uint_as_float(p2[1]);
      }
      const int j = ksvc - 32;
      if (c.lane < 2) {
        const unsigned o = c.wb + WB_EG + (unsigned)((j >> 3) * 128 + 16 * (j & 7) + 8 * c.lane) * 8;
#pragma unroll
        for (int m = 0; m < 4; ++m) bst_sc1(u32x4{__float_as_uint(r[2 * m]), c.tag, __float_as_uint(r[2 * m + 1]), c.tag}, c.rw, o, m * 16);
      }
    }
    __builtin_amdgcn_s_setprio(0);
    STAMP(2);
    Halo hl; halo_init(c, hl);
#pragma unroll
    for (int r = 0; r < 4; ++r) if (hl.inr[r] && !hl.valid[r]) *(u32x4*)(smem + hl.lo[r]) = u32x4{0, 0, 0, 0};
    Task0 t0; task0_init(c, t0);
    HTask ht; htask_init(c, ht);
    Clr cl; clear_init(c, cl);
    STAMP(3);
    {
      u32x4 v0, v1, ve[4], vc[2]; bool n0 = true, n1 = true, ne = c.w == 4, nc[2];
      const unsigned o = c.wb + WB_G1 + (unsigned)(g * 128 + c.lane) * 16;
      const bool xc = c.tx == 3 || c.tx == 4;
      unsigned co[2], cl[2]; bool cin[2];
#pragma unroll
      for (int r = 0; r < 2; ++r) {
        const int id = c.t - 256 + 256 * r, row = id >> 5, i = id & 31, y = c.y0 - 1 + row, col = c.tx == 3 ? 17 : 0;
        cin[r] = xc && id < 320;
        nc[r] = cin[r] && (unsigned)y < 128u;
        co[r] = c.wb + WB_XC + (unsigned)((y * 2 + (c.tx == 3 ? 1 : 0)) * 64 + 2 * i) * 8;
        cl[r] = L_T + row * RP + col * 256 + (((i >> 1) ^ swz(col & 15)) << 4) + (i & 1) * 8;
        vc[r] = u32x4{0, 0, 0, 0};
      }
      for (int spin = 0; spin < SPIN_MAX; ++spin) {
        cbar();
        if (n0) v0 = bld_sc1(c.rw, o, 0);
        if (n1) v1 = bld_sc1(c.rw, o, 1024);
        if (ne) {
#pragma unroll
          for (int k = 0; k < 4; ++k) ve[k] = bld_sc1(c.rw, c.wb + WB_EG + (unsigned)c.lane * 16, k * 1024);
        }
#pragma unroll
        for (int r = 0; r < 2; ++r) if (nc[r]) vc[r] = bld_sc1(c.rw, co[r], 0);
        n0 = !(v0[1] == c.tag && v0[3] == c.tag); n1 = !(v1[1] == c.tag && v1[3] == c.tag);
        if (ne) { bool ok = true;
#pragma unroll
          for (int k = 0; k < 4; ++k) ok = ok && ve[k][1] == c.tag && ve[k][3] == c.tag;
          ne = !ok; }
#pragma unroll
        for (int r = 0; r < 2; ++r) nc[r] = nc[r] && !(vc[r][1] == c.tag && vc[r][3] == c.tag);
        if (!__any(n0 || n1 || ne || nc[0] || nc[1])) break;
        __builtin_amdgcn_s_sleep(2);
      }
      stats_to_lds(c, g, v0, v1);
      if (c.w == 4) *(f32x2*)(smem + L_EMB + c.lane * 8) = f32x2{(__uint_as_float(ve[0][0]) + __uint_as_float(ve[1][0])) + (__uint_as_float(ve[2][0]) + __uint_as_float(ve[3][0])),
                                                               (__uint_as_float(ve[0][2]) + __uint_as_float(ve[1][2])) + (__uint_as_float(ve[2][2]) + __uint_as_float(ve[3][2]))};
#pragma unroll
      for (int r = 0; r < 2; ++r) if (cin[r]) *(u32x2*)(smem + cl[r]) = u32x2{vc[r][0], vc[r][2]};
    }
    STAMP(6);
    bar();
    if (c.t == 256 && c.jb == 0) __hip_atomic_store((GAS unsigned*)(ws + c.wb + WB_EPOCH), c.tag, __ATOMIC_RELAXED, __HIP_MEMORY_SCOPE_AGENT);
    norm_group0(c, t0, 0);
    bar();
#pragma unroll 1
    for (int k = 1; k < 4; ++k) {
      if (k == 2) clear_borders(c, cl);
      norm_group_h(c, ht, k);
      if (k == 2) asm volatile("s_waitcnt vmcnt(0)" ::: "memory");
      bar();
      if (k == 2 && c.t == 256) __hip_atomic_store((GAS unsigned*)(ws + c.wb + WB_CF + c.jb * 4), c.tag, __ATOMIC_RELAXED, __HIP_MEMORY_SCOPE_AGENT);
    }
    STAMP(7);
    {
      if (c.w == 5) {
        const int dy = (c.lane & 15) / 3 - 1, dx = (c.lane & 15) % 3 - 1;
        const int ny = c.ty + dy, nx = c.tx + dx;
        const bool chk = c.lane < 9 && (unsigned)ny < 16u && (unsigned)nx < 8u;
        for (int spin = 0; spin < SPIN_MAX; ++spin) {
          cbar();
          unsigned f = c.tag;
          if (chk) f = __builtin_amdgcn_raw_buffer_load_b32(c.rw, c.wb + WB_CF + (unsigned)(ny * 8 + nx) * 4, 0, SC1);
          if (!__any(f != c.tag)) break;
          __builtin_amdgcn_s_sleep(2);
        }
      }
      u32x4 hv[4]; bool need[4];
#pragma unroll
      for (int r = 0; r < 4; ++r) { need[r] = hl.valid[r]; hv[r] = u32x4{0, 0, 0, 0}; }
      u32x4 v0, v1; bool n0 = true, n1 = true;
      const unsigned o = c.wb + WB_G2 + (unsigned)(g * 128 + c.lane) * 16;
      bar_nowait();
      bar();
      STAMP(13);
      for (int spin = 0; spin < SPIN_MAX; ++spin) {
        cbar();
#pragma unroll
        for (int r = 0; r < 4; ++r) if (need[r]) hv[r] = bld_sc1(c.rw, hl.go[r], 0);
        if (n0) v0 = bld_sc1(c.rw, o, 0);
        if (n1) v1 = bld_sc1(c.rw, o, 1024);
        bool any = false;
#pragma unroll
        for (int r = 0; r < 4; ++r) { need[r] = need[r] && (hv[r][0] == ~0u || hv[r][1] == ~0u || hv[r][2] == ~0u || hv[r][3] == ~0u); any |= need[r]; }
        n0 = !(v0[1] == c.tag && v0[3] == c.tag); n1 = !(v1[1] == c.tag && v1[3] == c.tag);
        if (!__any(any || n0 || n1)) break;
        __builtin_amdgcn_s_sleep(1);
      }
      STAMP(8);
#pragma unroll
      for (int r = 0; r < 4; ++r) if (hl.valid[r]) *(u32x4*)(smem + hl.lo[r]) = hv[r];
      stats_to_lds(c, g, v0, v1);
    }
    bar();
    norm_group0(c, t0, 1);
    bar();
#pragma unroll 1
    for (int k = 1; k < 4; ++k) { if (k == 1) STAMP(9); norm_group_h(c, ht, k); if (k == 1) STAMP(10); if (k == 2) STAMP(11); bar(); }
    STAMP(12); STAMP_FLUSH;
  }
}

extern "C" void kernel_launch(void* const* d_in, const int* in_sizes, int n_in, void* d_out, int out_size, void* d_ws, size_t ws_size, hipStream_t stream) {
  (void)hipFuncSetAttribute((const void*)k_resblock, hipFuncAttributeMaxDynamicSharedMemorySize, L_END);
  k_resblock<<<256, 512, L_END, stream>>>((const u16*)d_in[0], (const u16*)d_in[1], (const u16*)d_in[2], (const u16*)d_in[3], (const u16*)d_in[4],
                                          (const u16*)d_in[5], (const u16*)d_in[6], (const u16*)d_in[7], (const u16*)d_in[8], (u16*)d_out, (char*)d_ws);
}
```

```cpp
#define DIAG 1
#define DIAG_WG 100
#define DIAG_WAVE 0
#define DIAG_A 0
#define DIAG_B 5
#define DIAG_SCALE 0
#include <hip/hip_runtime.h>
#include <hip/hip_bf16.h>
#include <stdint.h>

typedef float f32x4 __attribute__((ext_vector_type(4)));
typedef float f32x2 __attribute__((ext_vector_type(2)));
typedef __bf16 bf16x8 __attribute__((ext_vector_type(8)));
typedef __bf16 bf16x2 __attribute__((ext_vector_type(2)));
typedef unsigned u32x4 __attribute__((ext_vector_type(4)));
typedef unsigned u32x2 __attribute__((ext_vector_type(2)));
typedef unsigned short u16;
#define GAS __attribute__((address_space(1)))
#define DI __device__ __forceinline__
#define SB() __builtin_amdgcn_sched_barrier(0)

constexpr int RP = 4608;
constexpr int TILE_BYTES = 10 * RP;
constexpr int WD = 18;
constexpr float LOG2E = 1.4426950408889634f;
constexpr int SC1 = 16;
constexpr int SPIN_MAX = 1 << 16;

constexpr unsigned WB_SIZE = 1u << 20;
constexpr unsigned WB_WF = 0, WB_EPOCH = 589824, WB_FLAG = WB_EPOCH + 256  , WB_G1 = WB_FLAG + 1024, WB_G2 = WB_G1 + 8192,
                   WB_EG = WB_G2 + 8192  , WB_CF = WB_EG + 4096  , WB_XC = WB_CF + 512  ;
constexpr unsigned WS_HB = 2u << 20;
constexpr int RS = 8224;
constexpr int L_T = 0, L_RAW = TILE_BYTES, L_GB = L_RAW + 10 * RS  , L_PAR = L_GB + 2048  , L_EMB = L_PAR + 1024,
              L_STAT = L_EMB + 512  , L_DUMMY = (L_STAT + 64 + 255) / 256 * 256  , L_END = L_DUMMY + 1024;
#define LAS __attribute__((address_space(3)))

DI int swz(int c) { return c ^ ((c >> 1) & 4); }
DI unsigned cvtpk(float lo, float hi) { f32x2 v = {lo, hi}; bf16x2 b = __builtin_convertvector(v, bf16x2); return __builtin_bit_cast(unsigned, b); }
DI unsigned cvtpk_s(float lo, float hi) { unsigned r; asm("v_cvt_pk_bf16_f32 %0, %1, %2" : "=v"(r) : "v"(lo), "v"(hi)); return r; }
DI float bflo(unsigned d) { return __uint_as_float(d << 16); }
DI float bfhi(unsigned d) { return __uint_as_float(d & 0xffff0000u); }
DI float dot2(unsigned a, unsigned b, float c) { return __builtin_amdgcn_fdot2_f32_bf16(__builtin_bit_cast(bf16x2, a), __builtin_bit_cast(bf16x2, b), c, false); }
template <int CTRL> DI float dppadd(float v) { return v + __int_as_float(__builtin_amdgcn_update_dpp(0, __float_as_int(v), CTRL, 0xf, 0xf, true)); }
DI float wave_sum(float v) {
  v = dppadd<0xB1>(v); v = dppadd<0x4E>(v); v = dppadd<0x141>(v); v = dppadd<0x140>(v);
  auto p = __builtin_amdgcn_permlane16_swap(__float_as_uint(v), __float_as_uint(v), false, false);
  v = __uint_as_float(p[0]) + __uint_as_float(p[1]);
  auto q = __builtin_amdgcn_permlane32_swap(__float_as_uint(v), __float_as_uint(v), false, false);
  return __uint_as_float(q[0]) + __uint_as_float(q[1]);
}
DI void bar() { asm volatile("s_waitcnt lgkmcnt(0)\n\ts_barrier" ::: "memory"); }
DI void bar_nowait() { asm volatile("s_barrier" ::: "memory"); }
DI void cbar() { asm volatile("" ::: "memory"); }
typedef __amdgpu_buffer_rsrc_t rsrc_t;
DI rsrc_t mk_rsrc(const void* p, unsigned bytes) { return __builtin_amdgcn_make_buffer_rsrc((void*)p, 0, bytes, 0x00020000); }
DI u32x4 bld(rsrc_t r, unsigned voff, unsigned soff) { return __builtin_amdgcn_raw_buffer_load_b128(r, voff, soff, 0); }
DI u32x4 bld_sc1(rsrc_t r, unsigned voff, unsigned soff) { return __builtin_amdgcn_raw_buffer_load_b128(r, voff, soff, SC1); }
DI void bst_sc1(u32x4 v, rsrc_t r, unsigned voff, unsigned soff) { __builtin_amdgcn_raw_buffer_store_b128(v, r, voff, soff, SC1); }

struct Ctx {
  rsrc_t rx, rw;
  char* smem; char* ws;
  int b, ty, tx, jb, y0, x0, w, lane, t;
  unsigned wb;
  unsigned tag;
};

#define STAMP_DECL
#define STAMP(idx) do {} while (0)
#define STAMP_FLUSH do {} while (0)

DI void stat_par(const char* smem, int gbsel, int grp, int ch, f32x4& a, f32x4& b) {
  const f32x4 g = *(const f32x4*)(smem + L_GB + gbsel * 1024 + ch * 4), be = *(const f32x4*)(smem + L_GB + gbsel * 1024 + 512 + ch * 4);
  const f32x2 st = *(const f32x2*)(smem + L_STAT + grp * 8);
  a = g * (st[1] * LOG2E); b = be * LOG2E - a * st[0];
}
DI u32x2 act4_packed(u32x2 v, f32x4 a, f32x4 b) {
  u32x2 o;
#pragma unroll
  for (int m = 0; m < 2; ++m) {
    f32x2 xx = {bflo(v[m]), bfhi(v[m])}, aa = {a[2 * m], a[2 * m + 1]}, bb = {b[2 * m], b[2 * m + 1]};
    f32x2 u = xx * aa + bb;
    f32x2 e = {__builtin_amdgcn_exp2f(-u[0]), __builtin_amdgcn_exp2f(-u[1])};
    f32x2 d = e * LOG2E + LOG2E;
    f32x2 r = {__builtin_amdgcn_rcpf(d[0]), __builtin_amdgcn_rcpf(d[1])};
    f32x2 y = u * r;
    o[m] = cvtpk(y[0], y[1]);
  }
  return o;
}
DI float act1(float x, float a, float b) { float u = __builtin_fmaf(x, a, b); float e = __builtin_amdgcn_exp2f(-u); return u * __builtin_amdgcn_rcpf(__builtin_fmaf(e, LOG2E, LOG2E)); }
DI u32x2 act4_scalar(u32x2 v, f32x4 a, f32x4 b) {
  u32x2 o;
  o[0] = cvtpk_s(act1(bflo(v[0]), a[0], b[0]), act1(bfhi(v[0]), a[1], b[1]));
  o[1] = cvtpk_s(act1(bflo(v[1]), a[2], b[2]), act1(bfhi(v[1]), a[3], b[3]));
  return o;
}
DI unsigned ntask(const Ctx& c, unsigned px, int c4) {
  const unsigned row = px / 18, col = px - row * 18;
  const unsigned off = L_T + row * RP + col * 256 + (((c4 >> 1) ^ swz(col & 15)) << 4) + (c4 & 1) * 8;
  const bool valid = px < 180 && (unsigned)(c.y0 - 1 + (int)row) < 128u && (unsigned)(c.x0 - 1 + (int)col) < 128u;
  unsigned r = valid ? off : (unsigned)(L_DUMMY + c.lane * 16 + (c4 & 1) * 8);
  asm volatile("" : "+v"(r));
  return r;
}
struct Task0 { unsigned off[3]; };
DI void task0_init(const Ctx& c, Task0& t0) {
#pragma unroll
  for (int r = 0; r < 3; ++r) t0.off[r] = ntask(c, (c.t >> 3) + 64 * r, c.t & 7);
}
DI void norm_group0(const Ctx& c, const Task0& t0, int gbsel) {
  u32x2 v[3];
#pragma unroll
  for (int r = 0; r < 3; ++r) v[r] = *(const u32x2*)(c.smem + t0.off[r]);
  f32x4 a, b; stat_par(c.smem, gbsel, 0, 4 * (c.t & 7), a, b);
#pragma unroll
  for (int r = 0; r < 2; ++r) *(u32x2*)(c.smem + t0.off[r]) = act4_packed(v[r], a, b);
  if (c.w < 7) *(u32x2*)(c.smem + t0.off[2]) = act4_packed(v[2], a, b);
  else {
    if (c.lane < 48) {
      const int ch = 32 + 2 * c.lane;
      const f32x2 g = *(const f32x2*)(c.smem + L_GB + gbsel * 1024 + ch * 4), be = *(const f32x2*)(c.smem + L_GB + gbsel * 1024 + 512 + ch * 4);
      const f32x2 st = *(const f32x2*)(c.smem + L_STAT + (ch >> 5) * 8);
      const f32x2 aa = g * (st[1] * LOG2E);
      *(f32x2*)(c.smem + L_PAR + ch * 4) = aa; *(f32x2*)(c.smem + L_PAR + 512 + ch * 4) = be * LOG2E - aa * st[0];
    }
  }
}
struct HTask { unsigned off[3]; };
DI void htask_init(const Ctx& c, HTask& ht) {
#pragma unroll
  for (int r = 0; r < 3; ++r) ht.off[r] = ntask(c, ((c.t - 256) >> 2) + 64 * r, 2 * (c.t & 3));
}
DI void norm_group_h(const Ctx& c, const HTask& ht, int k) {
  const int ch = 32 * k + 8 * (c.t & 3);
  u32x4 v[3];
#pragma unroll
  for (int r = 0; r < 3; ++r) v[r] = *(const u32x4*)(c.smem + (ht.off[r] ^ (k << 6)));
  const f32x4 a0 = *(const f32x4*)(c.smem + L_PAR + ch * 4), a1 = *(const f32x4*)(c.smem + L_PAR + ch * 4 + 16);
  const f32x4 b0 = *(const f32x4*)(c.smem + L_PAR + 512 + ch * 4), b1 = *(const f32x4*)(c.smem + L_PAR + 512 + ch * 4 + 16);
#pragma unroll
  for (int r = 0; r < 3; ++r) {
    const u32x2 lo = act4_scalar(u32x2{v[r][0], v[r][1]}, a0, b0), hi = act4_scalar(u32x2{v[r][2], v[r][3]}, a1, b1);
    *(u32x4*)(c.smem + (ht.off[r] ^ (k << 6))) = u32x4{lo[0], lo[1], hi[0], hi[1]};
  }
}

DI void xraw_issue(const Ctx& c) {
  int xs = c.x0 - 8 + 8 * (c.lane & 3);
  { const int lo = c.x0 & 64; xs = xs < lo ? lo : (xs > lo + 56 ? lo + 56 : xs); }
  const unsigned vo = (unsigned)(((c.b * 128 + 32 * c.w + (c.lane >> 2)) * 128) * 128 + xs) * 2;
#pragma unroll
  for (int row = 0; row < 10; ++row) {
    const int y = row == 0 ? (c.ty == 0 ? 0 : c.y0 - 1) : row == 9 ? (c.ty == 15 ? 127 : c.y0 + 8) : c.y0 - 1 + row;
#pragma unroll
    for (int cbl = 0; cbl < 2; ++cbl)
      __builtin_amdgcn_raw_ptr_buffer_load_lds(c.rx, (LAS void*)(c.smem + L_RAW + row * RS + (2 * c.w + cbl) * 1024), 16, vo, y * 256 + cbl * 16 * 32768, 0, 0);
  }
}
template <int RPAIR>
DI void xraw_step(const Ctx& c, unsigned ra, unsigned rh, const unsigned (&wa)[4], const unsigned (&wh)[2], bool rowok, bool hok) {
  u32x2 v[6];
  constexpr int RO = (RPAIR & 1) * 2 * RS;
  const unsigned ra2 = ra + (RPAIR >> 1) * 4 * RS, rh2 = rh + (RPAIR >> 1) * 4 * RS;
  asm volatile("s_waitcnt vmcnt(%8)\n\t"
               "ds_read_b64_tr_b16 %0, %6 offset:%9\n\tds_read_b64_tr_b16 %1, %6 offset:%10\n\tds_read_b64_tr_b16 %2, %6 offset:%11\n\tds_read_b64_tr_b16 %3, %6 offset:%12\n\t"
               "ds_read_b64_tr_b16 %4, %7 offset:%9\n\tds_read_b64_tr_b16 %5, %7 offset:%13\n\ts_waitcnt lgkmcnt(0)"
               : "=&v"(v[0]), "=&v"(v[1]), "=&v"(v[2]), "=&v"(v[3]), "=&v"(v[4]), "=&v"(v[5])
               : "v"(ra2), "v"(rh2), "i"(16 - 4 * RPAIR), "i"(RO), "i"(RO + 512), "i"(RO + 1024), "i"(RO + 1536), "i"(RO + 1024) : "memory");
  if (rowok) {
#pragma unroll
    for (int a = 0; a < 4; ++a) asm volatile("ds_write_b64 %0, %1 offset:%2" :: "v"(wa[a]), "v"(v[a]), "i"(RPAIR * 2 * RP) : "memory");
  }
  if (hok) {
#pragma unroll
    for (int hb = 0; hb < 2; ++hb) asm volatile("ds_write_b64 %0, %1 offset:%2" :: "v"(wh[hb]), "v"(v[4 + hb]), "i"(RPAIR * 2 * RP) : "memory");
  }
}
DI void xraw_transpose(const Ctx& c) {
  const unsigned lb = (unsigned)(size_t)(LAS char*)c.smem;
  const int gsel = c.lane >> 4, i = c.lane & 15, qp = i >> 2, p = i & 3;
  const unsigned ra = lb + L_RAW + (gsel & 1) * RS + (4 * (8 * c.w + (gsel >> 1)) + qp) * 64 + 16 + p * 8;
  unsigned wa[4];
#pragma unroll
  for (int a = 0; a < 4; ++a) wa[a] = lb + L_T + (gsel & 1) * RP + (i + 1) * 256 + (((4 * c.w + a) ^ swz((i + 1) & 15)) << 4) + (gsel >> 1) * 8;
  const unsigned rh = lb + L_RAW + (p >> 1) * RS + (4 * (8 * c.w + gsel) + qp) * 64 + ((p & 1) ? 48 : 8);
  const int side = qp & 1, hrow = qp >> 1, hcol = side ? 17 : 0;
  unsigned wh[2];
#pragma unroll
  for (int hb = 0; hb < 2; ++hb) wh[hb] = lb + L_T + hrow * RP + hcol * 256 + (((4 * c.w + 2 * hb + (gsel >> 1)) ^ swz(hcol & 15)) << 4) + (gsel & 1) * 8;
  const bool huse = (side ? p == 0 : p == 3) && (side ? (c.tx != 7 && c.tx != 3) : (c.tx != 0 && c.tx != 4));
  const bool top = c.ty == 0, bot = c.ty == 15;
  xraw_step<0>(c, ra, rh, wa, wh, !(top && (gsel & 1) == 0), huse && !(top && hrow == 0));
  xraw_step<1>(c, ra, rh, wa, wh, true, huse);
  xraw_step<2>(c, ra, rh, wa, wh, true, huse);
  xraw_step<3>(c, ra, rh, wa, wh, true, huse);
  xraw_step<4>(c, ra, rh, wa, wh, !(bot && (gsel & 1) == 1), huse && !(bot && hrow == 1));
}
struct Halo { unsigned go[4], lo[4]; bool inr[4], valid[4]; };
DI void halo_init(const Ctx& c, Halo& h) {
  const int tid = c.t - 256, cg = tid & 15;
#pragma unroll
  for (int r = 0; r < 4; ++r) {
    const int hp = (tid >> 4) + 16 * r;
    int row, col;
    if (hp < 18) { row = 0; col = hp; } else if (hp < 36) { row = 9; col = hp - 18; } else { const int j = hp - 36; row = 1 + (j >> 1); col = (j & 1) ? 17 : 0; }
    h.inr[r] = hp < 52;
    h.valid[r] = h.inr[r] && (unsigned)(c.y0 - 1 + row) < 128u && (unsigned)(c.x0 - 1 + col) < 128u;
    h.go[r] = WS_HB + (unsigned)(((c.b * 128 + c.y0 - 1 + row) * 128 + c.x0 - 1 + col) * 256 + cg * 16);
    h.lo[r] = L_T + row * RP + col * 256 + ((cg ^ swz(col & 15)) << 4);
  }
}
struct Clr { unsigned o[3]; };
DI void clear_init(const Ctx& c, Clr& cl) {
#pragma unroll
  for (int r = 0; r < 3; ++r) {
    int id = c.t - 256 + 256 * r; id = id < 704 ? id : id - 256;
    const int i = id >> 4;
    int ry, rx;
    if (i < 16) { ry = 0; rx = i; } else if (i < 32) { ry = 7; rx = i - 16; } else { const int j = i - 32; ry = 1 + (j >> 1); rx = (j & 1) ? 15 : 0; }
    cl.o[r] = WS_HB + (unsigned)(((c.b * 128 + c.y0 + ry) * 128 + c.x0 + rx) * 256 + (id & 15) * 16);
    asm volatile("" : "+v"(cl.o[r]));
  }
}
DI void clear_borders(const Ctx& c, const Clr& cl) {
  const u32x4 s = {~0u, ~0u, ~0u, ~0u};
#pragma unroll
  for (int r = 0; r < 3; ++r) bst_sc1(s, c.rw, cl.o[r], 0);
}
DI void stats_to_lds(const Ctx& c, int g, const u32x4& v0, const u32x4& v1) {
  const float s = wave_sum(__uint_as_float(v0[0]) + __uint_as_float(v1[0])), q = wave_sum(__uint_as_float(v0[2]) + __uint_as_float(v1[2]));
  const float mean = s * (1.f / 524288.f); float var = q * (1.f / 524288.f) - mean * mean; var = var < 0.f ? 0.f : var;
  if (c.lane == 0) *(f32x2*)(c.smem + L_STAT + g * 8) = f32x2{mean, __builtin_amdgcn_rsqf(var + 1e-5f)};
}

#define WLD bld_sc1
template <int CONV>
DI void mainloop(const Ctx& c, f32x4 (&acc)[8][2], bf16x8 (&wr)[WD], unsigned wvoff, const f32x4 (&ini)[2]) {
  const char* T = c.smem + L_T;
  const int px = c.lane & 15, q = c.lane >> 4;
  const int colP = px + (q & 1), colD = px + 2;
  const unsigned baseP = colP * 256 + (((q >> 1) ^ swz(colP & 15)) << 4), baseD = colD * 256 + ((q ^ swz(colD & 15)) << 4);
  auto rd = [&](int B, int r) -> bf16x8 {
    const int sp = B / 3, ty = B % 3;
    const unsigned a = ty == 2 ? (baseD ^ (sp << 6)) : (baseP ^ ((2 * sp + ty) << 5));
    return *(const bf16x8*)(T + a + r * RP);
  };
  bf16x8 xf[4];
#pragma unroll
  for (int r = 0; r < 3; ++r) xf[r] = rd(0, r);
#pragma unroll
  for (int B = 0; B < 12; ++B) {
#pragma unroll
    for (int r = 0; r < 10; ++r) {
      const int G = B * 10 + r;
      if (r == 7 && (B % 3) == 2 && B < 11) { bar_nowait(); SB(); }
      { const int Gn = G + 3; if (Gn < 120) xf[Gn & 3] = rd(Gn / 10, Gn % 10); }
#pragma unroll
      for (int dy = 0; dy < 3; ++dy) {
        const int row = r - dy;
        if (row >= 0 && row < 8) {
#pragma unroll
          for (int nt = 0; nt < 2; ++nt) {
            const int ts = 6 * B + 2 * dy + nt;
            if (CONV == 1) asm volatile("v_mfma_f32_16x16x32_bf16 %0, %1, %2, %0" : "+v"(acc[row][nt]) : "v"(xf[G & 3]), "v"(wr[ts % WD]));
            else if (B == 0 && dy == 0) asm volatile("v_mfma_f32_16x16x32_bf16 %0, %1, %2, %3" : "=&v"(acc[row][nt]) : "v"(wr[ts % WD]), "v"(xf[G & 3]), "v"(ini[nt]));
            else           asm volatile("v_mfma_f32_16x16x32_bf16 %0, %1, %2, %0" : "+v"(acc[row][nt]) : "v"(wr[ts % WD]), "v"(xf[G & 3]));
            if (row == 7) { const int tn = CONV * 72 + ts + WD; if (tn < 144) wr[ts % WD] = __builtin_bit_cast(bf16x8, WLD(c.rw, wvoff, (unsigned)tn * 1024 + (tn >= 72 ? 294912u - 72u * 1024u : 0u))); }
            SB();
          }
        }
      }
    }
  }
  asm volatile("s_nop 15" ::: "memory");
  SB();
}

DI void pin_acc(f32x4 (&acc)[8][2]) {
#pragma unroll
  for (int r = 0; r < 8; ++r) asm volatile("" : "+v"(acc[r][0]), "+v"(acc[r][1]));
}
__global__ __launch_bounds__(512, 2) void k_resblock(const u16* __restrict__ x, const u16* __restrict__ t_emb, const u16* __restrict__ g1, const u16* __restrict__ be1,
                                                     const u16* __restrict__ w1, const u16* __restrict__ w_emb, const u16* __restrict__ g2, const u16* __restrict__ be2,
                                                     const u16* __restrict__ w2, u16* __restrict__ out, char* __restrict__ ws) {
  extern __shared__ __attribute__((aligned(256))) char smem[];
  Ctx c;
  c.smem = smem; c.ws = ws;
  c.t = threadIdx.x; c.lane = c.t & 63; c.w = __builtin_amdgcn_readfirstlane(c.t >> 6);
  const int xcd = blockIdx.x & 7, jx = blockIdx.x >> 3;
  c.b = xcd >> 2; c.ty = (xcd & 3) * 4 + (jx >> 3); c.tx = jx & 7;
  c.jb = c.ty * 8 + c.tx; c.y0 = c.ty * 8; c.x0 = c.tx * 16;
  c.rx = mk_rsrc(x, 8u << 20); c.rw = mk_rsrc(ws, 16u << 20);
  c.wb = (unsigned)c.b * WB_SIZE;
  const unsigned epoch = __hip_atomic_load((GAS unsigned*)(ws + c.wb + WB_EPOCH), __ATOMIC_RELAXED, __HIP_MEMORY_SCOPE_AGENT);

  unsigned long long dg[12];
#define DG(i) do { dg[i] = __builtin_amdgcn_s_memrealtime(); asm volatile("" : "+s"(dg[i])); } while (0)
  if (c.w < 4) {
    DG(0);
    STAMP_DECL STAMP(0);
    const int ch = c.t & 127;
    const unsigned gbA = (c.t < 128 ? g1 : be1)[ch], gbB = (c.t < 128 ? g2 : be2)[ch];
    SB();
    bar_nowait(); SB();
    STAMP(14);
    xraw_issue(c); SB();
    STAMP(15);
    Task0 t0; task0_init(c, t0);
    xraw_transpose(c); SB();
    c.tag = __builtin_amdgcn_readfirstlane(epoch) + 1u;
    { float* gbp = (float*)(smem + L_GB) + (c.t < 128 ? 0 : 128) + ch; gbp[0] = bflo(gbA); gbp[256] = bflo(gbB); }
    STAMP(3); DG(6);
    {
      const unsigned fo = c.wb + WB_FLAG + (unsigned)((((c.lane >> 3) & 1) * 4 + c.w) * 128 + (c.lane & 7) * 4);
      for (int spin = 0; spin < SPIN_MAX; ++spin) {
        cbar();
        const unsigned f = __builtin_amdgcn_raw_buffer_load_b32(c.rw, fo, 0, SC1);
        if (!__any(f != c.tag)) break;
        __builtin_amdgcn_s_sleep(2);
      }
    }
    STAMP(4); DG(7);
    const unsigned wvoff = c.wb + WB_WF + (unsigned)c.w * 73728u + (unsigned)c.lane * 16u;
    bf16x8 wr[WD];
#pragma unroll
    for (int k = 0; k < WD; ++k) wr[k] = __builtin_bit_cast(bf16x8, bld_sc1(c.rw, wvoff, (unsigned)k * 1024));
    const int px = c.lane & 15, q = c.lane >> 4;
    bar();
    DG(8);
    STAMP(5);
    f32x4 acc[8][2], ini[2];
    ini[0] = *(const f32x4*)(smem + L_EMB + (32 * c.w + 4 * q) * 4); ini[1] = *(const f32x4*)(smem + L_EMB + (32 * c.w + 16 + 4 * q) * 4);
    norm_group0(c, t0, 0);
    bar();
    STAMP(6); DG(1);
    __builtin_amdgcn_s_setprio(1);
    mainloop<0>(c, acc, wr, wvoff, ini);
    __builtin_amdgcn_s_setprio(0);
    STAMP(7); DG(2);
    {
      f32x2 s2 = {0.f, 0.f}, q2 = {0.f, 0.f};
#pragma unroll
      for (int r = 0; r < 8; ++r)
#pragma unroll
        for (int nt = 0; nt < 2; ++nt)
#pragma unroll
          for (int k = 0; k < 2; ++k) { const f32x2 v = {acc[r][nt][2 * k], acc[r][nt][2 * k + 1]}; s2 += v; q2 += v * v; }
      const float s = wave_sum(s2[0] + s2[1]), qs = wave_sum(q2[0] + q2[1]);
      if (c.lane == 0) bst_sc1(u32x4{__float_as_uint(s), c.tag, __float_as_uint(qs), c.tag}, c.rw, c.wb + WB_G2 + (unsigned)(c.w * 128 + c.jb) * 16, 0);
    }
    STAMP(1);
    bar_nowait();
    {
      const int chk = 4 * c.w + (((q & 1) << 1) | (q >> 1));
      const unsigned go = WS_HB + (unsigned)(((c.b * 128 + c.y0) * 128 + c.x0 + px) * 256 + chk * 16);
      char* lp = smem + L_T + RP + (px + 1) * 256 + ((chk ^ swz((px + 1) & 15)) << 4);
      const bool edge = px == 0 || px == 15;
#pragma unroll
      for (int rr = 0; rr < 8; ++rr) {
        const int r = rr == 0 ? 0 : (rr == 1 ? 7 : rr - 1);
        const unsigned d0 = cvtpk(acc[r][0][0], acc[r][0][1]), d1 = cvtpk(acc[r][0][2], acc[r][0][3]);
        const unsigned d2 = cvtpk(acc[r][1][0], acc[r][1][1]), d3 = cvtpk(acc[r][1][2], acc[r][1][3]);
        auto a = __builtin_amdgcn_permlane16_swap(d0, d2, false, false);
        auto e = __builtin_amdgcn_permlane16_swap(d1, d3, false, false);
        const u32x4 v = {a[0], e[0], a[1], e[1]};
        if (r == 0 || r == 7 || edge) bst_sc1(u32x4{min(v[0], 0xfffffffeu), min(v[1], 0xfffffffeu), min(v[2], 0xfffffffeu), min(v[3], 0xfffffffeu)}, c.rw, go, r * 32768);
        *(u32x4*)(lp + r * RP) = v;
      }
    }
    bar();
    STAMP(8);
#pragma unroll
    for (int r = 0; r < 8; ++r) {
      const u32x4 v = *(const u32x4*)(smem + L_RAW + (r + 1) * RS + (32 * c.w + 16 * (q & 1) + px) * 64 + 16 + (q >> 1) * 16);
      auto a = __builtin_amdgcn_permlane16_swap(v[0], v[2], false, false);
      auto e = __builtin_amdgcn_permlane16_swap(v[1], v[3], false, false);
      acc[r][0] = f32x4{bflo(a[0]), bfhi(a[0]), bflo(e[0]), bfhi(e[0])};
      acc[r][1] = f32x4{bflo(a[1]), bfhi(a[1]), bflo(e[1]), bfhi(e[1])};
    }
    pin_acc(acc);
    STAMP(9);
    bar();
    STAMP(10);
    norm_group0(c, t0, 1);
    bar();
    STAMP(11); DG(3);
    __builtin_amdgcn_s_setprio(1);
    mainloop<1>(c, acc, wr, wvoff, ini);
    __builtin_amdgcn_s_setprio(0);
    STAMP(12); DG(4);
    u16* op = out + ((size_t)(c.b * 128 + 32 * c.w + 16 * (q & 1) + px) * 128 + c.y0) * 128 + c.x0 + 8 * (q >> 1);
#pragma unroll
    for (int r = 0; r < 8; ++r) {
      const unsigned d0 = cvtpk(acc[r][0][0], acc[r][0][1]), d1 = cvtpk(acc[r][0][2], acc[r][0][3]);
      const unsigned d2 = cvtpk(acc[r][1][0], acc[r][1][1]), d3 = cvtpk(acc[r][1][2], acc[r][1][3]);
      auto a = __builtin_amdgcn_permlane16_swap(d0, d2, false, false);
      auto e = __builtin_amdgcn_permlane16_swap(d1, d3, false, false);
      *(u32x4*)(op + r * 128) = u32x4{a[0], e[0], a[1], e[1]};
    }
    STAMP(13); STAMP_FLUSH;
    DG(5);
    if (blockIdx.x == DIAG_WG && c.w == DIAG_WAVE) {
      const long long val = (long long)(dg[DIAG_B] - dg[DIAG_A]) * DIAG_SCALE;
      const unsigned long long t0_ = __builtin_amdgcn_s_memrealtime();
      while ((long long)(__builtin_amdgcn_s_memrealtime() - t0_) < val) __builtin_amdgcn_s_sleep(1);
    }
  } else {
    DG(0);
    STAMP_DECL STAMP(0);
    const int g = c.w - 4;
    const int yrow = (xcd & 3) * 32 + jx;
    __builtin_amdgcn_s_setprio(2);
    const int ksvc = jx * 4 + (xcd & 3);
    const bool svc_w = c.w == 7 && (ksvc < 32 || (ksvc >= 64 && ksvc < 96)), svc_e = c.w == 7 && ksvc >= 32 && ksvc < 64;
    const int wu = ksvc < 32 ? ksvc : ksvc - 32;
    u32x4 sv[9]; unsigned te[6];
    if (svc_w) {
      const u32x4* src = (const u32x4*)(((wu >> 5) ? w2 : w1) + (size_t)((32 * ((wu >> 3) & 3) + (c.lane & 31)) * 128 + 16 * (wu & 7) + 8 * (c.lane >> 5)) * 9);
#pragma unroll
      for (int k = 0; k < 9; ++k) sv[k] = src[k];
    }
    if (svc_e) {
      const int j = ksvc - 32, e0 = 192 * (j >> 3) + (c.lane >> 1);
#pragma unroll
      for (int k = 0; k < 6; ++k) { sv[k] = *(const u32x4*)(w_emb + (size_t)(e0 + 32 * k) * 128 + 16 * (j & 7) + 8 * (c.lane & 1)); te[k] = t_emb[c.b * 768 + e0 + 32 * k]; }
    }
    SB();
    u32x4 xr[8];
    { const unsigned o = (unsigned)(((c.b * 128 + 32 * g + 8 * (c.lane >> 4)) * 128 + yrow) * 128) * 2 + (c.lane & 15) * 16;
#pragma unroll
      for (int jj = 0; jj < 8; ++jj) xr[jj] = bld(c.rx, o, jj * 32768); }
    SB();
    bar_nowait(); SB();
    STAMP(4);
    c.tag = __builtin_amdgcn_readfirstlane(epoch) + 1u;
    {
      float s = 0.f, qs = 0.f;
#pragma unroll
      for (int jj = 0; jj < 8; ++jj)
#pragma unroll
        for (int k = 0; k < 4; ++k) { s = dot2(xr[jj][k], 0x3f803f80u, s); qs = dot2(xr[jj][k], xr[jj][k], qs); }
      s = wave_sum(s); qs = wave_sum(qs);
      if (c.lane == 0) bst_sc1(u32x4{__float_as_uint(s), c.tag, __float_as_uint(qs), c.tag}, c.rw, c.wb + WB_G1 + (unsigned)(g * 128 + c.jb) * 16, 0);
    }
    {
      const int k16 = c.lane & 15;
      if (k16 == 7 || k16 == 8) {
        unsigned v[4];
#pragma unroll
        for (int m = 0; m < 4; ++m) {
          const unsigned a = __builtin_amdgcn_perm(xr[2 * m + 1][3], xr[2 * m][3], 0x07060302u), b2 = __builtin_amdgcn_perm(xr[2 * m + 1][0], xr[2 * m][0], 0x05040100u);
          v[m] = k16 == 7 ? a : b2;
        }
        const unsigned o = c.wb + WB_XC + (unsigned)((yrow * 2 + (k16 - 7)) * 64 + 16 * g + 4 * (c.lane >> 4)) * 8;
        bst_sc1(u32x4{v[0], c.tag, v[1], c.tag}, c.rw, o, 0); bst_sc1(u32x4{v[2], c.tag, v[3], c.tag}, c.rw, o, 16);
      }
    }
    STAMP(1); DG(1);
    if (svc_w) {
      const int cs = wu & 7, o = c.lane & 31, h = c.lane >> 5;
      const unsigned dst = c.wb + WB_WF + (unsigned)(wu >> 5) * 294912u + (unsigned)((wu >> 3) & 3) * 73728u + (unsigned)(o >> 4) * 1024u + (unsigned)(o & 15) * 16u;
      const unsigned dP = dst + (unsigned)(3 * (cs >> 1) + (cs & 1)) * 6144u + (unsigned)h * 512u;
      const unsigned dD = dst + (unsigned)(3 * (cs >> 1) + 2) * 6144u + (unsigned)(2 * (cs & 1) + h) * 256u;
#pragma unroll
      for (int k = 0; k < 9; ++k) {
        const int dy = k / 3, dx = k % 3;
        u32x4 o4;
#pragma unroll
        for (int m = 0; m < 4; ++m) {
          const int e0 = 18 * m + k, e1 = e0 + 9;
          const unsigned sel = ((e0 & 1) ? 0x0302u : 0x0100u) | ((e1 & 1) ? 0x07060000u : 0x05040000u);
          o4[m] = __builtin_amdgcn_perm(sv[e1 >> 3][(e1 >> 1) & 3], sv[e0 >> 3][(e0 >> 1) & 3], sel);
        }
        bst_sc1(o4, c.rw, dx < 2 ? dP : dD, dy * 2048 + (dx < 2 ? dx * 256 : 0));
      }
      asm volatile("s_waitcnt vmcnt(0)" ::: "memory");
      if (c.lane == 0) __hip_atomic_store((GAS unsigned*)(ws + c.wb + WB_FLAG + (wu >> 3) * 128 + (wu & 7) * 4), c.tag, __ATOMIC_RELAXED, __HIP_MEMORY_SCOPE_AGENT);
    }
    if (svc_e) {
      f32x2 a[4];
#pragma unroll
      for (int m = 0; m < 4; ++m) a[m] = f32x2{0.f, 0.f};
#pragma unroll
      for (int k = 0; k < 6; ++k) {
        const float tv = bflo(te[k]);
        const float sl = tv * __builtin_amdgcn_rcpf(1.f + __builtin_amdgcn_exp2f(-tv * LOG2E));
#pragma unroll
        for (int m = 0; m < 4; ++m) a[m] += f32x2{bflo(sv[k][m]), bfhi(sv[k][m])} * sl;
      }
      float r[8];
#pragma unroll
      for (int m = 0; m < 8; ++m) {
        float v = a[m >> 1][m & 1];
        v = dppadd<0x4E>(v); v = dppadd<0x124>(v); v = dppadd<0x128>(v);
        auto p = __builtin_amdgcn_permlane16_swap(__float_as_uint(v), __float_as_uint(v), false, false);
        v = __uint_as_float(p[0]) + __uint_as_float(p[1]);
        auto p2 = __builtin_amdgcn_permlane32_swap(__float_as_uint(v), __float_as_uint(v), false, false);
        r[m] = __uint_as_float(p2[0]) + __uint_as_float(p2[1]);
      }
      const int j = ksvc - 32;
      if (c.lane < 2) {
        const unsigned o = c.wb + WB_EG + (unsigned)((j >> 3) * 128 + 16 * (j & 7) + 8 * c.lane) * 8;
#pragma unroll
        for (int m = 0; m < 4; ++m) bst_sc1(u32x4{__float_as_uint(r[2 * m]), c.tag, __float_as_uint(r[2 * m + 1]), c.tag}, c.rw, o, m * 16);
      }
    }
    __builtin_amdgcn_s_setprio(0);
    STAMP(2); DG(2);
    Halo hl; halo_init(c, hl);
#pragma unroll
    for (int r = 0; r < 4; ++r) if (hl.inr[r] && !hl.valid[r]) *(u32x4*)(smem + hl.lo[r]) = u32x4{0, 0, 0, 0};
    Task0 t0; task0_init(c, t0);
    HTask ht; htask_init(c, ht);
    Clr cl; clear_init(c, cl);
    STAMP(3);
    {
      u32x4 v0, v1, ve[4], vc[2]; bool n0 = true, n1 = true, ne = c.w == 4, nc[2];
      const unsigned o = c.wb + WB_G1 + (unsigned)(g * 128 + c.lane) * 16;
      const bool xc = c.tx == 3 || c.tx == 4;
      unsigned co[2], cl[2]; bool cin[2];
#pragma unroll
      for (int r = 0; r < 2; ++r) {
        const int id = c.t - 256 + 256 * r, row = id >> 5, i = id & 31, y = c.y0 - 1 + row, col = c.tx == 3 ? 17 : 0;
        cin[r] = xc && id < 320;
        nc[r] = cin[r] && (unsigned)y < 128u;
        co[r] = c.wb + WB_XC + (unsigned)((y * 2 + (c.tx == 3 ? 1 : 0)) * 64 + 2 * i) * 8;
        cl[r] = L_T + row * RP + col * 256 + (((i >> 1) ^ swz(col & 15)) << 4) + (i & 1) * 8;
        vc[r] = u32x4{0, 0, 0, 0};
      }
      for (int spin = 0; spin < SPIN_MAX; ++spin) {
        cbar();
        if (n0) v0 = bld_sc1(c.rw, o, 0);
        if (n1) v1 = bld_sc1(c.rw, o, 1024);
        if (ne) {
#pragma unroll
          for (int k = 0; k < 4; ++k) ve[k] = bld_sc1(c.rw, c.wb + WB_EG + (unsigned)c.lane * 16, k * 1024);
        }
#pragma unroll
        for (int r = 0; r < 2; ++r) if (nc[r]) vc[r] = bld_sc1(c.rw, co[r], 0);
        n0 = !(v0[1] == c.tag && v0[3] == c.tag); n1 = !(v1[1] == c.tag && v1[3] == c.tag);
        if (ne) { bool ok = true;
#pragma unroll
          for (int k = 0; k < 4; ++k) ok = ok && ve[k][1] == c.tag && ve[k][3] == c.tag;
          ne = !ok; }
#pragma unroll
        for (int r = 0; r < 2; ++r) nc[r] = nc[r] && !(vc[r][1] == c.tag && vc[r][3] == c.tag);
        if (!__any(n0 || n1 || ne || nc[0] || nc[1])) break;
        __builtin_amdgcn_s_sleep(2);
      }
      stats_to_lds(c, g, v0, v1);
      if (c.w == 4) *(f32x2*)(smem + L_EMB + c.lane * 8) = f32x2{(__uint_as_float(ve[0][0]) + __uint_as_float(ve[1][0])) + (__uint_as_float(ve[2][0]) + __uint_as_float(ve[3][0])),
                                                               (__uint_as_float(ve[0][2]) + __uint_as_float(ve[1][2])) + (__uint_as_float(ve[2][2]) + __uint_as_float(ve[3][2]))};
#pragma unroll
      for (int r = 0; r < 2; ++r) if (cin[r]) *(u32x2*)(smem + cl[r]) = u32x2{vc[r][0], vc[r][2]};
    }
    STAMP(6); DG(3);
    bar();
    if (c.t == 256 && c.jb == 0) __hip_atomic_store((GAS unsigned*)(ws + c.wb + WB_EPOCH), c.tag, __ATOMIC_RELAXED, __HIP_MEMORY_SCOPE_AGENT);
    norm_group0(c, t0, 0);
    bar();
#pragma unroll 1
    for (int k = 1; k < 4; ++k) {
      if (k == 2) clear_borders(c, cl);
      norm_group_h(c, ht, k);
      if (k == 2) asm volatile("s_waitcnt vmcnt(0)" ::: "memory");
      bar();
      if (k == 2 && c.t == 256) __hip_atomic_store((GAS unsigned*)(ws + c.wb + WB_CF + c.jb * 4), c.tag, __ATOMIC_RELAXED, __HIP_MEMORY_SCOPE_AGENT);
    }
    STAMP(7);
    {
      if (c.w == 5) {
        const int dy = (c.lane & 15) / 3 - 1, dx = (c.lane & 15) % 3 - 1;
        const int ny = c.ty + dy, nx = c.tx + dx;
        const bool chk = c.lane < 9 && (unsigned)ny < 16u && (unsigned)nx < 8u;
        for (int spin = 0; spin < SPIN_MAX; ++spin) {
          cbar();
          unsigned f = c.tag;
          if (chk) f = __builtin_amdgcn_raw_buffer_load_b32(c.rw, c.wb + WB_CF + (unsigned)(ny * 8 + nx) * 4, 0, SC1);
          if (!__any(f != c.tag)) break;
          __builtin_amdgcn_s_sleep(2);
        }
      }
      u32x4 hv[4]; bool need[4];
#pragma unroll
      for (int r = 0; r < 4; ++r) { need[r] = hl.valid[r]; hv[r] = u32x4{0, 0, 0, 0}; }
      u32x4 v0, v1; bool n0 = true, n1 = true;
      const unsigned o = c.wb + WB_G2 + (unsigned)(g * 128 + c.lane) * 16;
      bar_nowait();
      bar();
      STAMP(13);
      for (int spin = 0; spin < SPIN_MAX; ++spin) {
        cbar();
#pragma unroll
        for (int r = 0; r < 4; ++r) if (need[r]) hv[r] = bld_sc1(c.rw, hl.go[r], 0);
        if (n0) v0 = bld_sc1(c.rw, o, 0);
        if (n1) v1 = bld_sc1(c.rw, o, 1024);
        bool any = false;
#pragma unroll
        for (int r = 0; r < 4; ++r) { need[r] = need[r] && (hv[r][0] == ~0u || hv[r][1] == ~0u || hv[r][2] == ~0u || hv[r][3] == ~0u); any |= need[r]; }
        n0 = !(v0[1] == c.tag && v0[3] == c.tag); n1 = !(v1[1] == c.tag && v1[3] == c.tag);
        if (!__any(any || n0 || n1)) break;
        __builtin_amdgcn_s_sleep(1);
      }
      STAMP(8);
#pragma unroll
      for (int r = 0; r < 4; ++r) if (hl.valid[r]) *(u32x4*)(smem + hl.lo[r]) = hv[r];
      stats_to_lds(c, g, v0, v1);
    }
    bar();
    norm_group0(c, t0, 1);
    bar();
#pragma unroll 1
    for (int k = 1; k < 4; ++k) { if (k == 1) STAMP(9); norm_group_h(c, ht, k); if (k == 1) STAMP(10); if (k == 2) STAMP(11); bar(); }
    STAMP(12); STAMP_FLUSH;
    if (blockIdx.x == DIAG_WG && c.w == DIAG_WAVE) {
      const long long val = (long long)(dg[DIAG_B] - dg[DIAG_A]) * DIAG_SCALE;
      const unsigned long long t0_ = __builtin_amdgcn_s_memrealtime();
      while ((long long)(__builtin_amdgcn_s_memrealtime() - t0_) < val) __builtin_amdgcn_s_sleep(1);
    }
  }
}

extern "C" void kernel_launch(void* const* d_in, const int* in_sizes, int n_in, void* d_out, int out_size, void* d_ws, size_t ws_size, hipStream_t stream) {
  (void)hipFuncSetAttribute((const void*)k_resblock, hipFuncAttributeMaxDynamicSharedMemorySize, L_END);
  k_resblock<<<256, 512, L_END, stream>>>((const u16*)d_in[0], (const u16*)d_in[1], (const u16*)d_in[2], (const u16*)d_in[3], (const u16*)d_in[4],
                                          (const u16*)d_in[5], (const u16*)d_in[6], (const u16*)d_in[7], (const u16*)d_in[8], (u16*)d_out, (char*)d_ws);
}
```

```cpp
#define DIAG 1
#define DIAG_WG 100
#define DIAG_WAVE 0
#define DIAG_A 0
#define DIAG_B 13
#define DIAG_SCALE 0
#include <hip/hip_runtime.h>
#include <hip/hip_bf16.h>
#include <stdint.h>

typedef float f32x4 __attribute__((ext_vector_type(4)));
typedef float f32x2 __attribute__((ext_vector_type(2)));
typedef __bf16 bf16x8 __attribute__((ext_vector_type(8)));
typedef __bf16 bf16x2 __attribute__((ext_vector_type(2)));
typedef unsigned u32x4 __attribute__((ext_vector_type(4)));
typedef unsigned u32x2 __attribute__((ext_vector_type(2)));
typedef unsigned short u16;
#define GAS __attribute__((address_space(1)))
#define LAS __attribute__((address_space(3)))
#define DI __device__ __forceinline__
#define SB() __builtin_amdgcn_sched_barrier(0)
#define PIN(x) asm volatile("" : "+v"(x))

constexpr int RP = 4608;
constexpr int TILE_BYTES = 10 * RP;
constexpr int RS = 8224;
constexpr int WD = 18;
constexpr float LOG2E = 1.4426950408889634f;
constexpr int SC1 = 16, NT = 2;
constexpr int SPIN_MAX = 1 << 16;
#define POLL_SLEEP 4
#define POLL_SLEEP2 2
#define AUX_SVC 0
#define AUX_ROW 2

constexpr unsigned WB_SIZE = 1u << 20;
constexpr unsigned WB_WF = 0, WB_EPOCH = 589824, WB_FLAG = WB_EPOCH + 256  , WB_G1 = WB_FLAG + 1024, WB_G2 = WB_G1 + 8192,
                   WB_EG = WB_G2 + 8192  , WB_CF = WB_EG + 4096  , WB_XC = WB_CF + 512  ;
constexpr unsigned WS_HB = 2u << 20;
constexpr int L_T = 0, L_RAW = TILE_BYTES, L_GB = L_RAW + 10 * RS  , L_PAR = L_GB + 2048  , L_EMB = L_PAR + 1024,
              L_STAT = L_EMB + 512  , L_RED = L_STAT + 32  , L_DUMMY = (L_RED + 32 + 255) / 256 * 256  , L_SVC = L_DUMMY + 1024  ,
              L_END = L_SVC + 9504;

DI int swz(int c) { return c ^ ((c >> 1) & 4); }
DI unsigned cvtpk(float lo, float hi) { f32x2 v = {lo, hi}; bf16x2 b = __builtin_convertvector(v, bf16x2); return __builtin_bit_cast(unsigned, b); }
DI unsigned cvtpk_s(float lo, float hi) { unsigned r; asm("v_cvt_pk_bf16_f32 %0, %1, %2" : "=v"(r) : "v"(lo), "v"(hi)); return r; }
DI float bflo(unsigned d) { return __uint_as_float(d << 16); }
DI float bfhi(unsigned d) { return __uint_as_float(d & 0xffff0000u); }
DI float dot2(unsigned a, unsigned b, float c) { return __builtin_amdgcn_fdot2_f32_bf16(__builtin_bit_cast(bf16x2, a), __builtin_bit_cast(bf16x2, b), c, false); }
template <int CTRL> DI float dppadd(float v) { return v + __int_as_float(__builtin_amdgcn_update_dpp(0, __float_as_int(v), CTRL, 0xf, 0xf, true)); }
DI float rows_sum(float v) {
  auto p = __builtin_amdgcn_permlane16_swap(__float_as_uint(v), __float_as_uint(v), false, false);
  v = __uint_as_float(p[0]) + __uint_as_float(p[1]);
  auto q = __builtin_amdgcn_permlane32_swap(__float_as_uint(v), __float_as_uint(v), false, false);
  return __uint_as_float(q[0]) + __uint_as_float(q[1]);
}
DI float wave_sum(float v) { v = dppadd<0xB1>(v); v = dppadd<0x4E>(v); v = dppadd<0x141>(v); v = dppadd<0x140>(v); return rows_sum(v); }
DI void bar() { asm volatile("s_waitcnt lgkmcnt(0)\n\ts_barrier" ::: "memory"); }
DI void bar_nowait() { asm volatile("s_barrier" ::: "memory"); }
DI void cbar() { asm volatile("" ::: "memory"); }
typedef __amdgpu_buffer_rsrc_t rsrc_t;
DI rsrc_t mk_rsrc(const void* p, unsigned bytes) { return __builtin_amdgcn_make_buffer_rsrc((void*)p, 0, bytes, 0x00020000); }
template <int AUX> DI u32x4 bld(rsrc_t r, unsigned voff, unsigned soff) { return __builtin_amdgcn_raw_buffer_load_b128(r, voff, soff, AUX); }
DI void bst_sc1(u32x4 v, rsrc_t r, unsigned voff, unsigned off) { __builtin_amdgcn_raw_buffer_store_b128(v, r, voff + off, 0, SC1); }

struct Ctx {
  rsrc_t rx, rw;
  char* smem; char* ws;
  int b, ty, tx, jb, y0, x0, w, lane, t;
  unsigned wb;
  unsigned tag;
};

#define STAMP_DECL unsigned st_t[16]; _Pragma("unroll") for (int i_ = 0; i_ < 16; ++i_) st_t[i_] = 0;
#define STAMP(idx) do { if (idx == DIAG_A || idx == DIAG_B) { st_t[idx] = (unsigned)__builtin_amdgcn_s_memrealtime(); asm volatile("" : "+s"(st_t[idx])); } } while (0)
#define STAMP_FLUSH do { if (blockIdx.x == DIAG_WG && c.w == DIAG_WAVE) { const int val_ = (int)(st_t[DIAG_B] - st_t[DIAG_A]) * DIAG_SCALE; \
  const unsigned t0_ = (unsigned)__builtin_amdgcn_s_memrealtime(); while ((int)((unsigned)__builtin_amdgcn_s_memrealtime() - t0_) < val_) __builtin_amdgcn_s_sleep(1); } } while (0)

DI u32x2 act4_packed(u32x2 v, f32x4 a, f32x4 b) {
  u32x2 o;
#pragma unroll
  for (int m = 0; m < 2; ++m) {
    f32x2 xx = {bflo(v[m]), bfhi(v[m])}, aa = {a[2 * m], a[2 * m + 1]}, bb = {b[2 * m], b[2 * m + 1]};
    f32x2 u = xx * aa + bb;
    f32x2 e = {__builtin_amdgcn_exp2f(-u[0]), __builtin_amdgcn_exp2f(-u[1])};
    f32x2 d = e * LOG2E + LOG2E;
    f32x2 r = {__builtin_amdgcn_rcpf(d[0]), __builtin_amdgcn_rcpf(d[1])};
    f32x2 y = u * r;
    o[m] = cvtpk(y[0], y[1]);
  }
  return o;
}
DI float act1(float x, float a, float b) { float u = __builtin_fmaf(x, a, b); float e = __builtin_amdgcn_exp2f(-u); return u * __builtin_amdgcn_rcpf(__builtin_fmaf(e, LOG2E, LOG2E)); }
DI u32x2 act4_scalar(u32x2 v, f32x4 a, f32x4 b) {
  u32x2 o;
  o[0] = cvtpk_s(act1(bflo(v[0]), a[0], b[0]), act1(bfhi(v[0]), a[1], b[1]));
  o[1] = cvtpk_s(act1(bflo(v[1]), a[2], b[2]), act1(bfhi(v[1]), a[3], b[3]));
  return o;
}
DI bool in_image(const Ctx& c, int row, int col) { return ((unsigned)(c.y0 - 1 + row) < 128u) & ((unsigned)(c.x0 - 1 + col) < 128u); }
struct Tasks { unsigned off[3]; };
DI void tasks_init(const Ctx& c, Tasks& tk, int idx, int c4) {
  const unsigned lo = (c4 & 1) * 8, dummy = L_DUMMY + c.lane * 16 + lo;
  const int col = idx & 15, row = idx >> 4;
  const unsigned o0 = L_T + row * RP + col * 256 + (((c4 >> 1) ^ swz(col)) << 4) + lo;
  const int j = idx - 32, row2 = idx < 32 ? 8 + row : j >> 1, col2 = idx < 32 ? col : 16 + (j & 1);
  const unsigned o2 = L_T + row2 * RP + col2 * 256 + (((c4 >> 1) ^ swz(col2 & 15)) << 4) + lo;
  tk.off[0] = o0; tk.off[1] = o0 + 4 * RP; tk.off[2] = idx < 52 ? o2 : dummy;
  if ((c.ty == 0) | (c.ty == 15) | (c.tx == 0) | (c.tx == 7)) {
    tk.off[0] = in_image(c, row, col) ? tk.off[0] : dummy;
    tk.off[1] = in_image(c, row + 4, col) ? tk.off[1] : dummy;
    tk.off[2] = in_image(c, row2, col2) ? tk.off[2] : dummy;
  }
#pragma unroll
  for (int r = 0; r < 3; ++r) PIN(tk.off[r]);
}
typedef Tasks Task0;
DI void task0_init(const Ctx& c, Task0& t0) { tasks_init(c, t0, c.t >> 3, c.t & 7); }
DI void norm_group0(const Ctx& c, const Task0& t0, int gbsel) {
  u32x2 v[3];
#pragma unroll
  for (int r = 0; r < 3; ++r) v[r] = *(const u32x2*)(c.smem + t0.off[r]);
  const int ch = 4 * (c.t & 7);
  const f32x4 g = *(const f32x4*)(c.smem + L_GB + gbsel * 1024 + ch * 4), be = *(const f32x4*)(c.smem + L_GB + gbsel * 1024 + 512 + ch * 4);
  const f32x2 st = *(const f32x2*)(c.smem + L_STAT);
  const f32x4 a = g * st[1], b = be - a * st[0];
#pragma unroll
  for (int r = 0; r < 2; ++r) *(u32x2*)(c.smem + t0.off[r]) = act4_packed(v[r], a, b);
  if (c.w < 7) *(u32x2*)(c.smem + t0.off[2]) = act4_packed(v[2], a, b);
  else if (c.lane < 48) {
    const int c2 = 32 + 2 * c.lane;
    const f32x2 g2 = *(const f32x2*)(c.smem + L_GB + gbsel * 1024 + c2 * 4), b2 = *(const f32x2*)(c.smem + L_GB + gbsel * 1024 + 512 + c2 * 4);
    const f32x2 s2 = *(const f32x2*)(c.smem + L_STAT + (c2 >> 5) * 8);
    const f32x2 aa = g2 * s2[1];
    *(f32x2*)(c.smem + L_PAR + c2 * 4) = aa; *(f32x2*)(c.smem + L_PAR + 512 + c2 * 4) = b2 - aa * s2[0];
  }
}
typedef Tasks HTask;
DI void htask_init(const Ctx& c, HTask& ht) { tasks_init(c, ht, (c.t - 256) >> 2, 2 * (c.t & 3)); }
DI void norm_group_h(const Ctx& c, const HTask& ht, int k) {
  const int ch = 32 * k + 8 * (c.t & 3);
  u32x4 v[3];
#pragma unroll
  for (int r = 0; r < 3; ++r) v[r] = *(const u32x4*)(c.smem + (ht.off[r] ^ (k << 6)));
  const f32x4 a0 = *(const f32x4*)(c.smem + L_PAR + ch * 4), a1 = *(const f32x4*)(c.smem + L_PAR + ch * 4 + 16);
  const f32x4 b0 = *(const f32x4*)(c.smem + L_PAR + 512 + ch * 4), b1 = *(const f32x4*)(c.smem + L_PAR + 512 + ch * 4 + 16);
#pragma unroll
  for (int r = 0; r < 3; ++r) {
    const u32x2 lo = act4_scalar(u32x2{v[r][0], v[r][1]}, a0, b0), hi = act4_scalar(u32x2{v[r][2], v[r][3]}, a1, b1);
    *(u32x4*)(c.smem + (ht.off[r] ^ (k << 6))) = u32x4{lo[0], lo[1], hi[0], hi[1]};
  }
}

DI void xraw_issue(const Ctx& c, unsigned vo) {
#pragma unroll
  for (int row = 0; row < 10; ++row) {
    const int y = row == 0 ? (c.ty == 0 ? 0 : c.y0 - 1) : row == 9 ? (c.ty == 15 ? 127 : c.y0 + 8) : c.y0 - 1 + row;
    __builtin_amdgcn_raw_ptr_buffer_load_lds(c.rx, (LAS void*)(c.smem + L_RAW + row * RS + c.w * 1024), 16, vo, y * 256, 0, 0);
  }
}
struct XT { unsigned vo, ra, rh, wa[2], wh; bool huse, odd, hrow; };
DI void xraw_init(const Ctx& c, XT& x) {
  const unsigned lb = (unsigned)(size_t)(LAS char*)c.smem;
  int xs = c.x0 - 8 + 8 * (c.lane & 3);
  { const int lo = c.x0 & 64; xs = xs < lo ? lo : (xs > lo + 56 ? lo + 56 : xs); }
  x.vo = (unsigned)(((c.b * 128 + 16 * c.w + (c.lane >> 2)) * 128) * 128 + xs) * 2; PIN(x.vo);
  const int gsel = c.lane >> 4, i = c.lane & 15, qp = i >> 2, p = i & 3;
  x.ra = lb + L_RAW + (gsel & 1) * RS + (4 * (4 * c.w + (gsel >> 1)) + qp) * 64 + 16 + p * 8; PIN(x.ra);
#pragma unroll
  for (int a = 0; a < 2; ++a) { x.wa[a] = lb + L_T + (gsel & 1) * RP + (i + 1) * 256 + (((2 * c.w + a) ^ swz((i + 1) & 15)) << 4) + (gsel >> 1) * 8; PIN(x.wa[a]); }
  x.rh = lb + L_RAW + (p >> 1) * RS + (4 * (4 * c.w + gsel) + qp) * 64 + 8 + (p & 1) * 40; PIN(x.rh);
  const int side = qp & 1;
  x.wh = lb + L_T + (qp >> 1) * RP + side * 17 * 256 + (((2 * c.w + (gsel >> 1)) ^ side) << 4) + (gsel & 1) * 8; PIN(x.wh);
  const bool colok = side ? ((c.tx != 7) & (c.tx != 3)) : ((c.tx != 0) & (c.tx != 4));
  x.huse = (p == (side ? 0 : 3)) & colok; x.odd = gsel & 1; x.hrow = qp >> 1;
}
template <int RPAIR, int EXTRA>
DI void xraw_step(const XT& x, bool rowok, bool hok) {
  u32x2 v[3];
  constexpr int RO = (RPAIR & 1) * 2 * RS;
  const unsigned ra2 = x.ra + (RPAIR >> 1) * 4 * RS, rh2 = x.rh + (RPAIR >> 1) * 4 * RS;
  asm volatile("s_waitcnt vmcnt(%5)\n\t"
               "ds_read_b64_tr_b16 %0, %3 offset:%6\n\tds_read_b64_tr_b16 %1, %3 offset:%7\n\tds_read_b64_tr_b16 %2, %4 offset:%6\n\ts_waitcnt lgkmcnt(0)"
               : "=&v"(v[0]), "=&v"(v[1]), "=&v"(v[2])
               : "v"(ra2), "v"(rh2), "i"(8 - 2 * RPAIR + EXTRA), "i"(RO), "i"(RO + 512) : "memory");
  if (rowok) {
#pragma unroll
    for (int a = 0; a < 2; ++a) asm volatile("ds_write_b64 %0, %1 offset:%2" :: "v"(x.wa[a]), "v"(v[a]), "i"(RPAIR * 2 * RP) : "memory");
  }
  if (hok) asm volatile("ds_write_b64 %0, %1 offset:%2" :: "v"(x.wh), "v"(v[2]), "i"(RPAIR * 2 * RP) : "memory");
}
template <int EXTRA>
DI void xraw_transpose(const Ctx& c, const XT& x) {
  const bool top = c.ty == 0, bot = c.ty == 15;
  xraw_step<0, EXTRA>(x, !(top & !x.odd), x.huse & !(top & !x.hrow));
  xraw_step<1, EXTRA>(x, true, x.huse);
  xraw_step<2, EXTRA>(x, true, x.huse);
  xraw_step<3, EXTRA>(x, true, x.huse);
  xraw_step<4, EXTRA>(x, !(bot & x.odd), x.huse & !(bot & x.hrow));
}
struct XRow { u32x4 r[4]; };
DI void xrow_issue(const Ctx& c, XRow& xr, int yrow) {
  const unsigned o = (unsigned)(((c.b * 128 + 32 * (c.w & 3) + 16 * (c.w >> 2) + 4 * (c.lane >> 4)) * 128 + yrow) * 128) * 2 + (c.lane & 15) * 16;
#pragma unroll
  for (int jj = 0; jj < 4; ++jj) xr.r[jj] = bld<AUX_ROW>(c.rx, o, jj * 32768);
}
DI f32x2 xrow_stats(const XRow& xr) {
  float s = 0.f, s1 = 0.f, qs = 0.f, q1 = 0.f;
#pragma unroll
  for (int jj = 0; jj < 4; jj += 2)
#pragma unroll
    for (int k = 0; k < 4; ++k) { s = dot2(xr.r[jj][k], 0x3f803f80u, s); qs = dot2(xr.r[jj][k], xr.r[jj][k], qs); s1 = dot2(xr.r[jj + 1][k], 0x3f803f80u, s1); q1 = dot2(xr.r[jj + 1][k], xr.r[jj + 1][k], q1); }
  return f32x2{wave_sum(s + s1), wave_sum(qs + q1)};
}
DI void xrow_cross(const Ctx& c, const XRow& xr, int yrow) {
  const int k16 = c.lane & 15;
  if (k16 == 7 || k16 == 8) {
    unsigned v[2];
#pragma unroll
    for (int m = 0; m < 2; ++m) {
      const unsigned a = __builtin_amdgcn_perm(xr.r[2 * m + 1][3], xr.r[2 * m][3], 0x07060302u), b2 = __builtin_amdgcn_perm(xr.r[2 * m + 1][0], xr.r[2 * m][0], 0x05040100u);
      v[m] = k16 == 7 ? a : b2;
    }
    bst_sc1(u32x4{v[0], c.tag, v[1], c.tag}, c.rw, c.wb + WB_XC + (unsigned)((yrow * 2 + (k16 - 7)) * 64 + 16 * (c.w & 3) + 8 * (c.w >> 2) + 2 * (c.lane >> 4)) * 8, 0);
  }
}
struct Halo { unsigned go[4], lo[4]; bool inr[4], valid[4]; };
DI void halo_init(const Ctx& c, Halo& h) {
  const int tid = c.t - 256, cg = tid & 15, i = tid >> 4;
#pragma unroll
  for (int r = 0; r < 4; ++r) {
    const int row = r == 0 ? 0 : r == 1 ? 9 : r == 2 ? 1 + (i & 7) : (i >> 1) * 9;
    const int col = r < 2 ? i : r == 2 ? (i >> 3) * 17 : 16 + (i & 1);
    h.inr[r] = r < 3 ? true : i < 4;
    h.valid[r] = h.inr[r] & in_image(c, row, col);
    h.go[r] = WS_HB + (unsigned)(((c.b * 128 + c.y0 - 1 + row) * 128 + c.x0 - 1 + col) * 256 + cg * 16); PIN(h.go[r]);
    h.lo[r] = L_T + row * RP + col * 256 + ((cg ^ swz(col & 15)) << 4); PIN(h.lo[r]);
  }
}
struct Clr { unsigned o[3]; };
DI void clear_init(const Ctx& c, Clr& cl) {
  const int tid = c.t - 256, cg = tid & 15, i = tid >> 4, j = i < 12 ? i : 11;
#pragma unroll
  for (int r = 0; r < 3; ++r) {
    const int ry = r == 0 ? 0 : r == 1 ? 7 : 1 + (j >> 1), rx = r < 2 ? i : (j & 1) * 15;
    cl.o[r] = WS_HB + (unsigned)(((c.b * 128 + c.y0 + ry) * 128 + c.x0 + rx) * 256 + cg * 16); PIN(cl.o[r]);
  }
}
DI void stats_to_lds(const Ctx& c, int g, const u32x4& v0, const u32x4& v1) {
  const float s = wave_sum(__uint_as_float(v0[0]) + __uint_as_float(v1[0])), q = wave_sum(__uint_as_float(v0[2]) + __uint_as_float(v1[2]));
  const float mean = s * (1.f / 524288.f); float var = q * (1.f / 524288.f) - mean * mean; var = var < 0.f ? 0.f : var;
  if (c.lane == 0) *(f32x2*)(c.smem + L_STAT + g * 8) = f32x2{mean, __builtin_amdgcn_rsqf(var + 1e-5f)};
}

template <int CONV>
DI void mainloop(const Ctx& c, f32x4 (&acc)[8][2], bf16x8 (&wr)[WD], unsigned wvoff, const f32x4 (&ini)[2], unsigned baseP, unsigned baseD) {
  const char* T = c.smem + L_T;
  auto rd = [&](int B, int r) -> bf16x8 {
    const int sp = B / 3, ty = B % 3;
    const unsigned a = ty == 2 ? (baseD ^ (sp << 6)) : (baseP ^ ((2 * sp + ty) << 5));
    return *(const bf16x8*)(T + a + r * RP);
  };
  bf16x8 xf[4];
#pragma unroll
  for (int r = 0; r < 3; ++r) xf[r] = rd(0, r);
#pragma unroll
  for (int B = 0; B < 12; ++B) {
#pragma unroll
    for (int r = 0; r < 10; ++r) {
      const int G = B * 10 + r;
      if (r == 7 && (B % 3) == 2 && B < 11) { bar_nowait(); SB(); }
      { const int Gn = G + 3; if (Gn < 120) xf[Gn & 3] = rd(Gn / 10, Gn % 10); }
#pragma unroll
      for (int dy = 0; dy < 3; ++dy) {
        const int row = r - dy;
        if (row >= 0 && row < 8) {
#pragma unroll
          for (int nt = 0; nt < 2; ++nt) {
            const int ts = 6 * B + 2 * dy + nt;
            if (CONV == 1) asm volatile("v_mfma_f32_16x16x32_bf16 %0, %1, %2, %0" : "+v"(acc[row][nt]) : "v"(xf[G & 3]), "v"(wr[ts % WD]));
            else if (B == 0 && dy == 0) asm volatile("v_mfma_f32_16x16x32_bf16 %0, %1, %2, %3" : "=&v"(acc[row][nt]) : "v"(wr[ts % WD]), "v"(xf[G & 3]), "v"(ini[nt]));
            else           asm volatile("v_mfma_f32_16x16x32_bf16 %0, %1, %2, %0" : "+v"(acc[row][nt]) : "v"(wr[ts % WD]), "v"(xf[G & 3]));
            if (row == 7) { const int tn = CONV * 72 + ts + WD; if (tn < 144) wr[ts % WD] = __builtin_bit_cast(bf16x8, bld<SC1>(c.rw, wvoff, (unsigned)tn * 1024 + (tn >= 72 ? 294912u - 72u * 1024u : 0u))); }
            SB();
          }
        }
      }
    }
  }
  asm volatile("s_nop 15" ::: "memory");
  SB();
}
DI u32x4 pack_row(const f32x4 (&a)[2]) {
  const unsigned d0 = cvtpk(a[0][0], a[0][1]), d1 = cvtpk(a[0][2], a[0][3]), d2 = cvtpk(a[1][0], a[1][1]), d3 = cvtpk(a[1][2], a[1][3]);
  auto p = __builtin_amdgcn_permlane16_swap(d0, d2, false, false);
  auto q = __builtin_amdgcn_permlane16_swap(d1, d3, false, false);
  return u32x4{p[0], q[0], p[1], q[1]};
}

__global__ __launch_bounds__(512, 2) void k_resblock(const u16* __restrict__ x, const u16* __restrict__ t_emb, const u16* __restrict__ g1, const u16* __restrict__ be1,
                                                     const u16* __restrict__ w1, const u16* __restrict__ w_emb, const u16* __restrict__ g2, const u16* __restrict__ be2,
                                                     const u16* __restrict__ w2, u16* __restrict__ out, char* ws) {
  extern __shared__ __attribute__((aligned(256))) char smem[];
  Ctx c;
  c.smem = smem; c.ws = ws;
  c.t = threadIdx.x; c.lane = c.t & 63; c.w = __builtin_amdgcn_readfirstlane(c.t >> 6);
  const int xcd = blockIdx.x & 7, jx = blockIdx.x >> 3;
  c.b = xcd >> 2; c.ty = (xcd & 3) * 4 + (jx >> 3); c.tx = jx & 7;
  c.jb = c.ty * 8 + c.tx; c.y0 = c.ty * 8; c.x0 = c.tx * 16;
  c.rx = mk_rsrc(x, 8u << 20); c.rw = mk_rsrc(ws, 16u << 20);
  c.wb = (unsigned)c.b * WB_SIZE;

  if (c.w < 4) {
    STAMP_DECL STAMP(0);
    const int yrow = (xcd & 3) * 32 + jx;
    const unsigned epoch = __hip_atomic_load((GAS unsigned*)(ws + c.wb + WB_EPOCH), __ATOMIC_RELAXED, __HIP_MEMORY_SCOPE_AGENT);
    XRow xr; xrow_issue(c, xr, yrow);
    const int ch = c.t & 127;
    const unsigned gbA = (c.t < 128 ? g1 : be1)[ch], gbB = (c.t < 128 ? g2 : be2)[ch];
    const int px = c.lane & 15, q = c.lane >> 4;
    const rsrc_t ro = mk_rsrc(out, 8u << 20);
    SB();
    unsigned oo = (unsigned)(((c.b * 128 + 32 * c.w + 16 * (q & 1) + px) * 128 + c.y0) * 128 + c.x0 + 8 * (q >> 1)) * 2; PIN(oo);
    XT xt; xraw_init(c, xt);
    Task0 t0; task0_init(c, t0);
    unsigned baseP, baseD;
    { const int colP = px + (q & 1), colD = px + 2;
      baseP = colP * 256 + (((q >> 1) ^ swz(colP & 15)) << 4); baseD = colD * 256 + ((q ^ swz(colD & 15)) << 4); PIN(baseP); PIN(baseD); }
    unsigned wvoff = c.wb + WB_WF + (unsigned)c.w * 73728u + (unsigned)c.lane * 16u; PIN(wvoff);
    unsigned fo = c.wb + WB_FLAG + (unsigned)(c.w * 128 + (c.lane & 7) * 4); PIN(fo);
    SB();
    STAMP(2);
    c.tag = __builtin_amdgcn_readfirstlane(epoch) + 1u;
    { const f32x2 p = xrow_stats(xr); if (c.lane == 0) *(f32x2*)(smem + L_RED + c.w * 8) = p; }
    bar();
    STAMP(14);
    xrow_cross(c, xr, yrow);
    { float* gbp = (float*)(smem + L_GB) + (c.t < 128 ? 0 : 128) + ch; gbp[0] = bflo(gbA) * LOG2E; gbp[256] = bflo(gbB) * LOG2E; }
    SB();
    xraw_issue(c, xt.vo); SB();
    unsigned fl = __builtin_amdgcn_raw_buffer_load_b32(c.rw, fo, 0, SC1); SB();
    STAMP(15);
    xraw_transpose<1>(c, xt); SB();
    STAMP(3);
    for (int spin = 0; spin < SPIN_MAX; ++spin) {
      if (!__any(fl != c.tag)) break;
      __builtin_amdgcn_s_sleep(POLL_SLEEP);
      cbar();
      fl = __builtin_amdgcn_raw_buffer_load_b32(c.rw, fo, 0, SC1);
    }
    STAMP(4);
    bf16x8 wr[WD];
#pragma unroll
    for (int k = 0; k < WD; ++k) wr[k] = __builtin_bit_cast(bf16x8, bld<SC1>(c.rw, wvoff, (unsigned)k * 1024));
    bar();
    STAMP(5);
    norm_group0(c, t0, 0);
    bar();
    STAMP(6);
    f32x4 acc[8][2], ini[2];
    ini[0] = *(const f32x4*)(smem + L_EMB + (32 * c.w + 4 * q) * 4); ini[1] = *(const f32x4*)(smem + L_EMB + (32 * c.w + 16 + 4 * q) * 4);
    __builtin_amdgcn_s_setprio(1);
    mainloop<0>(c, acc, wr, wvoff, ini, baseP, baseD);
    __builtin_amdgcn_s_setprio(0);
    STAMP(7);
    {
      f32x2 s2 = {0.f, 0.f}, q2 = {0.f, 0.f};
#pragma unroll
      for (int r = 0; r < 8; ++r)
#pragma unroll
        for (int nt = 0; nt < 2; ++nt)
#pragma unroll
          for (int k = 0; k < 2; ++k) { const f32x2 v = {acc[r][nt][2 * k], acc[r][nt][2 * k + 1]}; s2 += v; q2 += v * v; }
      const float s = wave_sum(s2[0] + s2[1]), qs = wave_sum(q2[0] + q2[1]);
      if (c.lane == 0) bst_sc1(u32x4{__float_as_uint(s), c.tag, __float_as_uint(qs), c.tag}, c.rw, c.wb + WB_G2 + (unsigned)(c.w * 128 + c.jb) * 16, 0);
    }
    STAMP(1);
    {
      const int chk = 4 * c.w + (((q & 1) << 1) | (q >> 1));
      const unsigned go = WS_HB + (unsigned)(((c.b * 128 + c.y0) * 128 + c.x0 + px) * 256 + chk * 16);
      u32x4 hv[8];
#pragma unroll
      for (int r = 0; r < 8; ++r) hv[r] = pack_row(acc[r]);
      auto san = [](u32x4 v) { return __builtin_elementwise_min(v, u32x4{0xfffffffeu, 0xfffffffeu, 0xfffffffeu, 0xfffffffeu}); };
      bst_sc1(san(hv[0]), c.rw, go, 0); bst_sc1(san(hv[7]), c.rw, go, 7 * 32768);
      if (px == 0 || px == 15) {
#pragma unroll
        for (int r = 1; r < 7; ++r) bst_sc1(san(hv[r]), c.rw, go, r * 32768);
      }
      bar_nowait();
      char* lp = smem + L_T + RP + (px + 1) * 256 + ((chk ^ swz((px + 1) & 15)) << 4);
#pragma unroll
      for (int r = 0; r < 8; ++r) *(u32x4*)(lp + r * RP) = hv[r];
    }
    STAMP(8);
#pragma unroll
    for (int r = 0; r < 8; ++r) {
      const u32x4 v = *(const u32x4*)(smem + L_RAW + (r + 1) * RS + (32 * c.w + 16 * (q & 1) + px) * 64 + 16 + (q >> 1) * 16);
      auto a = __builtin_amdgcn_permlane16_swap(v[0], v[2], false, false);
      auto e = __builtin_amdgcn_permlane16_swap(v[1], v[3], false, false);
      acc[r][0] = f32x4{bflo(a[0]), bfhi(a[0]), bflo(e[0]), bfhi(e[0])};
      acc[r][1] = f32x4{bflo(a[1]), bfhi(a[1]), bflo(e[1]), bfhi(e[1])};
      asm volatile("" : "+v"(acc[r][0]), "+v"(acc[r][1]));
    }
    STAMP(9);
    bar();
    STAMP(10);
    norm_group0(c, t0, 1);
    bar();
    STAMP(11);
    __builtin_amdgcn_s_setprio(1);
    mainloop<1>(c, acc, wr, wvoff, ini, baseP, baseD);
    __builtin_amdgcn_s_setprio(0);
    STAMP(12);
#pragma unroll
    for (int r = 0; r < 8; ++r) __builtin_amdgcn_raw_buffer_store_b128(pack_row(acc[r]), ro, oo + r * 256, 0, 0);
    STAMP(13); STAMP_FLUSH;
  } else {
    STAMP_DECL STAMP(0);
    const int g = c.w - 4;
    const int yrow = (xcd & 3) * 32 + jx;
    const int ksvc = jx * 4 + (xcd & 3);
    const bool svc_w = c.w == 7 && (ksvc < 32 || (ksvc >= 64 && ksvc < 96)), svc_e = c.w == 7 && ksvc >= 32 && ksvc < 64;
    const int wu = ksvc < 32 ? ksvc : ksvc - 32;
    const unsigned epoch = __hip_atomic_load((GAS unsigned*)(ws + c.wb + WB_EPOCH), __ATOMIC_RELAXED, __HIP_MEMORY_SCOPE_AGENT);
    XRow xr; xrow_issue(c, xr, yrow);
    SB();
    u32x4 sv[11]; unsigned te[6];
    if (svc_w) {
      const int r3 = (c.lane * 57) >> 10, pc = c.lane - 18 * r3;
      const rsrc_t rws = mk_rsrc((wu >> 5) ? w2 : w1, 294912u);
      const unsigned o = (unsigned)(((32 * ((wu >> 3) & 3) + r3) * 128 + 16 * (wu & 7)) * 18 + pc * 16);
#pragma unroll
      for (int k = 0; k < 11; ++k) sv[k] = bld<AUX_SVC>(rws, o, k * 3 * 2304);
    } else if (svc_e) {
      const int j = ksvc - 32, e0 = 192 * (j >> 3) + (c.lane >> 1);
      const rsrc_t re = mk_rsrc(w_emb, 196608u), rt = mk_rsrc(t_emb, 3072u);
#pragma unroll
      for (int k = 0; k < 6; ++k) { sv[k] = bld<AUX_SVC>(re, (unsigned)(e0 * 256 + (16 * (j & 7) + 8 * (c.lane & 1)) * 2), k * 32 * 256);
                                    te[k] = __builtin_amdgcn_raw_buffer_load_b16(rt, (unsigned)(c.b * 768 + e0) * 2, k * 64, 0); }
    }
    SB();
    XT xt; xraw_init(c, xt);
    Task0 t0; task0_init(c, t0);
    HTask ht; htask_init(c, ht);
    if ((c.ty == 0) | (c.ty == 15) | (c.tx == 0) | (c.tx == 7)) {
      Halo hz; halo_init(c, hz);
#pragma unroll
      for (int r = 0; r < 4; ++r) if (hz.inr[r] & !hz.valid[r]) *(u32x4*)(smem + hz.lo[r]) = u32x4{0, 0, 0, 0};
    }
    SB();
    STAMP(4);
    c.tag = __builtin_amdgcn_readfirstlane(epoch) + 1u;
    const f32x2 psum = xrow_stats(xr);
    bar();
    { const f32x2 p = psum + *(const f32x2*)(smem + L_RED + g * 8);
      if (c.lane == 0) bst_sc1(u32x4{__float_as_uint(p[0]), c.tag, __float_as_uint(p[1]), c.tag}, c.rw, c.wb + WB_G1 + (unsigned)(g * 128 + c.jb) * 16, 0); }
    SB();
    STAMP(1);
    xrow_cross(c, xr, yrow);
    if (svc_w) {
      if (c.lane < 54) {
#pragma unroll
        for (int k = 0; k < 11; ++k) *(u32x4*)(smem + L_SVC + k * 864 + c.lane * 16) = sv[k];
      }
#pragma unroll
      for (int k = 0; k < 9; ++k) { sv[k] = *(const u32x4*)(smem + L_SVC + (c.lane & 31) * 288 + (c.lane >> 5) * 144 + k * 16); asm volatile("" : "+v"(sv[k])); }
    }
    SB();
    xraw_issue(c, xt.vo); SB();
    STAMP(5);
    if (svc_w) {
      const int cs = wu & 7, o = c.lane & 31, h = c.lane >> 5;
      const unsigned dst = c.wb + WB_WF + (unsigned)(wu >> 5) * 294912u + (unsigned)((wu >> 3) & 3) * 73728u + (unsigned)(o >> 4) * 1024u + (unsigned)(o & 15) * 16u;
      const unsigned dP = dst + (unsigned)(3 * (cs >> 1) + (cs & 1)) * 6144u + (unsigned)h * 512u;
      const unsigned dD = dst + (unsigned)(3 * (cs >> 1) + 2) * 6144u + (unsigned)(2 * (cs & 1) + h) * 256u;
#pragma unroll
      for (int k = 0; k < 9; ++k) {
        const int dy = k / 3, dx = k % 3;
        u32x4 o4;
#pragma unroll
        for (int m = 0; m < 4; ++m) {
          const int e0 = 18 * m + k, e1 = e0 + 9;
          const unsigned sel = ((e0 & 1) ? 0x0302u : 0x0100u) | ((e1 & 1) ? 0x07060000u : 0x05040000u);
          o4[m] = __builtin_amdgcn_perm(sv[e1 >> 3][(e1 >> 1) & 3], sv[e0 >> 3][(e0 >> 1) & 3], sel);
        }
        bst_sc1(o4, c.rw, dx < 2 ? dP : dD, dy * 2048 + (dx < 2 ? dx * 256 : 0));
      }
      SB();
      xraw_transpose<9>(c, xt);
      asm volatile("s_waitcnt vmcnt(0)" ::: "memory");
      if (c.lane == 0) __hip_atomic_store((GAS unsigned*)(ws + c.wb + WB_FLAG + (wu >> 3) * 128 + (wu & 7) * 4), c.tag, __ATOMIC_RELAXED, __HIP_MEMORY_SCOPE_AGENT);
    } else if (svc_e) {
      f32x2 a[4];
#pragma unroll
      for (int m = 0; m < 4; ++m) a[m] = f32x2{0.f, 0.f};
#pragma unroll
      for (int k = 0; k < 6; ++k) {
        unsigned tk = te[k]; PIN(tk);
        const float tv = bflo(tk);
        const float sl = tv * __builtin_amdgcn_rcpf(1.f + __builtin_amdgcn_exp2f(-tv * LOG2E));
#pragma unroll
        for (int m = 0; m < 4; ++m) a[m] += f32x2{bflo(sv[k][m]), bfhi(sv[k][m])} * sl;
      }
      float r[8];
#pragma unroll
      for (int m = 0; m < 8; ++m) r[m] = rows_sum(dppadd<0x128>(dppadd<0x124>(dppadd<0x4E>(a[m >> 1][m & 1]))));
      const int j = ksvc - 32;
      if (c.lane < 2) {
        const unsigned o = c.wb + WB_EG + (unsigned)((j >> 3) * 128 + 16 * (j & 7) + 8 * c.lane) * 8;
#pragma unroll
        for (int m = 0; m < 4; ++m) bst_sc1(u32x4{__float_as_uint(r[2 * m]), c.tag, __float_as_uint(r[2 * m + 1]), c.tag}, c.rw, o, m * 16);
      }
      SB();
      xraw_transpose<4>(c, xt);
    } else xraw_transpose<0>(c, xt);
    SB();
    STAMP(2);
    {
      u32x4 v0, v1, vc[2]; bool n0 = true, n1 = true, nc[2];
      const unsigned o = c.wb + WB_G1 + (unsigned)(g * 128 + c.lane) * 16;
      const bool xc = c.tx == 3 || c.tx == 4;
      unsigned co[2], cl2[2]; bool cin[2];
#pragma unroll
      for (int r = 0; r < 2; ++r) {
        const int id = c.t - 256 + 256 * r, row = id >> 5, i = id & 31, y = c.y0 - 1 + row, side = c.tx == 3 ? 1 : 0;
        cin[r] = xc & (id < 320);
        nc[r] = cin[r] & ((unsigned)y < 128u);
        co[r] = c.wb + WB_XC + (unsigned)((y * 2 + side) * 64 + 2 * i) * 8;
        cl2[r] = L_T + row * RP + side * 17 * 256 + (((i >> 1) ^ side) << 4) + (i & 1) * 8;
        vc[r] = u32x4{0, 0, 0, 0};
      }
      for (int spin = 0; spin < SPIN_MAX; ++spin) {
        cbar();
        if (n0) v0 = bld<SC1>(c.rw, o, 0);
        if (n1) v1 = bld<SC1>(c.rw, o, 1024);
        if (xc) {
#pragma unroll
          for (int r = 0; r < 2; ++r) if (nc[r]) vc[r] = bld<SC1>(c.rw, co[r], 0);
        }
        n0 = !((v0[1] == c.tag) & (v0[3] == c.tag)); n1 = !((v1[1] == c.tag) & (v1[3] == c.tag));
#pragma unroll
        for (int r = 0; r < 2; ++r) nc[r] = nc[r] & !((vc[r][1] == c.tag) & (vc[r][3] == c.tag));
        if (!__any(n0 | n1 | nc[0] | nc[1])) break;
        __builtin_amdgcn_s_sleep(POLL_SLEEP);
      }
      stats_to_lds(c, g, v0, v1);
      if (xc) {
#pragma unroll
        for (int r = 0; r < 2; ++r) if (cin[r]) *(u32x2*)(smem + cl2[r]) = u32x2{vc[r][0], vc[r][2]};
      }
    }
    u32x4 ve[4];
    if (c.w == 4) {
#pragma unroll
      for (int k = 0; k < 4; ++k) ve[k] = bld<SC1>(c.rw, c.wb + WB_EG + (unsigned)c.lane * 16, k * 1024);
    }
    STAMP(6);
    bar();
    if (c.t == 256 && c.jb == 0) __hip_atomic_store((GAS unsigned*)(ws + c.wb + WB_EPOCH), c.tag, __ATOMIC_RELAXED, __HIP_MEMORY_SCOPE_AGENT);
    norm_group0(c, t0, 0);
    if (c.w == 4) {
      for (int spin = 0; spin < SPIN_MAX; ++spin) {
        bool ok = true;
#pragma unroll
        for (int k = 0; k < 4; ++k) ok = ok & (ve[k][1] == c.tag) & (ve[k][3] == c.tag);
        if (!__any(!ok)) break;
        cbar();
#pragma unroll
        for (int k = 0; k < 4; ++k) ve[k] = bld<SC1>(c.rw, c.wb + WB_EG + (unsigned)c.lane * 16, k * 1024);
      }
      *(f32x2*)(smem + L_EMB + c.lane * 8) = f32x2{(__uint_as_float(ve[0][0]) + __uint_as_float(ve[1][0])) + (__uint_as_float(ve[2][0]) + __uint_as_float(ve[3][0])),
                                                  (__uint_as_float(ve[0][2]) + __uint_as_float(ve[1][2])) + (__uint_as_float(ve[2][2]) + __uint_as_float(ve[3][2]))};
    }
    bar();
#pragma unroll 1
    for (int k = 1; k < 4; ++k) {
      if (k == 2) {
        Clr cl; clear_init(c, cl);
#pragma unroll
        for (int r = 0; r < 3; ++r) bst_sc1(u32x4{~0u, ~0u, ~0u, ~0u}, c.rw, cl.o[r], 0);
      }
      norm_group_h(c, ht, k);
      if (k == 2) asm volatile("s_waitcnt vmcnt(0)" ::: "memory");
      if (k == 3 && c.w == 6) {
        const unsigned fo = c.wb + WB_FLAG + (unsigned)((4 + ((c.lane >> 3) & 3)) * 128 + (c.lane & 7) * 4);
        for (int spin = 0; spin < SPIN_MAX; ++spin) {
          cbar();
          const unsigned f = __builtin_amdgcn_raw_buffer_load_b32(c.rw, fo, 0, SC1);
          if (!__any(f != c.tag)) break;
        }
      }
      bar();
      if (k == 2 && c.t == 256) __hip_atomic_store((GAS unsigned*)(ws + c.wb + WB_CF + c.jb * 4), c.tag, __ATOMIC_RELAXED, __HIP_MEMORY_SCOPE_AGENT);
    }
    STAMP(7);
    {
      Halo hl; halo_init(c, hl);
      if (c.w == 5) {
        const int l9 = c.lane < 9 ? c.lane : 4, dy = ((l9 * 11) >> 5) - 1, dx = l9 - 3 * (dy + 1) - 1;
        const int ny = c.ty + dy, nx = c.tx + dx;
        const bool chk = ((unsigned)ny < 16u) & ((unsigned)nx < 8u);
        const unsigned fo = c.wb + WB_CF + (unsigned)(chk ? ny * 8 + nx : c.jb) * 4;
        for (int spin = 0; spin < SPIN_MAX; ++spin) {
          cbar();
          const unsigned f = __builtin_amdgcn_raw_buffer_load_b32(c.rw, fo, 0, SC1);
          if (!__any(f != c.tag)) break;
          __builtin_amdgcn_s_sleep(2);
        }
      }
      u32x4 hv[4]; bool need[4];
#pragma unroll
      for (int r = 0; r < 4; ++r) { need[r] = hl.valid[r]; hv[r] = u32x4{0, 0, 0, 0}; }
      u32x4 v0, v1; bool n0 = true, n1 = true;
      const unsigned o = c.wb + WB_G2 + (unsigned)(g * 128 + c.lane) * 16;
      bar_nowait();
      STAMP(13);
      for (int spin = 0; spin < SPIN_MAX; ++spin) {
        cbar();
        if (n0) v0 = bld<SC1>(c.rw, o, 0);
        if (n1) v1 = bld<SC1>(c.rw, o, 1024);
#pragma unroll
        for (int r = 0; r < 4; ++r) if (need[r]) hv[r] = bld<SC1>(c.rw, hl.go[r], 0);
        bool any = false;
#pragma unroll
        for (int r = 0; r < 4; ++r) { need[r] = need[r] & ((hv[r][0] == ~0u) | (hv[r][1] == ~0u) | (hv[r][2] == ~0u) | (hv[r][3] == ~0u)); any |= need[r]; }
        n0 = !((v0[1] == c.tag) & (v0[3] == c.tag)); n1 = !((v1[1] == c.tag) & (v1[3] == c.tag));
        if (!__any(any | n0 | n1)) break;
        __builtin_amdgcn_s_sleep(POLL_SLEEP2);
      }
      STAMP(8);
#pragma unroll
      for (int r = 0; r < 4; ++r) if (hl.valid[r]) *(u32x4*)(smem + hl.lo[r]) = hv[r];
      stats_to_lds(c, g, v0, v1);
    }
    bar();
    norm_group0(c, t0, 1);
    bar();
#pragma unroll 1
    for (int k = 1; k < 4; ++k) { if (k == 1) STAMP(9); norm_group_h(c, ht, k); if (k == 1) STAMP(10); if (k == 2) STAMP(11); bar(); }
    STAMP(12); STAMP_FLUSH;
  }
}

extern "C" void kernel_launch(void* const* d_in, const int* in_sizes, int n_in, void* d_out, int out_size, void* d_ws, size_t ws_size, hipStream_t stream) {
  (void)hipFuncSetAttribute((const void*)k_resblock, hipFuncAttributeMaxDynamicSharedMemorySize, L_END);
  k_resblock<<<256, 512, L_END, stream>>>((const u16*)d_in[0], (const u16*)d_in[1], (const u16*)d_in[2], (const u16*)d_in[3], (const u16*)d_in[4],
                                          (const u16*)d_in[5], (const u16*)d_in[6], (const u16*)d_in[7], (const u16*)d_in[8], (u16*)d_out, (char*)d_ws);
}
```

```cpp
#define DIAG 1
#define DIAG_WG 100
#define DIAG_WAVE 0
#define DIAG_A 5
#define DIAG_B 6
#define DIAG_SCALE 4
#include <hip/hip_runtime.h>
#include <hip/hip_bf16.h>
#include <stdint.h>

typedef float f32x4 __attribute__((ext_vector_type(4)));
typedef float f32x2 __attribute__((ext_vector_type(2)));
typedef __bf16 bf16x8 __attribute__((ext_vector_type(8)));
typedef __bf16 bf16x2 __attribute__((ext_vector_type(2)));
typedef unsigned u32x4 __attribute__((ext_vector_type(4)));
typedef unsigned u32x2 __attribute__((ext_vector_type(2)));
typedef unsigned short u16;
#define GAS __attribute__((address_space(1)))
#define LAS __attribute__((address_space(3)))
#define DI __device__ __forceinline__
#define SB() __builtin_amdgcn_sched_barrier(0)
#define PIN(x) asm volatile("" : "+v"(x))

constexpr int RP = 4608;
constexpr int TILE_BYTES = 10 * RP;
constexpr int RS = 8224;
constexpr int WD = 18;
constexpr float LOG2E = 1.4426950408889634f;
constexpr int SC1 = 16, NT = 2;
constexpr int SPIN_MAX = 1 << 16;
#define POLL_SLEEP 4
#define POLL_SLEEP2 2
#define AUX_SVC 0
#define AUX_ROW 2

constexpr unsigned WB_SIZE = 1u << 20;
constexpr unsigned WB_WF = 0, WB_EPOCH = 589824, WB_FLAG = WB_EPOCH + 256  , WB_G1 = WB_FLAG + 1024, WB_G2 = WB_G1 + 8192,
                   WB_EG = WB_G2 + 8192  , WB_CF = WB_EG + 4096  , WB_XC = WB_CF + 512  ;
constexpr unsigned WS_HB = 2u << 20;
constexpr int L_T = 0, L_RAW = TILE_BYTES, L_GB = L_RAW + 10 * RS  , L_PAR = L_GB + 2048  , L_EMB = L_PAR + 1024,
              L_STAT = L_EMB + 512  , L_RED = L_STAT + 32  , L_DUMMY = (L_RED + 32 + 255) / 256 * 256  , L_SVC = L_DUMMY + 1024  ,
              L_END = L_SVC + 9504;

DI int swz(int c) { return c ^ ((c >> 1) & 4); }
DI unsigned cvtpk(float lo, float hi) { f32x2 v = {lo, hi}; bf16x2 b = __builtin_convertvector(v, bf16x2); return __builtin_bit_cast(unsigned, b); }
DI unsigned cvtpk_s(float lo, float hi) { unsigned r; asm("v_cvt_pk_bf16_f32 %0, %1, %2" : "=v"(r) : "v"(lo), "v"(hi)); return r; }
DI float bflo(unsigned d) { return __uint_as_float(d << 16); }
DI float bfhi(unsigned d) { return __uint_as_float(d & 0xffff0000u); }
DI float dot2(unsigned a, unsigned b, float c) { return __builtin_amdgcn_fdot2_f32_bf16(__builtin_bit_cast(bf16x2, a), __builtin_bit_cast(bf16x2, b), c, false); }
template <int CTRL> DI float dppadd(float v) { return v + __int_as_float(__builtin_amdgcn_update_dpp(0, __float_as_int(v), CTRL, 0xf, 0xf, true)); }
DI float rows_sum(float v) {
  auto p = __builtin_amdgcn_permlane16_swap(__float_as_uint(v), __float_as_uint(v), false, false);
  v = __uint_as_float(p[0]) + __uint_as_float(p[1]);
  auto q = __builtin_amdgcn_permlane32_swap(__float_as_uint(v), __float_as_uint(v), false, false);
  return __uint_as_float(q[0]) + __uint_as_float(q[1]);
}
DI float wave_sum(float v) { v = dppadd<0xB1>(v); v = dppadd<0x4E>(v); v = dppadd<0x141>(v); v = dppadd<0x140>(v); return rows_sum(v); }
DI void bar() { asm volatile("s_waitcnt lgkmcnt(0)\n\ts_barrier" ::: "memory"); }
DI void bar_nowait() { asm volatile("s_barrier" ::: "memory"); }
DI void cbar() { asm volatile("" ::: "memory"); }
typedef __amdgpu_buffer_rsrc_t rsrc_t;
DI rsrc_t mk_rsrc(const void* p, unsigned bytes) { return __builtin_amdgcn_make_buffer_rsrc((void*)p, 0, bytes, 0x00020000); }
template <int AUX> DI u32x4 bld(rsrc_t r, unsigned voff, unsigned soff) { return __builtin_amdgcn_raw_buffer_load_b128(r, voff, soff, AUX); }
DI void bst_sc1(u32x4 v, rsrc_t r, unsigned voff, unsigned off) { __builtin_amdgcn_raw_buffer_store_b128(v, r, voff + off, 0, SC1); }

struct Ctx {
  rsrc_t rx, rw;
  char* smem; char* ws;
  int b, ty, tx, jb, y0, x0, w, lane, t;
  unsigned wb;
  unsigned tag;
};

#define STAMP_DECL unsigned st_t[16]; _Pragma("unroll") for (int i_ = 0; i_ < 16; ++i_) st_t[i_] = 0;
#define STAMP(idx) do { if (idx == DIAG_A || idx == DIAG_B) { st_t[idx] = (unsigned)__builtin_amdgcn_s_memrealtime(); asm volatile("" : "+s"(st_t[idx])); } } while (0)
#define STAMP_FLUSH do { if (blockIdx.x == DIAG_WG && c.w == DIAG_WAVE) { const int val_ = (int)(st_t[DIAG_B] - st_t[DIAG_A]) * DIAG_SCALE; \
  const unsigned t0_ = (unsigned)__builtin_amdgcn_s_memrealtime(); while ((int)((unsigned)__builtin_amdgcn_s_memrealtime() - t0_) < val_) __builtin_amdgcn_s_sleep(1); } } while (0)

DI u32x2 act4_packed(u32x2 v, f32x4 a, f32x4 b) {
  u32x2 o;
#pragma unroll
  for (int m = 0; m < 2; ++m) {
    f32x2 xx = {bflo(v[m]), bfhi(v[m])}, aa = {a[2 * m], a[2 * m + 1]}, bb = {b[2 * m], b[2 * m + 1]};
    f32x2 u = xx * aa + bb;
    f32x2 e = {__builtin_amdgcn_exp2f(-u[0]), __builtin_amdgcn_exp2f(-u[1])};
    f32x2 d = e * LOG2E + LOG2E;
    f32x2 r = {__builtin_amdgcn_rcpf(d[0]), __builtin_amdgcn_rcpf(d[1])};
    f32x2 y = u * r;
    o[m] = cvtpk(y[0], y[1]);
  }
  return o;
}
DI float act1(float x, float a, float b) { float u = __builtin_fmaf(x, a, b); float e = __builtin_amdgcn_exp2f(-u); return u * __builtin_amdgcn_rcpf(__builtin_fmaf(e, LOG2E, LOG2E)); }
DI u32x2 act4_scalar(u32x2 v, f32x4 a, f32x4 b) {
  u32x2 o;
  o[0] = cvtpk_s(act1(bflo(v[0]), a[0], b[0]), act1(bfhi(v[0]), a[1], b[1]));
  o[1] = cvtpk_s(act1(bflo(v[1]), a[2], b[2]), act1(bfhi(v[1]), a[3], b[3]));
  return o;
}
DI bool in_image(const Ctx& c, int row, int col) { return ((unsigned)(c.y0 - 1 + row) < 128u) & ((unsigned)(c.x0 - 1 + col) < 128u); }
struct Tasks { unsigned off[3]; };
DI void tasks_init(const Ctx& c, Tasks& tk, int idx, int c4) {
  const unsigned lo = (c4 & 1) * 8, dummy = L_DUMMY + c.lane * 16 + lo;
  const int col = idx & 15, row = idx >> 4;
  const unsigned o0 = L_T + row * RP + col * 256 + (((c4 >> 1) ^ swz(col)) << 4) + lo;
  const int j = idx - 32, row2 = idx < 32 ? 8 + row : j >> 1, col2 = idx < 32 ? col : 16 + (j & 1);
  const unsigned o2 = L_T + row2 * RP + col2 * 256 + (((c4 >> 1) ^ swz(col2 & 15)) << 4) + lo;
  tk.off[0] = o0; tk.off[1] = o0 + 4 * RP; tk.off[2] = idx < 52 ? o2 : dummy;
  if ((c.ty == 0) | (c.ty == 15) | (c.tx == 0) | (c.tx == 7)) {
    tk.off[0] = in_image(c, row, col) ? tk.off[0] : dummy;
    tk.off[1] = in_image(c, row + 4, col) ? tk.off[1] : dummy;
    tk.off[2] = in_image(c, row2, col2) ? tk.off[2] : dummy;
  }
#pragma unroll
  for (int r = 0; r < 3; ++r) PIN(tk.off[r]);
}
typedef Tasks Task0;
DI void task0_init(const Ctx& c, Task0& t0) { tasks_init(c, t0, c.t >> 3, c.t & 7); }
DI void norm_group0(const Ctx& c, const Task0& t0, int gbsel) {
  u32x2 v[3];
#pragma unroll
  for (int r = 0; r < 3; ++r) v[r] = *(const u32x2*)(c.smem + t0.off[r]);
  const int ch = 4 * (c.t & 7);
  const f32x4 g = *(const f32x4*)(c.smem + L_GB + gbsel * 1024 + ch * 4), be = *(const f32x4*)(c.smem + L_GB + gbsel * 1024 + 512 + ch * 4);
  const f32x2 st = *(const f32x2*)(c.smem + L_STAT);
  const f32x4 a = g * st[1], b = be - a * st[0];
#pragma unroll
  for (int r = 0; r < 2; ++r) *(u32x2*)(c.smem + t0.off[r]) = act4_packed(v[r], a, b);
  if (c.w < 7) *(u32x2*)(c.smem + t0.off[2]) = act4_packed(v[2], a, b);
  else if (c.lane < 48) {
    const int c2 = 32 + 2 * c.lane;
    const f32x2 g2 = *(const f32x2*)(c.smem + L_GB + gbsel * 1024 + c2 * 4), b2 = *(const f32x2*)(c.smem + L_GB + gbsel * 1024 + 512 + c2 * 4);
    const f32x2 s2 = *(const f32x2*)(c.smem + L_STAT + (c2 >> 5) * 8);
    const f32x2 aa = g2 * s2[1];
    *(f32x2*)(c.smem + L_PAR + c2 * 4) = aa; *(f32x2*)(c.smem + L_PAR + 512 + c2 * 4) = b2 - aa * s2[0];
  }
}
typedef Tasks HTask;
DI void htask_init(const Ctx& c, HTask& ht) { tasks_init(c, ht, (c.t - 256) >> 2, 2 * (c.t & 3)); }
DI void norm_group_h(const Ctx& c, const HTask& ht, int k) {
  const int ch = 32 * k + 8 * (c.t & 3);
  u32x4 v[3];
#pragma unroll
  for (int r = 0; r < 3; ++r) v[r] = *(const u32x4*)(c.smem + (ht.off[r] ^ (k << 6)));
  const f32x4 a0 = *(const f32x4*)(c.smem + L_PAR + ch * 4), a1 = *(const f32x4*)(c.smem + L_PAR + ch * 4 + 16);
  const f32x4 b0 = *(const f32x4*)(c.smem + L_PAR + 512 + ch * 4), b1 = *(const f32x4*)(c.smem + L_PAR + 512 + ch * 4 + 16);
#pragma unroll
  for (int r = 0; r < 3; ++r) {
    const u32x2 lo = act4_scalar(u32x2{v[r][0], v[r][1]}, a0, b0), hi = act4_scalar(u32x2{v[r][2], v[r][3]}, a1, b1);
    *(u32x4*)(c.smem + (ht.off[r] ^ (k << 6))) = u32x4{lo[0], lo[1], hi[0], hi[1]};
  }
}

DI void xraw_issue(const Ctx& c, unsigned vo) {
#pragma unroll
  for (int row = 0; row < 10; ++row) {
    const int y = row == 0 ? (c.ty == 0 ? 0 : c.y0 - 1) : row == 9 ? (c.ty == 15 ? 127 : c.y0 + 8) : c.y0 - 1 + row;
    __builtin_amdgcn_raw_ptr_buffer_load_lds(c.rx, (LAS void*)(c.smem + L_RAW + row * RS + c.w * 1024), 16, vo, y * 256, 0, 0);
  }
}
struct XT { unsigned vo, ra, rh, wa[2], wh; bool huse, odd, hrow; };
DI void xraw_init(const Ctx& c, XT& x) {
  const unsigned lb = (unsigned)(size_t)(LAS char*)c.smem;
  int xs = c.x0 - 8 + 8 * (c.lane & 3);
  { const int lo = c.x0 & 64; xs = xs < lo ? lo : (xs > lo + 56 ? lo + 56 : xs); }
  x.vo = (unsigned)(((c.b * 128 + 16 * c.w + (c.lane >> 2)) * 128) * 128 + xs) * 2; PIN(x.vo);
  const int gsel = c.lane >> 4, i = c.lane & 15, qp = i >> 2, p = i & 3;
  x.ra = lb + L_RAW + (gsel & 1) * RS + (4 * (4 * c.w + (gsel >> 1)) + qp) * 64 + 16 + p * 8; PIN(x.ra);
#pragma unroll
  for (int a = 0; a < 2; ++a) { x.wa[a] = lb + L_T + (gsel & 1) * RP + (i + 1) * 256 + (((2 * c.w + a) ^ swz((i + 1) & 15)) << 4) + (gsel >> 1) * 8; PIN(x.wa[a]); }
  x.rh = lb + L_RAW + (p >> 1) * RS + (4 * (4 * c.w + gsel) + qp) * 64 + 8 + (p & 1) * 40; PIN(x.rh);
  const int side = qp & 1;
  x.wh = lb + L_T + (qp >> 1) * RP + side * 17 * 256 + (((2 * c.w + (gsel >> 1)) ^ side) << 4) + (gsel & 1) * 8; PIN(x.wh);
  const bool colok = side ? ((c.tx != 7) & (c.tx != 3)) : ((c.tx != 0) & (c.tx != 4));
  x.huse = (p == (side ? 0 : 3)) & colok; x.odd = gsel & 1; x.hrow = qp >> 1;
}
template <int RPAIR, int EXTRA>
DI void xraw_step(const XT& x, bool rowok, bool hok) {
  u32x2 v[3];
  constexpr int RO = (RPAIR & 1) * 2 * RS;
  const unsigned ra2 = x.ra + (RPAIR >> 1) * 4 * RS, rh2 = x.rh + (RPAIR >> 1) * 4 * RS;
  asm volatile("s_waitcnt vmcnt(%5)\n\t"
               "ds_read_b64_tr_b16 %0, %3 offset:%6\n\tds_read_b64_tr_b16 %1, %3 offset:%7\n\tds_read_b64_tr_b16 %2, %4 offset:%6\n\ts_waitcnt lgkmcnt(0)"
               : "=&v"(v[0]), "=&v"(v[1]), "=&v"(v[2])
               : "v"(ra2), "v"(rh2), "i"(8 - 2 * RPAIR + EXTRA), "i"(RO), "i"(RO + 512) : "memory");
  if (rowok) {
#pragma unroll
    for (int a = 0; a < 2; ++a) asm volatile("ds_write_b64 %0, %1 offset:%2" :: "v"(x.wa[a]), "v"(v[a]), "i"(RPAIR * 2 * RP) : "memory");
  }
  if (hok) asm volatile("ds_write_b64 %0, %1 offset:%2" :: "v"(x.wh), "v"(v[2]), "i"(RPAIR * 2 * RP) : "memory");
}
template <int EXTRA>
DI void xraw_transpose(const Ctx& c, const XT& x) {
  const bool top = c.ty == 0, bot = c.ty == 15;
  xraw_step<0, EXTRA>(x, !(top & !x.odd), x.huse & !(top & !x.hrow));
  xraw_step<1, EXTRA>(x, true, x.huse);
  xraw_step<2, EXTRA>(x, true, x.huse);
  xraw_step<3, EXTRA>(x, true, x.huse);
  xraw_step<4, EXTRA>(x, !(bot & x.odd), x.huse & !(bot & x.hrow));
}
struct XRow { u32x4 r[4]; };
DI void xrow_issue(const Ctx& c, XRow& xr, int yrow) {
  const unsigned o = (unsigned)(((c.b * 128 + 32 * (c.w & 3) + 16 * (c.w >> 2) + 4 * (c.lane >> 4)) * 128 + yrow) * 128) * 2 + (c.lane & 15) * 16;
#pragma unroll
  for (int jj = 0; jj < 4; ++jj) xr.r[jj] = bld<AUX_ROW>(c.rx, o, jj * 32768);
}
DI f32x2 xrow_stats(const XRow& xr) {
  float s = 0.f, s1 = 0.f, qs = 0.f, q1 = 0.f;
#pragma unroll
  for (int jj = 0; jj < 4; jj += 2)
#pragma unroll
    for (int k = 0; k < 4; ++k) { s = dot2(xr.r[jj][k], 0x3f803f80u, s); qs = dot2(xr.r[jj][k], xr.r[jj][k], qs); s1 = dot2(xr.r[jj + 1][k], 0x3f803f80u, s1); q1 = dot2(xr.r[jj + 1][k], xr.r[jj + 1][k], q1); }
  return f32x2{wave_sum(s + s1), wave_sum(qs + q1)};
}
DI void xrow_cross(const Ctx& c, const XRow& xr, int yrow) {
  const int k16 = c.lane & 15;
  if (k16 == 7 || k16 == 8) {
    unsigned v[2];
#pragma unroll
    for (int m = 0; m < 2; ++m) {
      const unsigned a = __builtin_amdgcn_perm(xr.r[2 * m + 1][3], xr.r[2 * m][3], 0x07060302u), b2 = __builtin_amdgcn_perm(xr.r[2 * m + 1][0], xr.r[2 * m][0], 0x05040100u);
      v[m] = k16 == 7 ? a : b2;
    }
    bst_sc1(u32x4{v[0], c.tag, v[1], c.tag}, c.rw, c.wb + WB_XC + (unsigned)((yrow * 2 + (k16 - 7)) * 64 + 16 * (c.w & 3) + 8 * (c.w >> 2) + 2 * (c.lane >> 4)) * 8, 0);
  }
}
struct Halo { unsigned go[4], lo[4]; bool inr[4], valid[4]; };
DI void halo_init(const Ctx& c, Halo& h) {
  const int tid = c.t - 256, cg = tid & 15, i = tid >> 4;
#pragma unroll
  for (int r = 0; r < 4; ++r) {
    const int row = r == 0 ? 0 : r == 1 ? 9 : r == 2 ? 1 + (i & 7) : (i >> 1) * 9;
    const int col = r < 2 ? i : r == 2 ? (i >> 3) * 17 : 16 + (i & 1);
    h.inr[r] = r < 3 ? true : i < 4;
    h.valid[r] = h.inr[r] & in_image(c, row, col);
    h.go[r] = WS_HB + (unsigned)(((c.b * 128 + c.y0 - 1 + row) * 128 + c.x0 - 1 + col) * 256 + cg * 16); PIN(h.go[r]);
    h.lo[r] = L_T + row * RP + col * 256 + ((cg ^ swz(col & 15)) << 4); PIN(h.lo[r]);
  }
}
struct Clr { unsigned o[3]; };
DI void clear_init(const Ctx& c, Clr& cl) {
  const int tid = c.t - 256, cg = tid & 15, i = tid >> 4, j = i < 12 ? i : 11;
#pragma unroll
  for (int r = 0; r < 3; ++r) {
    const int ry = r == 0 ? 0 : r == 1 ? 7 : 1 + (j >> 1), rx = r < 2 ? i : (j & 1) * 15;
    cl.o[r] = WS_HB + (unsigned)(((c.b * 128 + c.y0 + ry) * 128 + c.x0 + rx) * 256 + cg * 16); PIN(cl.o[r]);
  }
}
DI void stats_to_lds(const Ctx& c, int g, const u32x4& v0, const u32x4& v1) {
  const float s = wave_sum(__uint_as_float(v0[0]) + __uint_as_float(v1[0])), q = wave_sum(__uint_as_float(v0[2]) + __uint_as_float(v1[2]));
  const float mean = s * (1.f / 524288.f); float var = q * (1.f / 524288.f) - mean * mean; var = var < 0.f ? 0.f : var;
  if (c.lane == 0) *(f32x2*)(c.smem + L_STAT + g * 8) = f32x2{mean, __builtin_amdgcn_rsqf(var + 1e-5f)};
}

template <int CONV>
DI void mainloop(const Ctx& c, f32x4 (&acc)[8][2], bf16x8 (&wr)[WD], unsigned wvoff, const f32x4 (&ini)[2], unsigned baseP, unsigned baseD) {
  const char* T = c.smem + L_T;
  auto rd = [&](int B, int r) -> bf16x8 {
    const int sp = B / 3, ty = B % 3;
    const unsigned a = ty == 2 ? (baseD ^ (sp << 6)) : (baseP ^ ((2 * sp + ty) << 5));
    return *(const bf16x8*)(T + a + r * RP);
  };
  bf16x8 xf[4];
#pragma unroll
  for (int r = 0; r < 3; ++r) xf[r] = rd(0, r);
#pragma unroll
  for (int B = 0; B < 12; ++B) {
#pragma unroll
    for (int r = 0; r < 10; ++r) {
      const int G = B * 10 + r;
      if (r == 7 && (B % 3) == 2 && B < 11) { bar_nowait(); SB(); }
      { const int Gn = G + 3; if (Gn < 120) xf[Gn & 3] = rd(Gn / 10, Gn % 10); }
#pragma unroll
      for (int dy = 0; dy < 3; ++dy) {
        const int row = r - dy;
        if (row >= 0 && row < 8) {
#pragma unroll
          for (int nt = 0; nt < 2; ++nt) {
            const int ts = 6 * B + 2 * dy + nt;
            if (CONV == 1) asm volatile("v_mfma_f32_16x16x32_bf16 %0, %1, %2, %0" : "+v"(acc[row][nt]) : "v"(xf[G & 3]), "v"(wr[ts % WD]));
            else if (B == 0 && dy == 0) asm volatile("v_mfma_f32_16x16x32_bf16 %0, %1, %2, %3" : "=&v"(acc[row][nt]) : "v"(wr[ts % WD]), "v"(xf[G & 3]), "v"(ini[nt]));
            else           asm volatile("v_mfma_f32_16x16x32_bf16 %0, %1, %2, %0" : "+v"(acc[row][nt]) : "v"(wr[ts % WD]), "v"(xf[G & 3]));
            if (row == 7) { const int tn = CONV * 72 + ts + WD; if (tn < 144) wr[ts % WD] = __builtin_bit_cast(bf16x8, bld<SC1>(c.rw, wvoff, (unsigned)tn * 1024 + (tn >= 72 ? 294912u - 72u * 1024u : 0u))); }
            SB();
          }
        }
      }
    }
  }
  asm volatile("s_nop 15" ::: "memory");
  SB();
}
DI u32x4 pack_row(const f32x4 (&a)[2]) {
  const unsigned d0 = cvtpk(a[0][0], a[0][1]), d1 = cvtpk(a[0][2], a[0][3]), d2 = cvtpk(a[1][0], a[1][1]), d3 = cvtpk(a[1][2], a[1][3]);
  auto p = __builtin_amdgcn_permlane16_swap(d0, d2, false, false);
  auto q = __builtin_amdgcn_permlane16_swap(d1, d3, false, false);
  return u32x4{p[0], q[0], p[1], q[1]};
}

__global__ __launch_bounds__(512, 2) void k_resblock(const u16* __restrict__ x, const u16* __restrict__ t_emb, const u16* __restrict__ g1, const u16* __restrict__ be1,
                                                     const u16* __restrict__ w1, const u16* __restrict__ w_emb, const u16* __restrict__ g2, const u16* __restrict__ be2,
                                                     const u16* __restrict__ w2, u16* __restrict__ out, char* ws) {
  extern __shared__ __attribute__((aligned(256))) char smem[];
  Ctx c;
  c.smem = smem; c.ws = ws;
  c.t = threadIdx.x; c.lane = c.t & 63; c.w = __builtin_amdgcn_readfirstlane(c.t >> 6);
  const int xcd = blockIdx.x & 7, jx = blockIdx.x >> 3;
  c.b = xcd >> 2; c.ty = (xcd & 3) * 4 + (jx >> 3); c.tx = jx & 7;
  c.jb = c.ty * 8 + c.tx; c.y0 = c.ty * 8; c.x0 = c.tx * 16;
  c.rx = mk_rsrc(x, 8u << 20); c.rw = mk_rsrc(ws, 16u << 20);
  c.wb = (unsigned)c.b * WB_SIZE;

  if (c.w < 4) {
    STAMP_DECL STAMP(0);
    const int yrow = (xcd & 3) * 32 + jx;
    const unsigned epoch = __hip_atomic_load((GAS unsigned*)(ws + c.wb + WB_EPOCH), __ATOMIC_RELAXED, __HIP_MEMORY_SCOPE_AGENT);
    XRow xr; xrow_issue(c, xr, yrow);
    const int ch = c.t & 127;
    const unsigned gbA = (c.t < 128 ? g1 : be1)[ch], gbB = (c.t < 128 ? g2 : be2)[ch];
    const int px = c.lane & 15, q = c.lane >> 4;
    const rsrc_t ro = mk_rsrc(out, 8u << 20);
    SB();
    unsigned oo = (unsigned)(((c.b * 128 + 32 * c.w + 16 * (q & 1) + px) * 128 + c.y0) * 128 + c.x0 + 8 * (q >> 1)) * 2; PIN(oo);
    XT xt; xraw_init(c, xt);
    Task0 t0; task0_init(c, t0);
    unsigned baseP, baseD;
    { const int colP = px + (q & 1), colD = px + 2;
      baseP = colP * 256 + (((q >> 1) ^ swz(colP & 15)) << 4); baseD = colD * 256 + ((q ^ swz(colD & 15)) << 4); PIN(baseP); PIN(baseD); }
    unsigned wvoff = c.wb + WB_WF + (unsigned)c.w * 73728u + (unsigned)c.lane * 16u; PIN(wvoff);
    unsigned fo = c.wb + WB_FLAG + (unsigned)(c.w * 128 + (c.lane & 7) * 4); PIN(fo);
    SB();
    STAMP(2);
    c.tag = __builtin_amdgcn_readfirstlane(epoch) + 1u;
    { const f32x2 p = xrow_stats(xr); if (c.lane == 0) *(f32x2*)(smem + L_RED + c.w * 8) = p; }
    bar();
    STAMP(14);
    xrow_cross(c, xr, yrow);
    { float* gbp = (float*)(smem + L_GB) + (c.t < 128 ? 0 : 128) + ch; gbp[0] = bflo(gbA) * LOG2E; gbp[256] = bflo(gbB) * LOG2E; }
    SB();
    xraw_issue(c, xt.vo); SB();
    unsigned fl = __builtin_amdgcn_raw_buffer_load_b32(c.rw, fo, 0, SC1); SB();
    STAMP(15);
    xraw_transpose<1>(c, xt); SB();
    STAMP(3);
    for (int spin = 0; spin < SPIN_MAX; ++spin) {
      if (!__any(fl != c.tag)) break;
      __builtin_amdgcn_s_sleep(POLL_SLEEP);
      cbar();
      fl = __builtin_amdgcn_raw_buffer_load_b32(c.rw, fo, 0, SC1);
    }
    STAMP(4);
    bf16x8 wr[WD];
#pragma unroll
    for (int k = 0; k < WD; ++k) wr[k] = __builtin_bit_cast(bf16x8, bld<SC1>(c.rw, wvoff, (unsigned)k * 1024));
    bar();
    STAMP(5);
    norm_group0(c, t0, 0);
    bar();
    STAMP(6);
    f32x4 acc[8][2], ini[2];
    ini[0] = *(const f32x4*)(smem + L_EMB + (32 * c.w + 4 * q) * 4); ini[1] = *(const f32x4*)(smem + L_EMB + (32 * c.w + 16 + 4 * q) * 4);
    __builtin_amdgcn_s_setprio(1);
    mainloop<0>(c, acc, wr, wvoff, ini, baseP, baseD);
    __builtin_amdgcn_s_setprio(0);
    STAMP(7);
    {
      f32x2 s2 = {0.f, 0.f}, q2 = {0.f, 0.f};
#pragma unroll
      for (int r = 0; r < 8; ++r)
#pragma unroll
        for (int nt = 0; nt < 2; ++nt)
#pragma unroll
          for (int k = 0; k < 2; ++k) { const f32x2 v = {acc[r][nt][2 * k], acc[r][nt][2 * k + 1]}; s2 += v; q2 += v * v; }
      const float s = wave_sum(s2[0] + s2[1]), qs = wave_sum(q2[0] + q2[1]);
      if (c.lane == 0) bst_sc1(u32x4{__float_as_uint(s), c.tag, __float_as_uint(qs), c.tag}, c.rw, c.wb + WB_G2 + (unsigned)(c.w * 128 + c.jb) * 16, 0);
    }
    STAMP(1);
    {
      const int chk = 4 * c.w + (((q & 1) << 1) | (q >> 1));
      const unsigned go = WS_HB + (unsigned)(((c.b * 128 + c.y0) * 128 + c.x0 + px) * 256 + chk * 16);
      u32x4 hv[8];
#pragma unroll
      for (int r = 0; r < 8; ++r) hv[r] = pack_row(acc[r]);
      auto san = [](u32x4 v) { return __builtin_elementwise_min(v, u32x4{0xfffffffeu, 0xfffffffeu, 0xfffffffeu, 0xfffffffeu}); };
      bst_sc1(san(hv[0]), c.rw, go, 0); bst_sc1(san(hv[7]), c.rw, go, 7 * 32768);
      if (px == 0 || px == 15) {
#pragma unroll
        for (int r = 1; r < 7; ++r) bst_sc1(san(hv[r]), c.rw, go, r * 32768);
      }
      bar_nowait();
      char* lp = smem + L_T + RP + (px + 1) * 256 + ((chk ^ swz((px + 1) & 15)) << 4);
#pragma unroll
      for (int r = 0; r < 8; ++r) *(u32x4*)(lp + r * RP) = hv[r];
    }
    STAMP(8);
#pragma unroll
    for (int r = 0; r < 8; ++r) {
      const u32x4 v = *(const u32x4*)(smem + L_RAW + (r + 1) * RS + (32 * c.w + 16 * (q & 1) + px) * 64 + 16 + (q >> 1) * 16);
      auto a = __builtin_amdgcn_permlane16_swap(v[0], v[2], false, false);
      auto e = __builtin_amdgcn_permlane16_swap(v[1], v[3], false, false);
      acc[r][0] = f32x4{bflo(a[0]), bfhi(a[0]), bflo(e[0]), bfhi(e[0])};
      acc[r][1] = f32x4{bflo(a[1]), bfhi(a[1]), bflo(e[1]), bfhi(e[1])};
      asm volatile("" : "+v"(acc[r][0]), "+v"(acc[r][1]));
    }
    STAMP(9);
    bar();
    STAMP(10);
    norm_group0(c, t0, 1);
    bar();
    STAMP(11);
    __builtin_amdgcn_s_setprio(1);
    mainloop<1>(c, acc, wr, wvoff, ini, baseP, baseD);
    __builtin_amdgcn_s_setprio(0);
    STAMP(12);
#pragma unroll
    for (int r = 0; r < 8; ++r) __builtin_amdgcn_raw_buffer_store_b128(pack_row(acc[r]), ro, oo + r * 256, 0, 0);
    STAMP(13); STAMP_FLUSH;
  } else {
    STAMP_DECL STAMP(0);
    const int g = c.w - 4;
    const int yrow = (xcd & 3) * 32 + jx;
    const int ksvc = jx * 4 + (xcd & 3);
    const bool svc_w = c.w == 7 && (ksvc < 32 || (ksvc >= 64 && ksvc < 96)), svc_e = c.w == 7 && ksvc >= 32 && ksvc < 64;
    const int wu = ksvc < 32 ? ksvc : ksvc - 32;
    const unsigned epoch = __hip_atomic_load((GAS unsigned*)(ws + c.wb + WB_EPOCH), __ATOMIC_RELAXED, __HIP_MEMORY_SCOPE_AGENT);
    XRow xr; xrow_issue(c, xr, yrow);
    SB();
    u32x4 sv[11]; unsigned te[6];
    if (svc_w) {
      const int r3 = (c.lane * 57) >> 10, pc = c.lane - 18 * r3;
      const rsrc_t rws = mk_rsrc((wu >> 5) ? w2 : w1, 294912u);
      const unsigned o = (unsigned)(((32 * ((wu >> 3) & 3) + r3) * 128 + 16 * (wu & 7)) * 18 + pc * 16);
#pragma unroll
      for (int k = 0; k < 11; ++k) sv[k] = bld<AUX_SVC>(rws, o, k * 3 * 2304);
    } else if (svc_e) {
      const int j = ksvc - 32, e0 = 192 * (j >> 3) + (c.lane >> 1);
      const rsrc_t re = mk_rsrc(w_emb, 196608u), rt = mk_rsrc(t_emb, 3072u);
#pragma unroll
      for (int k = 0; k < 6; ++k) { sv[k] = bld<AUX_SVC>(re, (unsigned)(e0 * 256 + (16 * (j & 7) + 8 * (c.lane & 1)) * 2), k * 32 * 256);
                                    te[k] = __builtin_amdgcn_raw_buffer_load_b16(rt, (unsigned)(c.b * 768 + e0) * 2, k * 64, 0); }
    }
    SB();
    XT xt; xraw_init(c, xt);
    Task0 t0; task0_init(c, t0);
    HTask ht; htask_init(c, ht);
    if ((c.ty == 0) | (c.ty == 15) | (c.tx == 0) | (c.tx == 7)) {
      Halo hz; halo_init(c, hz);
#pragma unroll
      for (int r = 0; r < 4; ++r) if (hz.inr[r] & !hz.valid[r]) *(u32x4*)(smem + hz.lo[r]) = u32x4{0, 0, 0, 0};
    }
    SB();
    STAMP(4);
    c.tag = __builtin_amdgcn_readfirstlane(epoch) + 1u;
    STAMP(3);
    const f32x2 psum = xrow_stats(xr);
    asm volatile("" :: "v"(psum));
    STAMP(14);
    bar();
    { const f32x2 p = psum + *(const f32x2*)(smem + L_RED + g * 8);
      if (c.lane == 0) bst_sc1(u32x4{__float_as_uint(p[0]), c.tag, __float_as_uint(p[1]), c.tag}, c.rw, c.wb + WB_G1 + (unsigned)(g * 128 + c.jb) * 16, 0); }
    SB();
    STAMP(1);
    xrow_cross(c, xr, yrow);
    if (svc_w) {
      if (c.lane < 54) {
#pragma unroll
        for (int k = 0; k < 11; ++k) *(u32x4*)(smem + L_SVC + k * 864 + c.lane * 16) = sv[k];
      }
#pragma unroll
      for (int k = 0; k < 9; ++k) { sv[k] = *(const u32x4*)(smem + L_SVC + (c.lane & 31) * 288 + (c.lane >> 5) * 144 + k * 16); asm volatile("" : "+v"(sv[k])); }
    }
    SB();
    xraw_issue(c, xt.vo); SB();
    STAMP(5);
    if (svc_w) {
      const int cs = wu & 7, o = c.lane & 31, h = c.lane >> 5;
      const unsigned dst = c.wb + WB_WF + (unsigned)(wu >> 5) * 294912u + (unsigned)((wu >> 3) & 3) * 73728u + (unsigned)(o >> 4) * 1024u + (unsigned)(o & 15) * 16u;
      const unsigned dP = dst + (unsigned)(3 * (cs >> 1) + (cs & 1)) * 6144u + (unsigned)h * 512u;
      const unsigned dD = dst + (unsigned)(3 * (cs >> 1) + 2) * 6144u + (unsigned)(2 * (cs & 1) + h) * 256u;
#pragma unroll
      for (int k = 0; k < 9; ++k) {
        const int dy = k / 3, dx = k % 3;
        u32x4 o4;
#pragma unroll
        for (int m = 0; m < 4; ++m) {
          const int e0 = 18 * m + k, e1 = e0 + 9;
          const unsigned sel = ((e0 & 1) ? 0x0302u : 0x0100u) | ((e1 & 1) ? 0x07060000u : 0x05040000u);
          o4[m] = __builtin_amdgcn_perm(sv[e1 >> 3][(e1 >> 1) & 3], sv[e0 >> 3][(e0 >> 1) & 3], sel);
        }
        bst_sc1(o4, c.rw, dx < 2 ? dP : dD, dy * 2048 + (dx < 2 ? dx * 256 : 0));
      }
      SB();
      xraw_transpose<9>(c, xt);
      asm volatile("s_waitcnt vmcnt(0)" ::: "memory");
      if (c.lane == 0) __hip_atomic_store((GAS unsigned*)(ws + c.wb + WB_FLAG + (wu >> 3) * 128 + (wu & 7) * 4), c.tag, __ATOMIC_RELAXED, __HIP_MEMORY_SCOPE_AGENT);
    } else if (svc_e) {
      f32x2 a[4];
#pragma unroll
      for (int m = 0; m < 4; ++m) a[m] = f32x2{0.f, 0.f};
#pragma unroll
      for (int k = 0; k < 6; ++k) {
        unsigned tk = te[k]; PIN(tk);
        const float tv = bflo(tk);
        const float sl = tv * __builtin_amdgcn_rcpf(1.f + __builtin_amdgcn_exp2f(-tv * LOG2E));
#pragma unroll
        for (int m = 0; m < 4; ++m) a[m] += f32x2{bflo(sv[k][m]), bfhi(sv[k][m])} * sl;
      }
      float r[8];
#pragma unroll
      for (int m = 0; m < 8; ++m) r[m] = rows_sum(dppadd<0x128>(dppadd<0x124>(dppadd<0x4E>(a[m >> 1][m & 1]))));
      const int j = ksvc - 32;
      if (c.lane < 2) {
        const unsigned o = c.wb + WB_EG + (unsigned)((j >> 3) * 128 + 16 * (j & 7) + 8 * c.lane) * 8;
#pragma unroll
        for (int m = 0; m < 4; ++m) bst_sc1(u32x4{__float_as_uint(r[2 * m]), c.tag, __float_as_uint(r[2 * m + 1]), c.tag}, c.rw, o, m * 16);
      }
      SB();
      xraw_transpose<4>(c, xt);
    } else xraw_transpose<0>(c, xt);
    SB();
    STAMP(2);
    {
      u32x4 v0, v1, vc[2]; bool n0 = true, n1 = true, nc[2];
      const unsigned o = c.wb + WB_G1 + (unsigned)(g * 128 + c.lane) * 16;
      const bool xc = c.tx == 3 || c.tx == 4;
      unsigned co[2], cl2[2]; bool cin[2];
#pragma unroll
      for (int r = 0; r < 2; ++r) {
        const int id = c.t - 256 + 256 * r, row = id >> 5, i = id & 31, y = c.y0 - 1 + row, side = c.tx == 3 ? 1 : 0;
        cin[r] = xc & (id < 320);
        nc[r] = cin[r] & ((unsigned)y < 128u);
        co[r] = c.wb + WB_XC + (unsigned)((y * 2 + side) * 64 + 2 * i) * 8;
        cl2[r] = L_T + row * RP + side * 17 * 256 + (((i >> 1) ^ side) << 4) + (i & 1) * 8;
        vc[r] = u32x4{0, 0, 0, 0};
      }
      for (int spin = 0; spin < SPIN_MAX; ++spin) {
        cbar();
        if (n0) v0 = bld<SC1>(c.rw, o, 0);
        if (n1) v1 = bld<SC1>(c.rw, o, 1024);
        if (xc) {
#pragma unroll
          for (int r = 0; r < 2; ++r) if (nc[r]) vc[r] = bld<SC1>(c.rw, co[r], 0);
        }
        n0 = !((v0[1] == c.tag) & (v0[3] == c.tag)); n1 = !((v1[1] == c.tag) & (v1[3] == c.tag));
#pragma unroll
        for (int r = 0; r < 2; ++r) nc[r] = nc[r] & !((vc[r][1] == c.tag) & (vc[r][3] == c.tag));
        if (!__any(n0 | n1 | nc[0] | nc[1])) break;
        __builtin_amdgcn_s_sleep(POLL_SLEEP);
      }
      stats_to_lds(c, g, v0, v1);
      if (xc) {
#pragma unroll
        for (int r = 0; r < 2; ++r) if (cin[r]) *(u32x2*)(smem + cl2[r]) = u32x2{vc[r][0], vc[r][2]};
      }
    }
    u32x4 ve[4];
    if (c.w == 4) {
#pragma unroll
      for (int k = 0; k < 4; ++k) ve[k] = bld<SC1>(c.rw, c.wb + WB_EG + (unsigned)c.lane * 16, k * 1024);
    }
    STAMP(6);
    bar();
    if (c.t == 256 && c.jb == 0) __hip_atomic_store((GAS unsigned*)(ws + c.wb + WB_EPOCH), c.tag, __ATOMIC_RELAXED, __HIP_MEMORY_SCOPE_AGENT);
    norm_group0(c, t0, 0);
    if (c.w == 4) {
      for (int spin = 0; spin < SPIN_MAX; ++spin) {
        bool ok = true;
#pragma unroll
        for (int k = 0; k < 4; ++k) ok = ok & (ve[k][1] == c.tag) & (ve[k][3] == c.tag);
        if (!__any(!ok)) break;
        cbar();
#pragma unroll
        for (int k = 0; k < 4; ++k) ve[k] = bld<SC1>(c.rw, c.wb + WB_EG + (unsigned)c.lane * 16, k * 1024);
      }
      *(f32x2*)(smem + L_EMB + c.lane * 8) = f32x2{(__uint_as_float(ve[0][0]) + __uint_as_float(ve[1][0])) + (__uint_as_float(ve[2][0]) + __uint_as_float(ve[3][0])),
                                                  (__uint_as_float(ve[0][2]) + __uint_as_float(ve[1][2])) + (__uint_as_float(ve[2][2]) + __uint_as_float(ve[3][2]))};
    }
    STAMP(15);
    bar();
#pragma unroll 1
    for (int k = 1; k < 4; ++k) {
      if (k == 2) {
        Clr cl; clear_init(c, cl);
#pragma unroll
        for (int r = 0; r < 3; ++r) bst_sc1(u32x4{~0u, ~0u, ~0u, ~0u}, c.rw, cl.o[r], 0);
      }
      norm_group_h(c, ht, k);
      if (k == 2) asm volatile("s_waitcnt vmcnt(0)" ::: "memory");
      if (k == 3 && c.w == 6) {
        const unsigned fo = c.wb + WB_FLAG + (unsigned)((4 + ((c.lane >> 3) & 3)) * 128 + (c.lane & 7) * 4);
        for (int spin = 0; spin < SPIN_MAX; ++spin) {
          cbar();
          const unsigned f = __builtin_amdgcn_raw_buffer_load_b32(c.rw, fo, 0, SC1);
          if (!__any(f != c.tag)) break;
        }
      }
      bar();
      if (k == 2 && c.t == 256) __hip_atomic_store((GAS unsigned*)(ws + c.wb + WB_CF + c.jb * 4), c.tag, __ATOMIC_RELAXED, __HIP_MEMORY_SCOPE_AGENT);
    }
    STAMP(7);
    {
      Halo hl; halo_init(c, hl);
      if (c.w == 5) {
        const int l9 = c.lane < 9 ? c.lane : 4, dy = ((l9 * 11) >> 5) - 1, dx = l9 - 3 * (dy + 1) - 1;
        const int ny = c.ty + dy, nx = c.tx + dx;
        const bool chk = ((unsigned)ny < 16u) & ((unsigned)nx < 8u);
        const unsigned fo = c.wb + WB_CF + (unsigned)(chk ? ny * 8 + nx : c.jb) * 4;
        for (int spin = 0; spin < SPIN_MAX; ++spin) {
          cbar();
          const unsigned f = __builtin_amdgcn_raw_buffer_load_b32(c.rw, fo, 0, SC1);
          if (!__any(f != c.tag)) break;
          __builtin_amdgcn_s_sleep(2);
        }
      }
      u32x4 hv[4]; bool need[4];
#pragma unroll
      for (int r = 0; r < 4; ++r) { need[r] = hl.valid[r]; hv[r] = u32x4{0, 0, 0, 0}; }
      u32x4 v0, v1; bool n0 = true, n1 = true;
      const unsigned o = c.wb + WB_G2 + (unsigned)(g * 128 + c.lane) * 16;
      bar_nowait();
      STAMP(13);
      for (int spin = 0; spin < SPIN_MAX; ++spin) {
        cbar();
        if (n0) v0 = bld<SC1>(c.rw, o, 0);
        if (n1) v1 = bld<SC1>(c.rw, o, 1024);
#pragma unroll
        for (int r = 0; r < 4; ++r) if (need[r]) hv[r] = bld<SC1>(c.rw, hl.go[r], 0);
        bool any = false;
#pragma unroll
        for (int r = 0; r < 4; ++r) { need[r] = need[r] & ((hv[r][0] == ~0u) | (hv[r][1] == ~0u) | (hv[r][2] == ~0u) | (hv[r][3] == ~0u)); any |= need[r]; }
        n0 = !((v0[1] == c.tag) & (v0[3] == c.tag)); n1 = !((v1[1] == c.tag) & (v1[3] == c.tag));
        if (!__any(any | n0 | n1)) break;
        __builtin_amdgcn_s_sleep(POLL_SLEEP2);
      }
      STAMP(8);
#pragma unroll
      for (int r = 0; r < 4; ++r) if (hl.valid[r]) *(u32x4*)(smem + hl.lo[r]) = hv[r];
      stats_to_lds(c, g, v0, v1);
    }
    bar();
    norm_group0(c, t0, 1);
    bar();
#pragma unroll 1
    for (int k = 1; k < 4; ++k) { if (k == 1) STAMP(9); norm_group_h(c, ht, k); if (k == 1) STAMP(10); if (k == 2) STAMP(11); bar(); }
    STAMP(12); STAMP_FLUSH;
  }
}

extern "C" void kernel_launch(void* const* d_in, const int* in_sizes, int n_in, void* d_out, int out_size, void* d_ws, size_t ws_size, hipStream_t stream) {
  (void)hipFuncSetAttribute((const void*)k_resblock, hipFuncAttributeMaxDynamicSharedMemorySize, L_END);
  k_resblock<<<256, 512, L_END, stream>>>((const u16*)d_in[0], (const u16*)d_in[1], (const u16*)d_in[2], (const u16*)d_in[3], (const u16*)d_in[4],
                                          (const u16*)d_in[5], (const u16*)d_in[6], (const u16*)d_in[7], (const u16*)d_in[8], (u16*)d_out, (char*)d_ws);
}
```

```cpp
#define DIAG_WG 100
#define PROBE 0
#define PSCALE 0
#include <hip/hip_runtime.h>
#include <hip/hip_bf16.h>
#include <stdint.h>

typedef float f32x4 __attribute__((ext_vector_type(4)));
typedef float f32x2 __attribute__((ext_vector_type(2)));
typedef __bf16 bf16x8 __attribute__((ext_vector_type(8)));
typedef __bf16 bf16x2 __attribute__((ext_vector_type(2)));
typedef unsigned u32x4 __attribute__((ext_vector_type(4)));
typedef unsigned u32x2 __attribute__((ext_vector_type(2)));
typedef unsigned short u16;
#define GAS __attribute__((address_space(1)))
#define LAS __attribute__((address_space(3)))
#define DI __device__ __forceinline__
#define SB() __builtin_amdgcn_sched_barrier(0)
#define PIN(x) asm volatile("" : "+v"(x))

constexpr int RP = 4608;
constexpr int TILE_BYTES = 10 * RP;
constexpr int RS = 8224;
constexpr int WD = 18;
constexpr float LOG2E = 1.4426950408889634f;
constexpr int SC1 = 16, NT = 2;
constexpr int SPIN_MAX = 1 << 16;
#define POLL_SLEEP 4
#define POLL_SLEEP2 2
#define AUX_SVC 0
#define AUX_ROW 2

constexpr unsigned WB_SIZE = 1u << 20;
constexpr unsigned WB_WF = 0, WB_EPOCH = 589824, WB_FLAG = WB_EPOCH + 256  , WB_G1 = WB_FLAG + 1024, WB_G2 = WB_G1 + 8192,
                   WB_EG = WB_G2 + 8192  , WB_CF = WB_EG + 4096  , WB_XC = WB_CF + 512  ;
constexpr unsigned WS_HB = 2u << 20;
constexpr int L_T = 0, L_RAW = TILE_BYTES, L_GB = L_RAW + 10 * RS  , L_PAR = L_GB + 2048  , L_EMB = L_PAR + 1024,
              L_STAT = L_EMB + 512  , L_RED = L_STAT + 32  , L_DUMMY = (L_RED + 32 + 255) / 256 * 256  , L_SVC = L_DUMMY + 1024  ,
              L_END = L_SVC + 9504;

DI int swz(int c) { return c ^ ((c >> 1) & 4); }
DI unsigned cvtpk(float lo, float hi) { f32x2 v = {lo, hi}; bf16x2 b = __builtin_convertvector(v, bf16x2); return __builtin_bit_cast(unsigned, b); }
DI unsigned cvtpk_s(float lo, float hi) { unsigned r; asm("v_cvt_pk_bf16_f32 %0, %1, %2" : "=v"(r) : "v"(lo), "v"(hi)); return r; }
DI float bflo(unsigned d) { return __uint_as_float(d << 16); }
DI float bfhi(unsigned d) { return __uint_as_float(d & 0xffff0000u); }
DI float dot2(unsigned a, unsigned b, float c) { return __builtin_amdgcn_fdot2_f32_bf16(__builtin_bit_cast(bf16x2, a), __builtin_bit_cast(bf16x2, b), c, false); }
template <int CTRL> DI float dppadd(float v) { return v + __int_as_float(__builtin_amdgcn_update_dpp(0, __float_as_int(v), CTRL, 0xf, 0xf, true)); }
DI float rows_sum(float v) {
  auto p = __builtin_amdgcn_permlane16_swap(__float_as_uint(v), __float_as_uint(v), false, false);
  v = __uint_as_float(p[0]) + __uint_as_float(p[1]);
  auto q = __builtin_amdgcn_permlane32_swap(__float_as_uint(v), __float_as_uint(v), false, false);
  return __uint_as_float(q[0]) + __uint_as_float(q[1]);
}
DI float wave_sum(float v) { v = dppadd<0xB1>(v); v = dppadd<0x4E>(v); v = dppadd<0x141>(v); v = dppadd<0x140>(v); return rows_sum(v); }
DI void bar() { asm volatile("s_waitcnt lgkmcnt(0)\n\ts_barrier" ::: "memory"); }
DI void bar_nowait() { asm volatile("s_barrier" ::: "memory"); }
DI void cbar() { asm volatile("" ::: "memory"); }
typedef __amdgpu_buffer_rsrc_t rsrc_t;
DI rsrc_t mk_rsrc(const void* p, unsigned bytes) { return __builtin_amdgcn_make_buffer_rsrc((void*)p, 0, bytes, 0x00020000); }
template <int AUX> DI u32x4 bld(rsrc_t r, unsigned voff, unsigned soff) { return __builtin_amdgcn_raw_buffer_load_b128(r, voff, soff, AUX); }
DI void bst_sc1(u32x4 v, rsrc_t r, unsigned voff, unsigned off) { __builtin_amdgcn_raw_buffer_store_b128(v, r, voff + off, 0, SC1); }

struct Ctx {
  rsrc_t rx, rw;
  char* smem; char* ws;
  int b, ty, tx, jb, y0, x0, w, lane, t;
  unsigned wb;
  unsigned tag;
};

#define STAMP_DECL
#define STAMP(idx) do {} while (0)
#define STAMP_FLUSH do {} while (0)

DI u32x2 act4_packed(u32x2 v, f32x4 a, f32x4 b) {
  u32x2 o;
#pragma unroll
  for (int m = 0; m < 2; ++m) {
    f32x2 xx = {bflo(v[m]), bfhi(v[m])}, aa = {a[2 * m], a[2 * m + 1]}, bb = {b[2 * m], b[2 * m + 1]};
    f32x2 u = xx * aa + bb;
    f32x2 e = {__builtin_amdgcn_exp2f(-u[0]), __builtin_amdgcn_exp2f(-u[1])};
    f32x2 d = e * LOG2E + LOG2E;
    f32x2 r = {__builtin_amdgcn_rcpf(d[0]), __builtin_amdgcn_rcpf(d[1])};
    f32x2 y = u * r;
    o[m] = cvtpk(y[0], y[1]);
  }
  return o;
}
DI float act1(float x, float a, float b) { float u = __builtin_fmaf(x, a, b); float e = __builtin_amdgcn_exp2f(-u); return u * __builtin_amdgcn_rcpf(__builtin_fmaf(e, LOG2E, LOG2E)); }
DI u32x2 act4_scalar(u32x2 v, f32x4 a, f32x4 b) {
  u32x2 o;
  o[0] = cvtpk_s(act1(bflo(v[0]), a[0], b[0]), act1(bfhi(v[0]), a[1], b[1]));
  o[1] = cvtpk_s(act1(bflo(v[1]), a[2], b[2]), act1(bfhi(v[1]), a[3], b[3]));
  return o;
}
DI bool in_image(const Ctx& c, int row, int col) { return ((unsigned)(c.y0 - 1 + row) < 128u) & ((unsigned)(c.x0 - 1 + col) < 128u); }
struct Tasks { unsigned off[3]; };
DI void tasks_init(const Ctx& c, Tasks& tk, int idx, int c4) {
  const unsigned lo = (c4 & 1) * 8, dummy = L_DUMMY + c.lane * 16 + lo;
  const int col = idx & 15, row = idx >> 4;
  const unsigned o0 = L_T + row * RP + col * 256 + (((c4 >> 1) ^ swz(col)) << 4) + lo;
  const int j = idx - 32, row2 = idx < 32 ? 8 + row : j >> 1, col2 = idx < 32 ? col : 16 + (j & 1);
  const unsigned o2 = L_T + row2 * RP + col2 * 256 + (((c4 >> 1) ^ swz(col2 & 15)) << 4) + lo;
  tk.off[0] = o0; tk.off[1] = o0 + 4 * RP; tk.off[2] = idx < 52 ? o2 : dummy;
  if ((c.ty == 0) | (c.ty == 15) | (c.tx == 0) | (c.tx == 7)) {
    tk.off[0] = in_image(c, row, col) ? tk.off[0] : dummy;
    tk.off[1] = in_image(c, row + 4, col) ? tk.off[1] : dummy;
    tk.off[2] = in_image(c, row2, col2) ? tk.off[2] : dummy;
  }
#pragma unroll
  for (int r = 0; r < 3; ++r) PIN(tk.off[r]);
}
typedef Tasks Task0;
DI void task0_init(const Ctx& c, Task0& t0) { tasks_init(c, t0, c.t >> 3, c.t & 7); }
DI void norm_group0(const Ctx& c, const Task0& t0, int gbsel) {
  u32x2 v[3];
#pragma unroll
  for (int r = 0; r < 3; ++r) v[r] = *(const u32x2*)(c.smem + t0.off[r]);
  const int ch = 4 * (c.t & 7);
  const f32x4 g = *(const f32x4*)(c.smem + L_GB + gbsel * 1024 + ch * 4), be = *(const f32x4*)(c.smem + L_GB + gbsel * 1024 + 512 + ch * 4);
  const f32x2 st = *(const f32x2*)(c.smem + L_STAT);
  const f32x4 a = g * st[1], b = be - a * st[0];
#pragma unroll
  for (int r = 0; r < 2; ++r) *(u32x2*)(c.smem + t0.off[r]) = act4_packed(v[r], a, b);
  if (c.w < 7) *(u32x2*)(c.smem + t0.off[2]) = act4_packed(v[2], a, b);
  else if (c.lane < 48) {
    const int c2 = 32 + 2 * c.lane;
    const f32x2 g2 = *(const f32x2*)(c.smem + L_GB + gbsel * 1024 + c2 * 4), b2 = *(const f32x2*)(c.smem + L_GB + gbsel * 1024 + 512 + c2 * 4);
    const f32x2 s2 = *(const f32x2*)(c.smem + L_STAT + (c2 >> 5) * 8);
    const f32x2 aa = g2 * s2[1];
    *(f32x2*)(c.smem + L_PAR + c2 * 4) = aa; *(f32x2*)(c.smem + L_PAR + 512 + c2 * 4) = b2 - aa * s2[0];
  }
}
typedef Tasks HTask;
DI void htask_init(const Ctx& c, HTask& ht) { tasks_init(c, ht, (c.t - 256) >> 2, 2 * (c.t & 3)); }
DI void norm_group_h(const Ctx& c, const HTask& ht, int k) {
  const int ch = 32 * k + 8 * (c.t & 3);
  u32x4 v[3];
#pragma unroll
  for (int r = 0; r < 3; ++r) v[r] = *(const u32x4*)(c.smem + (ht.off[r] ^ (k << 6)));
  const f32x4 a0 = *(const f32x4*)(c.smem + L_PAR + ch * 4), a1 = *(const f32x4*)(c.smem + L_PAR + ch * 4 + 16);
  const f32x4 b0 = *(const f32x4*)(c.smem + L_PAR + 512 + ch * 4), b1 = *(const f32x4*)(c.smem + L_PAR + 512 + ch * 4 + 16);
#pragma unroll
  for (int r = 0; r < 3; ++r) {
    const u32x2 lo = act4_scalar(u32x2{v[r][0], v[r][1]}, a0, b0), hi = act4_scalar(u32x2{v[r][2], v[r][3]}, a1, b1);
    *(u32x4*)(c.smem + (ht.off[r] ^ (k << 6))) = u32x4{lo[0], lo[1], hi[0], hi[1]};
  }
}

DI void xraw_issue(const Ctx& c, unsigned vo) {
#pragma unroll
  for (int row = 0; row < 10; ++row) {
    const int y = row == 0 ? (c.ty == 0 ? 0 : c.y0 - 1) : row == 9 ? (c.ty == 15 ? 127 : c.y0 + 8) : c.y0 - 1 + row;
    __builtin_amdgcn_raw_ptr_buffer_load_lds(c.rx, (LAS void*)(c.smem + L_RAW + row * RS + c.w * 1024), 16, vo, y * 256, 0, 0);
  }
}
struct XT { unsigned vo, ra, rh, wa[2], wh; bool huse, odd, hrow; };
DI void xraw_init(const Ctx& c, XT& x) {
  const unsigned lb = (unsigned)(size_t)(LAS char*)c.smem;
  int xs = c.x0 - 8 + 8 * (c.lane & 3);
  { const int lo = c.x0 & 64; xs = xs < lo ? lo : (xs > lo + 56 ? lo + 56 : xs); }
  x.vo = (unsigned)(((c.b * 128 + 16 * c.w + (c.lane >> 2)) * 128) * 128 + xs) * 2; PIN(x.vo);
  const int gsel = c.lane >> 4, i = c.lane & 15, qp = i >> 2, p = i & 3;
  x.ra = lb + L_RAW + (gsel & 1) * RS + (4 * (4 * c.w + (gsel >> 1)) + qp) * 64 + 16 + p * 8; PIN(x.ra);
#pragma unroll
  for (int a = 0; a < 2; ++a) { x.wa[a] = lb + L_T + (gsel & 1) * RP + (i + 1) * 256 + (((2 * c.w + a) ^ swz((i + 1) & 15)) << 4) + (gsel >> 1) * 8; PIN(x.wa[a]); }
  x.rh = lb + L_RAW + (p >> 1) * RS + (4 * (4 * c.w + gsel) + qp) * 64 + 8 + (p & 1) * 40; PIN(x.rh);
  const int side = qp & 1;
  x.wh = lb + L_T + (qp >> 1) * RP + side * 17 * 256 + (((2 * c.w + (gsel >> 1)) ^ side) << 4) + (gsel & 1) * 8; PIN(x.wh);
  const bool colok = side ? ((c.tx != 7) & (c.tx != 3)) : ((c.tx != 0) & (c.tx != 4));
  x.huse = (p == (side ? 0 : 3)) & colok; x.odd = gsel & 1; x.hrow = qp >> 1;
}
template <int RPAIR, int EXTRA>
DI void xraw_step(const XT& x, bool rowok, bool hok) {
  u32x2 v[3];
  constexpr int RO = (RPAIR & 1) * 2 * RS;
  const unsigned ra2 = x.ra + (RPAIR >> 1) * 4 * RS, rh2 = x.rh + (RPAIR >> 1) * 4 * RS;
  asm volatile("s_waitcnt vmcnt(%5)\n\t"
               "ds_read_b64_tr_b16 %0, %3 offset:%6\n\tds_read_b64_tr_b16 %1, %3 offset:%7\n\tds_read_b64_tr_b16 %2, %4 offset:%6\n\ts_waitcnt lgkmcnt(0)"
               : "=&v"(v[0]), "=&v"(v[1]), "=&v"(v[2])
               : "v"(ra2), "v"(rh2), "i"(8 - 2 * RPAIR + EXTRA), "i"(RO), "i"(RO + 512) : "memory");
  if (rowok) {
#pragma unroll
    for (int a = 0; a < 2; ++a) asm volatile("ds_write_b64 %0, %1 offset:%2" :: "v"(x.wa[a]), "v"(v[a]), "i"(RPAIR * 2 * RP) : "memory");
  }
  if (hok) asm volatile("ds_write_b64 %0, %1 offset:%2" :: "v"(x.wh), "v"(v[2]), "i"(RPAIR * 2 * RP) : "memory");
}
template <int EXTRA>
DI void xraw_transpose(const Ctx& c, const XT& x) {
  const bool top = c.ty == 0, bot = c.ty == 15;
  xraw_step<0, EXTRA>(x, !(top & !x.odd), x.huse & !(top & !x.hrow));
  xraw_step<1, EXTRA>(x, true, x.huse);
  xraw_step<2, EXTRA>(x, true, x.huse);
  xraw_step<3, EXTRA>(x, true, x.huse);
  xraw_step<4, EXTRA>(x, !(bot & x.odd), x.huse & !(bot & x.hrow));
}
struct XRow { u32x4 r[4]; };
DI void xrow_issue(const Ctx& c, XRow& xr, int yrow) {
  const unsigned o = (unsigned)(((c.b * 128 + 32 * (c.w & 3) + 16 * (c.w >> 2) + 4 * (c.lane >> 4)) * 128 + yrow) * 128) * 2 + (c.lane & 15) * 16;
#pragma unroll
  for (int jj = 0; jj < 4; ++jj) xr.r[jj] = bld<AUX_ROW>(c.rx, o, jj * 32768);
}
DI f32x2 xrow_stats(const XRow& xr) {
  float s = 0.f, s1 = 0.f, qs = 0.f, q1 = 0.f;
#pragma unroll
  for (int jj = 0; jj < 4; jj += 2)
#pragma unroll
    for (int k = 0; k < 4; ++k) { s = dot2(xr.r[jj][k], 0x3f803f80u, s); qs = dot2(xr.r[jj][k], xr.r[jj][k], qs); s1 = dot2(xr.r[jj + 1][k], 0x3f803f80u, s1); q1 = dot2(xr.r[jj + 1][k], xr.r[jj + 1][k], q1); }
  return f32x2{wave_sum(s + s1), wave_sum(qs + q1)};
}
DI void xrow_cross(const Ctx& c, const XRow& xr, int yrow) {
  const int k16 = c.lane & 15;
  if (k16 == 7 || k16 == 8) {
    unsigned v[2];
#pragma unroll
    for (int m = 0; m < 2; ++m) {
      const unsigned a = __builtin_amdgcn_perm(xr.r[2 * m + 1][3], xr.r[2 * m][3], 0x07060302u), b2 = __builtin_amdgcn_perm(xr.r[2 * m + 1][0], xr.r[2 * m][0], 0x05040100u);
      v[m] = k16 == 7 ? a : b2;
    }
    bst_sc1(u32x4{v[0], c.tag, v[1], c.tag}, c.rw, c.wb + WB_XC + (unsigned)((yrow * 2 + (k16 - 7)) * 64 + 16 * (c.w & 3) + 8 * (c.w >> 2) + 2 * (c.lane >> 4)) * 8, 0);
  }
}
struct Halo { unsigned go[4], lo[4]; bool inr[4], valid[4]; };
DI void halo_init(const Ctx& c, Halo& h) {
  const int tid = c.t - 256, cg = tid & 15, i = tid >> 4;
#pragma unroll
  for (int r = 0; r < 4; ++r) {
    const int row = r == 0 ? 0 : r == 1 ? 9 : r == 2 ? 1 + (i & 7) : (i >> 1) * 9;
    const int col = r < 2 ? i : r == 2 ? (i >> 3) * 17 : 16 + (i & 1);
    h.inr[r] = r < 3 ? true : i < 4;
    h.valid[r] = h.inr[r] & in_image(c, row, col);
    h.go[r] = WS_HB + (unsigned)(((c.b * 128 + c.y0 - 1 + row) * 128 + c.x0 - 1 + col) * 256 + cg * 16); PIN(h.go[r]);
    h.lo[r] = L_T + row * RP + col * 256 + ((cg ^ swz(col & 15)) << 4); PIN(h.lo[r]);
  }
}
struct Clr { unsigned o[3]; };
DI void clear_init(const Ctx& c, Clr& cl) {
  const int tid = c.t - 256, cg = tid & 15, i = tid >> 4, j = i < 12 ? i : 11;
#pragma unroll
  for (int r = 0; r < 3; ++r) {
    const int ry = r == 0 ? 0 : r == 1 ? 7 : 1 + (j >> 1), rx = r < 2 ? i : (j & 1) * 15;
    cl.o[r] = WS_HB + (unsigned)(((c.b * 128 + c.y0 + ry) * 128 + c.x0 + rx) * 256 + cg * 16); PIN(cl.o[r]);
  }
}
DI void stats_to_lds(const Ctx& c, int g, const u32x4& v0, const u32x4& v1) {
  const float s = wave_sum(__uint_as_float(v0[0]) + __uint_as_float(v1[0])), q = wave_sum(__uint_as_float(v0[2]) + __uint_as_float(v1[2]));
  const float mean = s * (1.f / 524288.f); float var = q * (1.f / 524288.f) - mean * mean; var = var < 0.f ? 0.f : var;
  if (c.lane == 0) *(f32x2*)(c.smem + L_STAT + g * 8) = f32x2{mean, __builtin_amdgcn_rsqf(var + 1e-5f)};
}

template <int CONV>
DI void mainloop(const Ctx& c, f32x4 (&acc)[8][2], bf16x8 (&wr)[WD], unsigned wvoff, const f32x4 (&ini)[2], unsigned baseP, unsigned baseD) {
  const char* T = c.smem + L_T;
  auto rd = [&](int B, int r) -> bf16x8 {
    const int sp = B / 3, ty = B % 3;
    const unsigned a = ty == 2 ? (baseD ^ (sp << 6)) : (baseP ^ ((2 * sp + ty) << 5));
    return *(const bf16x8*)(T + a + r * RP);
  };
  bf16x8 xf[4];
#pragma unroll
  for (int r = 0; r < 3; ++r) xf[r] = rd(0, r);
#pragma unroll
  for (int B = 0; B < 12; ++B) {
#pragma unroll
    for (int r = 0; r < 10; ++r) {
      const int G = B * 10 + r;
      if (r == 7 && (B % 3) == 2 && B < 11) { bar_nowait(); SB(); }
      { const int Gn = G + 3; if (Gn < 120) xf[Gn & 3] = rd(Gn / 10, Gn % 10); }
#pragma unroll
      for (int dy = 0; dy < 3; ++dy) {
        const int row = r - dy;
        if (row >= 0 && row < 8) {
#pragma unroll
          for (int nt = 0; nt < 2; ++nt) {
            const int ts = 6 * B + 2 * dy + nt;
            if (CONV == 1) asm volatile("v_mfma_f32_16x16x32_bf16 %0, %1, %2, %0" : "+v"(acc[row][nt]) : "v"(xf[G & 3]), "v"(wr[ts % WD]));
            else if (B == 0 && dy == 0) asm volatile("v_mfma_f32_16x16x32_bf16 %0, %1, %2, %3" : "=&v"(acc[row][nt]) : "v"(wr[ts % WD]), "v"(xf[G & 3]), "v"(ini[nt]));
            else           asm volatile("v_mfma_f32_16x16x32_bf16 %0, %1, %2, %0" : "+v"(acc[row][nt]) : "v"(wr[ts % WD]), "v"(xf[G & 3]));
            if (row == 7) { const int tn = CONV * 72 + ts + WD; if (tn < 144) wr[ts % WD] = __builtin_bit_cast(bf16x8, bld<SC1>(c.rw, wvoff, (unsigned)tn * 1024 + (tn >= 72 ? 294912u - 72u * 1024u : 0u))); }
            SB();
          }
        }
      }
    }
  }
  asm volatile("s_nop 15" ::: "memory");
  SB();
}
DI u32x4 pack_row(const f32x4 (&a)[2]) {
  const unsigned d0 = cvtpk(a[0][0], a[0][1]), d1 = cvtpk(a[0][2], a[0][3]), d2 = cvtpk(a[1][0], a[1][1]), d3 = cvtpk(a[1][2], a[1][3]);
  auto p = __builtin_amdgcn_permlane16_swap(d0, d2, false, false);
  auto q = __builtin_amdgcn_permlane16_swap(d1, d3, false, false);
  return u32x4{p[0], q[0], p[1], q[1]};
}

__global__ __launch_bounds__(512, 2) void k_resblock(const u16* __restrict__ x, const u16* __restrict__ t_emb, const u16* __restrict__ g1, const u16* __restrict__ be1,
                                                     const u16* __restrict__ w1, const u16* __restrict__ w_emb, const u16* __restrict__ g2, const u16* __restrict__ be2,
                                                     const u16* __restrict__ w2, u16* __restrict__ out, char* ws) {
  extern __shared__ __attribute__((aligned(256))) char smem[];
  Ctx c;
  c.smem = smem; c.ws = ws;
  c.t = threadIdx.x; c.lane = c.t & 63; c.w = __builtin_amdgcn_readfirstlane(c.t >> 6);
  const int xcd = blockIdx.x & 7, jx = blockIdx.x >> 3;
  c.b = xcd >> 2; c.ty = (xcd & 3) * 4 + (jx >> 3); c.tx = jx & 7;
  c.jb = c.ty * 8 + c.tx; c.y0 = c.ty * 8; c.x0 = c.tx * 16;
  c.rx = mk_rsrc(x, 8u << 20); c.rw = mk_rsrc(ws, 16u << 20);
  c.wb = (unsigned)c.b * WB_SIZE;

  if (c.w < 4) {
    STAMP_DECL STAMP(0);
    const int yrow = (xcd & 3) * 32 + jx;
    const unsigned epoch = __hip_atomic_load((GAS unsigned*)(ws + c.wb + WB_EPOCH), __ATOMIC_RELAXED, __HIP_MEMORY_SCOPE_AGENT);
    XRow xr; xrow_issue(c, xr, yrow);
    const int ch = c.t & 127;
    const unsigned gbA = (c.t < 128 ? g1 : be1)[ch], gbB = (c.t < 128 ? g2 : be2)[ch];
    const int px = c.lane & 15, q = c.lane >> 4;
    const rsrc_t ro = mk_rsrc(out, 8u << 20);
    SB();
    unsigned oo = (unsigned)(((c.b * 128 + 32 * c.w + 16 * (q & 1) + px) * 128 + c.y0) * 128 + c.x0 + 8 * (q >> 1)) * 2; PIN(oo);
    XT xt; xraw_init(c, xt);
    Task0 t0; task0_init(c, t0);
    unsigned baseP, baseD;
    { const int colP = px + (q & 1), colD = px + 2;
      baseP = colP * 256 + (((q >> 1) ^ swz(colP & 15)) << 4); baseD = colD * 256 + ((q ^ swz(colD & 15)) << 4); PIN(baseP); PIN(baseD); }
    unsigned wvoff = c.wb + WB_WF + (unsigned)c.w * 73728u + (unsigned)c.lane * 16u; PIN(wvoff);
    unsigned fo = c.wb + WB_FLAG + (unsigned)(c.w * 128 + (c.lane & 7) * 4); PIN(fo);
    SB();
    STAMP(2);
    c.tag = __builtin_amdgcn_readfirstlane(epoch) + 1u;
    { const f32x2 p = xrow_stats(xr); if (c.lane == 0) *(f32x2*)(smem + L_RED + c.w * 8) = p; }
    bar();
    STAMP(14);
    xrow_cross(c, xr, yrow);
    { float* gbp = (float*)(smem + L_GB) + (c.t < 128 ? 0 : 128) + ch; gbp[0] = bflo(gbA) * LOG2E; gbp[256] = bflo(gbB) * LOG2E; }
    SB();
    xraw_issue(c, xt.vo); SB();
    unsigned fl = __builtin_amdgcn_raw_buffer_load_b32(c.rw, fo, 0, SC1); SB();
    STAMP(15);
    xraw_transpose<1>(c, xt); SB();
    STAMP(3);
    for (int spin = 0; spin < SPIN_MAX; ++spin) {
      if (!__any(fl != c.tag)) break;
      __builtin_amdgcn_s_sleep(POLL_SLEEP);
      cbar();
      fl = __builtin_amdgcn_raw_buffer_load_b32(c.rw, fo, 0, SC1);
    }
    STAMP(4);
    bf16x8 wr[WD];
#pragma unroll
    for (int k = 0; k < WD; ++k) wr[k] = __builtin_bit_cast(bf16x8, bld<SC1>(c.rw, wvoff, (unsigned)k * 1024));
    bar();
    STAMP(5);
    norm_group0(c, t0, 0);
    bar();
    STAMP(6);
    f32x4 acc[8][2], ini[2];
    ini[0] = *(const f32x4*)(smem + L_EMB + (32 * c.w + 4 * q) * 4); ini[1] = *(const f32x4*)(smem + L_EMB + (32 * c.w + 16 + 4 * q) * 4);
    __builtin_amdgcn_s_setprio(1);
    mainloop<0>(c, acc, wr, wvoff, ini, baseP, baseD);
    __builtin_amdgcn_s_setprio(0);
    STAMP(7);
    {
      f32x2 s2 = {0.f, 0.f}, q2 = {0.f, 0.f};
#pragma unroll
      for (int r = 0; r < 8; ++r)
#pragma unroll
        for (int nt = 0; nt < 2; ++nt)
#pragma unroll
          for (int k = 0; k < 2; ++k) { const f32x2 v = {acc[r][nt][2 * k], acc[r][nt][2 * k + 1]}; s2 += v; q2 += v * v; }
      const float s = wave_sum(s2[0] + s2[1]), qs = wave_sum(q2[0] + q2[1]);
      if (c.lane == 0) bst_sc1(u32x4{__float_as_uint(s), c.tag, __float_as_uint(qs), c.tag}, c.rw, c.wb + WB_G2 + (unsigned)(c.w * 128 + c.jb) * 16, 0);
    }
    STAMP(1);
    {
      const int chk = 4 * c.w + (((q & 1) << 1) | (q >> 1));
      const unsigned go = WS_HB + (unsigned)(((c.b * 128 + c.y0) * 128 + c.x0 + px) * 256 + chk * 16);
      u32x4 hv[8];
#pragma unroll
      for (int r = 0; r < 8; ++r) hv[r] = pack_row(acc[r]);
      auto san = [](u32x4 v) { return __builtin_elementwise_min(v, u32x4{0xfffffffeu, 0xfffffffeu, 0xfffffffeu, 0xfffffffeu}); };
      bst_sc1(san(hv[0]), c.rw, go, 0); bst_sc1(san(hv[7]), c.rw, go, 7 * 32768);
      if (px == 0 || px == 15) {
#pragma unroll
        for (int r = 1; r < 7; ++r) bst_sc1(san(hv[r]), c.rw, go, r * 32768);
      }
      bar_nowait();
      char* lp = smem + L_T + RP + (px + 1) * 256 + ((chk ^ swz((px + 1) & 15)) << 4);
#pragma unroll
      for (int r = 0; r < 8; ++r) *(u32x4*)(lp + r * RP) = hv[r];
    }
    STAMP(8);
#pragma unroll
    for (int r = 0; r < 8; ++r) {
      const u32x4 v = *(const u32x4*)(smem + L_RAW + (r + 1) * RS + (32 * c.w + 16 * (q & 1) + px) * 64 + 16 + (q >> 1) * 16);
      auto a = __builtin_amdgcn_permlane16_swap(v[0], v[2], false, false);
      auto e = __builtin_amdgcn_permlane16_swap(v[1], v[3], false, false);
      acc[r][0] = f32x4{bflo(a[0]), bfhi(a[0]), bflo(e[0]), bfhi(e[0])};
      acc[r][1] = f32x4{bflo(a[1]), bfhi(a[1]), bflo(e[1]), bfhi(e[1])};
      asm volatile("" : "+v"(acc[r][0]), "+v"(acc[r][1]));
    }
    STAMP(9);
    bar();
    STAMP(10);
    norm_group0(c, t0, 1);
    bar();
    STAMP(11);
    __builtin_amdgcn_s_setprio(1);
    mainloop<1>(c, acc, wr, wvoff, ini, baseP, baseD);
    __builtin_amdgcn_s_setprio(0);
    STAMP(12);
#pragma unroll
    for (int r = 0; r < 8; ++r) __builtin_amdgcn_raw_buffer_store_b128(pack_row(acc[r]), ro, oo + r * 256, 0, 0);
    STAMP(13); STAMP_FLUSH;
  } else {
    STAMP_DECL STAMP(0);

    unsigned pr_t[6];
    if (blockIdx.x == DIAG_WG && c.w == 5) {
      const rsrc_t rp = mk_rsrc(out, 8u << 20);
      const unsigned offs[5] = {0x340000u, 0x340100u, 0x342000u, 0x360000u, 0x540000u};
      pr_t[0] = (unsigned)__builtin_amdgcn_s_memrealtime();
#pragma unroll
      for (int i = 0; i < 5; ++i) {
        unsigned v = __builtin_amdgcn_raw_buffer_load_b32(rp, offs[i] + c.lane * 4, 0, 0);
        asm volatile("s_waitcnt vmcnt(0)" : "+v"(v) :: "memory");
        pr_t[i + 1] = (unsigned)__builtin_amdgcn_s_memrealtime();
        asm volatile("" : "+s"(pr_t[i + 1]));
      }
    }
    const int g = c.w - 4;
    const int yrow = (xcd & 3) * 32 + jx;
    const int ksvc = jx * 4 + (xcd & 3);
    const bool svc_w = c.w == 7 && (ksvc < 32 || (ksvc >= 64 && ksvc < 96)), svc_e = c.w == 7 && ksvc >= 32 && ksvc < 64;
    const int wu = ksvc < 32 ? ksvc : ksvc - 32;
    const unsigned epoch = __hip_atomic_load((GAS unsigned*)(ws + c.wb + WB_EPOCH), __ATOMIC_RELAXED, __HIP_MEMORY_SCOPE_AGENT);
    XRow xr; xrow_issue(c, xr, yrow);
    SB();
    u32x4 sv[11]; unsigned te[6];
    if (svc_w) {
      const int r3 = (c.lane * 57) >> 10, pc = c.lane - 18 * r3;
      const rsrc_t rws = mk_rsrc((wu >> 5) ? w2 : w1, 294912u);
      const unsigned o = (unsigned)(((32 * ((wu >> 3) & 3) + r3) * 128 + 16 * (wu & 7)) * 18 + pc * 16);
#pragma unroll
      for (int k = 0; k < 11; ++k) sv[k] = bld<AUX_SVC>(rws, o, k * 3 * 2304);
    } else if (svc_e) {
      const int j = ksvc - 32, e0 = 192 * (j >> 3) + (c.lane >> 1);
      const rsrc_t re = mk_rsrc(w_emb, 196608u), rt = mk_rsrc(t_emb, 3072u);
#pragma unroll
      for (int k = 0; k < 6; ++k) { sv[k] = bld<AUX_SVC>(re, (unsigned)(e0 * 256 + (16 * (j & 7) + 8 * (c.lane & 1)) * 2), k * 32 * 256);
                                    te[k] = __builtin_amdgcn_raw_buffer_load_b16(rt, (unsigned)(c.b * 768 + e0) * 2, k * 64, 0); }
    }
    SB();
    XT xt; xraw_init(c, xt);
    Task0 t0; task0_init(c, t0);
    HTask ht; htask_init(c, ht);
    if ((c.ty == 0) | (c.ty == 15) | (c.tx == 0) | (c.tx == 7)) {
      Halo hz; halo_init(c, hz);
#pragma unroll
      for (int r = 0; r < 4; ++r) if (hz.inr[r] & !hz.valid[r]) *(u32x4*)(smem + hz.lo[r]) = u32x4{0, 0, 0, 0};
    }
    SB();
    STAMP(4);
    c.tag = __builtin_amdgcn_readfirstlane(epoch) + 1u;
    STAMP(3);
    const f32x2 psum = xrow_stats(xr);
    asm volatile("" :: "v"(psum));
    STAMP(14);
    bar();
    { const f32x2 p = psum + *(const f32x2*)(smem + L_RED + g * 8);
      if (c.lane == 0) bst_sc1(u32x4{__float_as_uint(p[0]), c.tag, __float_as_uint(p[1]), c.tag}, c.rw, c.wb + WB_G1 + (unsigned)(g * 128 + c.jb) * 16, 0); }
    SB();
    STAMP(1);
    xrow_cross(c, xr, yrow);
    if (svc_w) {
      if (c.lane < 54) {
#pragma unroll
        for (int k = 0; k < 11; ++k) *(u32x4*)(smem + L_SVC + k * 864 + c.lane * 16) = sv[k];
      }
#pragma unroll
      for (int k = 0; k < 9; ++k) { sv[k] = *(const u32x4*)(smem + L_SVC + (c.lane & 31) * 288 + (c.lane >> 5) * 144 + k * 16); asm volatile("" : "+v"(sv[k])); }
    }
    SB();
    xraw_issue(c, xt.vo); SB();
    STAMP(5);
    if (svc_w) {
      const int cs = wu & 7, o = c.lane & 31, h = c.lane >> 5;
      const unsigned dst = c.wb + WB_WF + (unsigned)(wu >> 5) * 294912u + (unsigned)((wu >> 3) & 3) * 73728u + (unsigned)(o >> 4) * 1024u + (unsigned)(o & 15) * 16u;
      const unsigned dP = dst + (unsigned)(3 * (cs >> 1) + (cs & 1)) * 6144u + (unsigned)h * 512u;
      const unsigned dD = dst + (unsigned)(3 * (cs >> 1) + 2) * 6144u + (unsigned)(2 * (cs & 1) + h) * 256u;
#pragma unroll
      for (int k = 0; k < 9; ++k) {
        const int dy = k / 3, dx = k % 3;
        u32x4 o4;
#pragma unroll
        for (int m = 0; m < 4; ++m) {
          const int e0 = 18 * m + k, e1 = e0 + 9;
          const unsigned sel = ((e0 & 1) ? 0x0302u : 0x0100u) | ((e1 & 1) ? 0x07060000u : 0x05040000u);
          o4[m] = __builtin_amdgcn_perm(sv[e1 >> 3][(e1 >> 1) & 3], sv[e0 >> 3][(e0 >> 1) & 3], sel);
        }
        bst_sc1(o4, c.rw, dx < 2 ? dP : dD, dy * 2048 + (dx < 2 ? dx * 256 : 0));
      }
      SB();
      xraw_transpose<9>(c, xt);
      asm volatile("s_waitcnt vmcnt(0)" ::: "memory");
      if (c.lane == 0) __hip_atomic_store((GAS unsigned*)(ws + c.wb + WB_FLAG + (wu >> 3) * 128 + (wu & 7) * 4), c.tag, __ATOMIC_RELAXED, __HIP_MEMORY_SCOPE_AGENT);
    } else if (svc_e) {
      f32x2 a[4];
#pragma unroll
      for (int m = 0; m < 4; ++m) a[m] = f32x2{0.f, 0.f};
#pragma unroll
      for (int k = 0; k < 6; ++k) {
        unsigned tk = te[k]; PIN(tk);
        const float tv = bflo(tk);
        const float sl = tv * __builtin_amdgcn_rcpf(1.f + __builtin_amdgcn_exp2f(-tv * LOG2E));
#pragma unroll
        for (int m = 0; m < 4; ++m) a[m] += f32x2{bflo(sv[k][m]), bfhi(sv[k][m])} * sl;
      }
      float r[8];
#pragma unroll
      for (int m = 0; m < 8; ++m) r[m] = rows_sum(dppadd<0x128>(dppadd<0x124>(dppadd<0x4E>(a[m >> 1][m & 1]))));
      const int j = ksvc - 32;
      if (c.lane < 2) {
        const unsigned o = c.wb + WB_EG + (unsigned)((j >> 3) * 128 + 16 * (j & 7) + 8 * c.lane) * 8;
#pragma unroll
        for (int m = 0; m < 4; ++m) bst_sc1(u32x4{__float_as_uint(r[2 * m]), c.tag, __float_as_uint(r[2 * m + 1]), c.tag}, c.rw, o, m * 16);
      }
      SB();
      xraw_transpose<4>(c, xt);
    } else xraw_transpose<0>(c, xt);
    SB();
    STAMP(2);
    {
      u32x4 v0, v1, vc[2]; bool n0 = true, n1 = true, nc[2];
      const unsigned o = c.wb + WB_G1 + (unsigned)(g * 128 + c.lane) * 16;
      const bool xc = c.tx == 3 || c.tx == 4;
      unsigned co[2], cl2[2]; bool cin[2];
#pragma unroll
      for (int r = 0; r < 2; ++r) {
        const int id = c.t - 256 + 256 * r, row = id >> 5, i = id & 31, y = c.y0 - 1 + row, side = c.tx == 3 ? 1 : 0;
        cin[r] = xc & (id < 320);
        nc[r] = cin[r] & ((unsigned)y < 128u);
        co[r] = c.wb + WB_XC + (unsigned)((y * 2 + side) * 64 + 2 * i) * 8;
        cl2[r] = L_T + row * RP + side * 17 * 256 + (((i >> 1) ^ side) << 4) + (i & 1) * 8;
        vc[r] = u32x4{0, 0, 0, 0};
      }
      for (int spin = 0; spin < SPIN_MAX; ++spin) {
        cbar();
        if (n0) v0 = bld<SC1>(c.rw, o, 0);
        if (n1) v1 = bld<SC1>(c.rw, o, 1024);
        if (xc) {
#pragma unroll
          for (int r = 0; r < 2; ++r) if (nc[r]) vc[r] = bld<SC1>(c.rw, co[r], 0);
        }
        n0 = !((v0[1] == c.tag) & (v0[3] == c.tag)); n1 = !((v1[1] == c.tag) & (v1[3] == c.tag));
#pragma unroll
        for (int r = 0; r < 2; ++r) nc[r] = nc[r] & !((vc[r][1] == c.tag) & (vc[r][3] == c.tag));
        if (!__any(n0 | n1 | nc[0] | nc[1])) break;
        __builtin_amdgcn_s_sleep(POLL_SLEEP);
      }
      stats_to_lds(c, g, v0, v1);
      if (xc) {
#pragma unroll
        for (int r = 0; r < 2; ++r) if (cin[r]) *(u32x2*)(smem + cl2[r]) = u32x2{vc[r][0], vc[r][2]};
      }
    }
    u32x4 ve[4];
    if (c.w == 4) {
#pragma unroll
      for (int k = 0; k < 4; ++k) ve[k] = bld<SC1>(c.rw, c.wb + WB_EG + (unsigned)c.lane * 16, k * 1024);
    }
    STAMP(6);
    bar();
    if (c.t == 256 && c.jb == 0) __hip_atomic_store((GAS unsigned*)(ws + c.wb + WB_EPOCH), c.tag, __ATOMIC_RELAXED, __HIP_MEMORY_SCOPE_AGENT);
    norm_group0(c, t0, 0);
    if (c.w == 4) {
      for (int spin = 0; spin < SPIN_MAX; ++spin) {
        bool ok = true;
#pragma unroll
        for (int k = 0; k < 4; ++k) ok = ok & (ve[k][1] == c.tag) & (ve[k][3] == c.tag);
        if (!__any(!ok)) break;
        cbar();
#pragma unroll
        for (int k = 0; k < 4; ++k) ve[k] = bld<SC1>(c.rw, c.wb + WB_EG + (unsigned)c.lane * 16, k * 1024);
      }
      *(f32x2*)(smem + L_EMB + c.lane * 8) = f32x2{(__uint_as_float(ve[0][0]) + __uint_as_float(ve[1][0])) + (__uint_as_float(ve[2][0]) + __uint_as_float(ve[3][0])),
                                                  (__uint_as_float(ve[0][2]) + __uint_as_float(ve[1][2])) + (__uint_as_float(ve[2][2]) + __uint_as_float(ve[3][2]))};
    }
    STAMP(15);
    bar();
#pragma unroll 1
    for (int k = 1; k < 4; ++k) {
      if (k == 2) {
        Clr cl; clear_init(c, cl);
#pragma unroll
        for (int r = 0; r < 3; ++r) bst_sc1(u32x4{~0u, ~0u, ~0u, ~0u}, c.rw, cl.o[r], 0);
      }
      norm_group_h(c, ht, k);
      if (k == 2) asm volatile("s_waitcnt vmcnt(0)" ::: "memory");
      if (k == 3 && c.w == 6) {
        const unsigned fo = c.wb + WB_FLAG + (unsigned)((4 + ((c.lane >> 3) & 3)) * 128 + (c.lane & 7) * 4);
        for (int spin = 0; spin < SPIN_MAX; ++spin) {
          cbar();
          const unsigned f = __builtin_amdgcn_raw_buffer_load_b32(c.rw, fo, 0, SC1);
          if (!__any(f != c.tag)) break;
        }
      }
      bar();
      if (k == 2 && c.t == 256) __hip_atomic_store((GAS unsigned*)(ws + c.wb + WB_CF + c.jb * 4), c.tag, __ATOMIC_RELAXED, __HIP_MEMORY_SCOPE_AGENT);
    }
    STAMP(7);
    {
      Halo hl; halo_init(c, hl);
      if (c.w == 5) {
        const int l9 = c.lane < 9 ? c.lane : 4, dy = ((l9 * 11) >> 5) - 1, dx = l9 - 3 * (dy + 1) - 1;
        const int ny = c.ty + dy, nx = c.tx + dx;
        const bool chk = ((unsigned)ny < 16u) & ((unsigned)nx < 8u);
        const unsigned fo = c.wb + WB_CF + (unsigned)(chk ? ny * 8 + nx : c.jb) * 4;
        for (int spin = 0; spin < SPIN_MAX; ++spin) {
          cbar();
          const unsigned f = __builtin_amdgcn_raw_buffer_load_b32(c.rw, fo, 0, SC1);
          if (!__any(f != c.tag)) break;
          __builtin_amdgcn_s_sleep(2);
        }
      }
      u32x4 hv[4]; bool need[4];
#pragma unroll
      for (int r = 0; r < 4; ++r) { need[r] = hl.valid[r]; hv[r] = u32x4{0, 0, 0, 0}; }
      u32x4 v0, v1; bool n0 = true, n1 = true;
      const unsigned o = c.wb + WB_G2 + (unsigned)(g * 128 + c.lane) * 16;
      bar_nowait();
      STAMP(13);
      for (int spin = 0; spin < SPIN_MAX; ++spin) {
        cbar();
        if (n0) v0 = bld<SC1>(c.rw, o, 0);
        if (n1) v1 = bld<SC1>(c.rw, o, 1024);
#pragma unroll
        for (int r = 0; r < 4; ++r) if (need[r]) hv[r] = bld<SC1>(c.rw, hl.go[r], 0);
        bool any = false;
#pragma unroll
        for (int r = 0; r < 4; ++r) { need[r] = need[r] & ((hv[r][0] == ~0u) | (hv[r][1] == ~0u) | (hv[r][2] == ~0u) | (hv[r][3] == ~0u)); any |= need[r]; }
        n0 = !((v0[1] == c.tag) & (v0[3] == c.tag)); n1 = !((v1[1] == c.tag) & (v1[3] == c.tag));
        if (!__any(any | n0 | n1)) break;
        __builtin_amdgcn_s_sleep(POLL_SLEEP2);
      }
      STAMP(8);
#pragma unroll
      for (int r = 0; r < 4; ++r) if (hl.valid[r]) *(u32x4*)(smem + hl.lo[r]) = hv[r];
      stats_to_lds(c, g, v0, v1);
    }
    bar();
    norm_group0(c, t0, 1);
    bar();
#pragma unroll 1
    for (int k = 1; k < 4; ++k) { if (k == 1) STAMP(9); norm_group_h(c, ht, k); if (k == 1) STAMP(10); if (k == 2) STAMP(11); bar(); }
    STAMP(12); STAMP_FLUSH;
    if (blockIdx.x == DIAG_WG && c.w == 5) { const int val_ = (int)(pr_t[PROBE + 1] - pr_t[PROBE]) * PSCALE;
      const unsigned t0_ = (unsigned)__builtin_amdgcn_s_memrealtime(); while ((int)((unsigned)__builtin_amdgcn_s_memrealtime() - t0_) < val_) __builtin_amdgcn_s_sleep(1); }
  }
}

extern "C" void kernel_launch(void* const* d_in, const int* in_sizes, int n_in, void* d_out, int out_size, void* d_ws, size_t ws_size, hipStream_t stream) {
  (void)hipFuncSetAttribute((const void*)k_resblock, hipFuncAttributeMaxDynamicSharedMemorySize, L_END);
  k_resblock<<<256, 512, L_END, stream>>>((const u16*)d_in[0], (const u16*)d_in[1], (const u16*)d_in[2], (const u16*)d_in[3], (const u16*)d_in[4],
                                          (const u16*)d_in[5], (const u16*)d_in[6], (const u16*)d_in[7], (const u16*)d_in[8], (u16*)d_out, (char*)d_ws);
}
```

```cpp
#define DIAG_WG 100
#define PROBE 0
#define PSCALE 0
#define PBUF 0
#include <hip/hip_runtime.h>
#include <hip/hip_bf16.h>
#include <stdint.h>

typedef float f32x4 __attribute__((ext_vector_type(4)));
typedef float f32x2 __attribute__((ext_vector_type(2)));
typedef __bf16 bf16x8 __attribute__((ext_vector_type(8)));
typedef __bf16 bf16x2 __attribute__((ext_vector_type(2)));
typedef unsigned u32x4 __attribute__((ext_vector_type(4)));
typedef unsigned u32x2 __attribute__((ext_vector_type(2)));
typedef unsigned short u16;
#define GAS __attribute__((address_space(1)))
#define LAS __attribute__((address_space(3)))
#define DI __device__ __forceinline__
#define SB() __builtin_amdgcn_sched_barrier(0)
#define PIN(x) asm volatile("" : "+v"(x))

constexpr int RP = 4608;
constexpr int TILE_BYTES = 10 * RP;
constexpr int RS = 8224;
constexpr int WD = 18;
constexpr float LOG2E = 1.4426950408889634f;
constexpr int SC1 = 16, NT = 2;
constexpr int SPIN_MAX = 1 << 16;
#define POLL_SLEEP 4
#define POLL_SLEEP2 2
#define AUX_SVC 0
#define AUX_ROW 2

constexpr unsigned WB_SIZE = 1u << 20;
constexpr unsigned WB_WF = 0, WB_EPOCH = 589824, WB_FLAG = WB_EPOCH + 256  , WB_G1 = WB_FLAG + 1024, WB_G2 = WB_G1 + 8192,
                   WB_EG = WB_G2 + 8192  , WB_CF = WB_EG + 4096  , WB_XC = WB_CF + 512  ;
constexpr unsigned WS_HB = 2u << 20;
constexpr int L_T = 0, L_RAW = TILE_BYTES, L_GB = L_RAW + 10 * RS  , L_PAR = L_GB + 2048  , L_EMB = L_PAR + 1024,
              L_STAT = L_EMB + 512  , L_RED = L_STAT + 32  , L_DUMMY = (L_RED + 32 + 255) / 256 * 256  , L_SVC = L_DUMMY + 1024  ,
              L_END = L_SVC + 9504;

DI int swz(int c) { return c ^ ((c >> 1) & 4); }
DI unsigned cvtpk(float lo, float hi) { f32x2 v = {lo, hi}; bf16x2 b = __builtin_convertvector(v, bf16x2); return __builtin_bit_cast(unsigned, b); }
DI unsigned cvtpk_s(float lo, float hi) { unsigned r; asm("v_cvt_pk_bf16_f32 %0, %1, %2" : "=v"(r) : "v"(lo), "v"(hi)); return r; }
DI float bflo(unsigned d) { return __uint_as_float(d << 16); }
DI float bfhi(unsigned d) { return __uint_as_float(d & 0xffff0000u); }
DI float dot2(unsigned a, unsigned b, float c) { return __builtin_amdgcn_fdot2_f32_bf16(__builtin_bit_cast(bf16x2, a), __builtin_bit_cast(bf16x2, b), c, false); }
template <int CTRL> DI float dppadd(float v) { return v + __int_as_float(__builtin_amdgcn_update_dpp(0, __float_as_int(v), CTRL, 0xf, 0xf, true)); }
DI float rows_sum(float v) {
  auto p = __builtin_amdgcn_permlane16_swap(__float_as_uint(v), __float_as_uint(v), false, false);
  v = __uint_as_float(p[0]) + __uint_as_float(p[1]);
  auto q = __builtin_amdgcn_permlane32_swap(__float_as_uint(v), __float_as_uint(v), false, false);
  return __uint_as_float(q[0]) + __uint_as_float(q[1]);
}
DI float wave_sum(float v) { v = dppadd<0xB1>(v); v = dppadd<0x4E>(v); v = dppadd<0x141>(v); v = dppadd<0x140>(v); return rows_sum(v); }
DI void bar() { asm volatile("s_waitcnt lgkmcnt(0)\n\ts_barrier" ::: "memory"); }
DI void bar_nowait() { asm volatile("s_barrier" ::: "memory"); }
DI void cbar() { asm volatile("" ::: "memory"); }
typedef __amdgpu_buffer_rsrc_t rsrc_t;
DI rsrc_t mk_rsrc(const void* p, unsigned bytes) { return __builtin_amdgcn_make_buffer_rsrc((void*)p, 0, bytes, 0x00020000); }
template <int AUX> DI u32x4 bld(rsrc_t r, unsigned voff, unsigned soff) { return __builtin_amdgcn_raw_buffer_load_b128(r, voff, soff, AUX); }
DI void bst_sc1(u32x4 v, rsrc_t r, unsigned voff, unsigned off) { __builtin_amdgcn_raw_buffer_store_b128(v, r, voff + off, 0, SC1); }

struct Ctx {
  rsrc_t rx, rw;
  char* smem; char* ws;
  int b, ty, tx, jb, y0, x0, w, lane, t;
  unsigned wb;
  unsigned tag;
};

#define STAMP_DECL
#define STAMP(idx) do {} while (0)
#define STAMP_FLUSH do {} while (0)

DI u32x2 act4_packed(u32x2 v, f32x4 a, f32x4 b) {
  u32x2 o;
#pragma unroll
  for (int m = 0; m < 2; ++m) {
    f32x2 xx = {bflo(v[m]), bfhi(v[m])}, aa = {a[2 * m], a[2 * m + 1]}, bb = {b[2 * m], b[2 * m + 1]};
    f32x2 u = xx * aa + bb;
    f32x2 e = {__builtin_amdgcn_exp2f(-u[0]), __builtin_amdgcn_exp2f(-u[1])};
    f32x2 d = e * LOG2E + LOG2E;
    f32x2 r = {__builtin_amdgcn_rcpf(d[0]), __builtin_amdgcn_rcpf(d[1])};
    f32x2 y = u * r;
    o[m] = cvtpk(y[0], y[1]);
  }
  return o;
}
DI float act1(float x, float a, float b) { float u = __builtin_fmaf(x, a, b); float e = __builtin_amdgcn_exp2f(-u); return u * __builtin_amdgcn_rcpf(__builtin_fmaf(e, LOG2E, LOG2E)); }
DI u32x2 act4_scalar(u32x2 v, f32x4 a, f32x4 b) {
  u32x2 o;
  o[0] = cvtpk_s(act1(bflo(v[0]), a[0], b[0]), act1(bfhi(v[0]), a[1], b[1]));
  o[1] = cvtpk_s(act1(bflo(v[1]), a[2], b[2]), act1(bfhi(v[1]), a[3], b[3]));
  return o;
}
DI bool in_image(const Ctx& c, int row, int col) { return ((unsigned)(c.y0 - 1 + row) < 128u) & ((unsigned)(c.x0 - 1 + col) < 128u); }
struct Tasks { unsigned off[3]; };
DI void tasks_init(const Ctx& c, Tasks& tk, int idx, int c4) {
  const unsigned lo = (c4 & 1) * 8, dummy = L_DUMMY + c.lane * 16 + lo;
  const int col = idx & 15, row = idx >> 4;
  const unsigned o0 = L_T + row * RP + col * 256 + (((c4 >> 1) ^ swz(col)) << 4) + lo;
  const int j = idx - 32, row2 = idx < 32 ? 8 + row : j >> 1, col2 = idx < 32 ? col : 16 + (j & 1);
  const unsigned o2 = L_T + row2 * RP + col2 * 256 + (((c4 >> 1) ^ swz(col2 & 15)) << 4) + lo;
  tk.off[0] = o0; tk.off[1] = o0 + 4 * RP; tk.off[2] = idx < 52 ? o2 : dummy;
  if ((c.ty == 0) | (c.ty == 15) | (c.tx == 0) | (c.tx == 7)) {
    tk.off[0] = in_image(c, row, col) ? tk.off[0] : dummy;
    tk.off[1] = in_image(c, row + 4, col) ? tk.off[1] : dummy;
    tk.off[2] = in_image(c, row2, col2) ? tk.off[2] : dummy;
  }
#pragma unroll
  for (int r = 0; r < 3; ++r) PIN(tk.off[r]);
}
typedef Tasks Task0;
DI void task0_init(const Ctx& c, Task0& t0) { tasks_init(c, t0, c.t >> 3, c.t & 7); }
DI void norm_group0(const Ctx& c, const Task0& t0, int gbsel) {
  u32x2 v[3];
#pragma unroll
  for (int r = 0; r < 3; ++r) v[r] = *(const u32x2*)(c.smem + t0.off[r]);
  const int ch = 4 * (c.t & 7);
  const f32x4 g = *(const f32x4*)(c.smem + L_GB + gbsel * 1024 + ch * 4), be = *(const f32x4*)(c.smem + L_GB + gbsel * 1024 + 512 + ch * 4);
  const f32x2 st = *(const f32x2*)(c.smem + L_STAT);
  const f32x4 a = g * st[1], b = be - a * st[0];
#pragma unroll
  for (int r = 0; r < 2; ++r) *(u32x2*)(c.smem + t0.off[r]) = act4_packed(v[r], a, b);
  if (c.w < 7) *(u32x2*)(c.smem + t0.off[2]) = act4_packed(v[2], a, b);
  else if (c.lane < 48) {
    const int c2 = 32 + 2 * c.lane;
    const f32x2 g2 = *(const f32x2*)(c.smem + L_GB + gbsel * 1024 + c2 * 4), b2 = *(const f32x2*)(c.smem + L_GB + gbsel * 1024 + 512 + c2 * 4);
    const f32x2 s2 = *(const f32x2*)(c.smem + L_STAT + (c2 >> 5) * 8);
    const f32x2 aa = g2 * s2[1];
    *(f32x2*)(c.smem + L_PAR + c2 * 4) = aa; *(f32x2*)(c.smem + L_PAR + 512 + c2 * 4) = b2 - aa * s2[0];
  }
}
typedef Tasks HTask;
DI void htask_init(const Ctx& c, HTask& ht) { tasks_init(c, ht, (c.t - 256) >> 2, 2 * (c.t & 3)); }
DI void norm_group_h(const Ctx& c, const HTask& ht, int k) {
  const int ch = 32 * k + 8 * (c.t & 3);
  u32x4 v[3];
#pragma unroll
  for (int r = 0; r < 3; ++r) v[r] = *(const u32x4*)(c.smem + (ht.off[r] ^ (k << 6)));
  const f32x4 a0 = *(const f32x4*)(c.smem + L_PAR + ch * 4), a1 = *(const f32x4*)(c.smem + L_PAR + ch * 4 + 16);
  const f32x4 b0 = *(const f32x4*)(c.smem + L_PAR + 512 + ch * 4), b1 = *(const f32x4*)(c.smem + L_PAR + 512 + ch * 4 + 16);
#pragma unroll
  for (int r = 0; r < 3; ++r) {
    const u32x2 lo = act4_scalar(u32x2{v[r][0], v[r][1]}, a0, b0), hi = act4_scalar(u32x2{v[r][2], v[r][3]}, a1, b1);
    *(u32x4*)(c.smem + (ht.off[r] ^ (k << 6))) = u32x4{lo[0], lo[1], hi[0], hi[1]};
  }
}

DI void xraw_issue(const Ctx& c, unsigned vo) {
#pragma unroll
  for (int row = 0; row < 10; ++row) {
    const int y = row == 0 ? (c.ty == 0 ? 0 : c.y0 - 1) : row == 9 ? (c.ty == 15 ? 127 : c.y0 + 8) : c.y0 - 1 + row;
    __builtin_amdgcn_raw_ptr_buffer_load_lds(c.rx, (LAS void*)(c.smem + L_RAW + row * RS + c.w * 1024), 16, vo, y * 256, 0, 0);
  }
}
struct XT { unsigned vo, ra, rh, wa[2], wh; bool huse, odd, hrow; };
DI void xraw_init(const Ctx& c, XT& x) {
  const unsigned lb = (unsigned)(size_t)(LAS char*)c.smem;
  int xs = c.x0 - 8 + 8 * (c.lane & 3);
  { const int lo = c.x0 & 64; xs = xs < lo ? lo : (xs > lo + 56 ? lo + 56 : xs); }
  x.vo = (unsigned)(((c.b * 128 + 16 * c.w + (c.lane >> 2)) * 128) * 128 + xs) * 2; PIN(x.vo);
  const int gsel = c.lane >> 4, i = c.lane & 15, qp = i >> 2, p = i & 3;
  x.ra = lb + L_RAW + (gsel & 1) * RS + (4 * (4 * c.w + (gsel >> 1)) + qp) * 64 + 16 + p * 8; PIN(x.ra);
#pragma unroll
  for (int a = 0; a < 2; ++a) { x.wa[a] = lb + L_T + (gsel & 1) * RP + (i + 1) * 256 + (((2 * c.w + a) ^ swz((i + 1) & 15)) << 4) + (gsel >> 1) * 8; PIN(x.wa[a]); }
  x.rh = lb + L_RAW + (p >> 1) * RS + (4 * (4 * c.w + gsel) + qp) * 64 + 8 + (p & 1) * 40; PIN(x.rh);
  const int side = qp & 1;
  x.wh = lb + L_T + (qp >> 1) * RP + side * 17 * 256 + (((2 * c.w + (gsel >> 1)) ^ side) << 4) + (gsel & 1) * 8; PIN(x.wh);
  const bool colok = side ? ((c.tx != 7) & (c.tx != 3)) : ((c.tx != 0) & (c.tx != 4));
  x.huse = (p == (side ? 0 : 3)) & colok; x.odd = gsel & 1; x.hrow = qp >> 1;
}
template <int RPAIR, int EXTRA>
DI void xraw_step(const XT& x, bool rowok, bool hok) {
  u32x2 v[3];
  constexpr int RO = (RPAIR & 1) * 2 * RS;
  const unsigned ra2 = x.ra + (RPAIR >> 1) * 4 * RS, rh2 = x.rh + (RPAIR >> 1) * 4 * RS;
  asm volatile("s_waitcnt vmcnt(%5)\n\t"
               "ds_read_b64_tr_b16 %0, %3 offset:%6\n\tds_read_b64_tr_b16 %1, %3 offset:%7\n\tds_read_b64_tr_b16 %2, %4 offset:%6\n\ts_waitcnt lgkmcnt(0)"
               : "=&v"(v[0]), "=&v"(v[1]), "=&v"(v[2])
               : "v"(ra2), "v"(rh2), "i"(8 - 2 * RPAIR + EXTRA), "i"(RO), "i"(RO + 512) : "memory");
  if (rowok) {
#pragma unroll
    for (int a = 0; a < 2; ++a) asm volatile("ds_write_b64 %0, %1 offset:%2" :: "v"(x.wa[a]), "v"(v[a]), "i"(RPAIR * 2 * RP) : "memory");
  }
  if (hok) asm volatile("ds_write_b64 %0, %1 offset:%2" :: "v"(x.wh), "v"(v[2]), "i"(RPAIR * 2 * RP) : "memory");
}
template <int EXTRA>
DI void xraw_transpose(const Ctx& c, const XT& x) {
  const bool top = c.ty == 0, bot = c.ty == 15;
  xraw_step<0, EXTRA>(x, !(top & !x.odd), x.huse & !(top & !x.hrow));
  xraw_step<1, EXTRA>(x, true, x.huse);
  xraw_step<2, EXTRA>(x, true, x.huse);
  xraw_step<3, EXTRA>(x, true, x.huse);
  xraw_step<4, EXTRA>(x, !(bot & x.odd), x.huse & !(bot & x.hrow));
}
struct XRow { u32x4 r[4]; };
DI void xrow_issue(const Ctx& c, XRow& xr, int yrow) {
  const unsigned o = (unsigned)(((c.b * 128 + 32 * (c.w & 3) + 16 * (c.w >> 2) + 4 * (c.lane >> 4)) * 128 + yrow) * 128) * 2 + (c.lane & 15) * 16;
#pragma unroll
  for (int jj = 0; jj < 4; ++jj) xr.r[jj] = bld<AUX_ROW>(c.rx, o, jj * 32768);
}
DI f32x2 xrow_stats(const XRow& xr) {
  float s = 0.f, s1 = 0.f, qs = 0.f, q1 = 0.f;
#pragma unroll
  for (int jj = 0; jj < 4; jj += 2)
#pragma unroll
    for (int k = 0; k < 4; ++k) { s = dot2(xr.r[jj][k], 0x3f803f80u, s); qs = dot2(xr.r[jj][k], xr.r[jj][k], qs); s1 = dot2(xr.r[jj + 1][k], 0x3f803f80u, s1); q1 = dot2(xr.r[jj + 1][k], xr.r[jj + 1][k], q1); }
  return f32x2{wave_sum(s + s1), wave_sum(qs + q1)};
}
DI void xrow_cross(const Ctx& c, const XRow& xr, int yrow) {
  const int k16 = c.lane & 15;
  if (k16 == 7 || k16 == 8) {
    unsigned v[2];
#pragma unroll
    for (int m = 0; m < 2; ++m) {
      const unsigned a = __builtin_amdgcn_perm(xr.r[2 * m + 1][3], xr.r[2 * m][3], 0x07060302u), b2 = __builtin_amdgcn_perm(xr.r[2 * m + 1][0], xr.r[2 * m][0], 0x05040100u);
      v[m] = k16 == 7 ? a : b2;
    }
    bst_sc1(u32x4{v[0], c.tag, v[1], c.tag}, c.rw, c.wb + WB_XC + (unsigned)((yrow * 2 + (k16 - 7)) * 64 + 16 * (c.w & 3) + 8 * (c.w >> 2) + 2 * (c.lane >> 4)) * 8, 0);
  }
}
struct Halo { unsigned go[4], lo[4]; bool inr[4], valid[4]; };
DI void halo_init(const Ctx& c, Halo& h) {
  const int tid = c.t - 256, cg = tid & 15, i = tid >> 4;
#pragma unroll
  for (int r = 0; r < 4; ++r) {
    const int row = r == 0 ? 0 : r == 1 ? 9 : r == 2 ? 1 + (i & 7) : (i >> 1) * 9;
    const int col = r < 2 ? i : r == 2 ? (i >> 3) * 17 : 16 + (i & 1);
    h.inr[r] = r < 3 ? true : i < 4;
    h.valid[r] = h.inr[r] & in_image(c, row, col);
    h.go[r] = WS_HB + (unsigned)(((c.b * 128 + c.y0 - 1 + row) * 128 + c.x0 - 1 + col) * 256 + cg * 16); PIN(h.go[r]);
    h.lo[r] = L_T + row * RP + col * 256 + ((cg ^ swz(col & 15)) << 4); PIN(h.lo[r]);
  }
}
struct Clr { unsigned o[3]; };
DI void clear_init(const Ctx& c, Clr& cl) {
  const int tid = c.t - 256, cg = tid & 15, i = tid >> 4, j = i < 12 ? i : 11;
#pragma unroll
  for (int r = 0; r < 3; ++r) {
    const int ry = r == 0 ? 0 : r == 1 ? 7 : 1 + (j >> 1), rx = r < 2 ? i : (j & 1) * 15;
    cl.o[r] = WS_HB + (unsigned)(((c.b * 128 + c.y0 + ry) * 128 + c.x0 + rx) * 256 + cg * 16); PIN(cl.o[r]);
  }
}
DI void stats_to_lds(const Ctx& c, int g, const u32x4& v0, const u32x4& v1) {
  const float s = wave_sum(__uint_as_float(v0[0]) + __uint_as_float(v1[0])), q = wave_sum(__uint_as_float(v0[2]) + __uint_as_float(v1[2]));
  const float mean = s * (1.f / 524288.f); float var = q * (1.f / 524288.f) - mean * mean; var = var < 0.f ? 0.f : var;
  if (c.lane == 0) *(f32x2*)(c.smem + L_STAT + g * 8) = f32x2{mean, __builtin_amdgcn_rsqf(var + 1e-5f)};
}

template <int CONV>
DI void mainloop(const Ctx& c, f32x4 (&acc)[8][2], bf16x8 (&wr)[WD], unsigned wvoff, const f32x4 (&ini)[2], unsigned baseP, unsigned baseD) {
  const char* T = c.smem + L_T;
  auto rd = [&](int B, int r) -> bf16x8 {
    const int sp = B / 3, ty = B % 3;
    const unsigned a = ty == 2 ? (baseD ^ (sp << 6)) : (baseP ^ ((2 * sp + ty) << 5));
    return *(const bf16x8*)(T + a + r * RP);
  };
  bf16x8 xf[4];
#pragma unroll
  for (int r = 0; r < 3; ++r) xf[r] = rd(0, r);
#pragma unroll
  for (int B = 0; B < 12; ++B) {
#pragma unroll
    for (int r = 0; r < 10; ++r) {
      const int G = B * 10 + r;
      if (r == 7 && (B % 3) == 2 && B < 11) { bar_nowait(); SB(); }
      { const int Gn = G + 3; if (Gn < 120) xf[Gn & 3] = rd(Gn / 10, Gn % 10); }
#pragma unroll
      for (int dy = 0; dy < 3; ++dy) {
        const int row = r - dy;
        if (row >= 0 && row < 8) {
#pragma unroll
          for (int nt = 0; nt < 2; ++nt) {
            const int ts = 6 * B + 2 * dy + nt;
            if (CONV == 1) asm volatile("v_mfma_f32_16x16x32_bf16 %0, %1, %2, %0" : "+v"(acc[row][nt]) : "v"(xf[G & 3]), "v"(wr[ts % WD]));
            else if (B == 0 && dy == 0) asm volatile("v_mfma_f32_16x16x32_bf16 %0, %1, %2, %3" : "=&v"(acc[row][nt]) : "v"(wr[ts % WD]), "v"(xf[G & 3]), "v"(ini[nt]));
            else           asm volatile("v_mfma_f32_16x16x32_bf16 %0, %1, %2, %0" : "+v"(acc[row][nt]) : "v"(wr[ts % WD]), "v"(xf[G & 3]));
            if (row == 7) { const int tn = CONV * 72 + ts + WD; if (tn < 144) wr[ts % WD] = __builtin_bit_cast(bf16x8, bld<SC1>(c.rw, wvoff, (unsigned)tn * 1024 + (tn >= 72 ? 294912u - 72u * 1024u : 0u))); }
            SB();
          }
        }
      }
    }
  }
  asm volatile("s_nop 15" ::: "memory");
  SB();
}
DI u32x4 pack_row(const f32x4 (&a)[2]) {
  const unsigned d0 = cvtpk(a[0][0], a[0][1]), d1 = cvtpk(a[0][2], a[0][3]), d2 = cvtpk(a[1][0], a[1][1]), d3 = cvtpk(a[1][2], a[1][3]);
  auto p = __builtin_amdgcn_permlane16_swap(d0, d2, false, false);
  auto q = __builtin_amdgcn_permlane16_swap(d1, d3, false, false);
  return u32x4{p[0], q[0], p[1], q[1]};
}

__global__ __launch_bounds__(512, 2) void k_resblock(const u16* __restrict__ x, const u16* __restrict__ t_emb, const u16* __restrict__ g1, const u16* __restrict__ be1,
                                                     const u16* __restrict__ w1, const u16* __restrict__ w_emb, const u16* __restrict__ g2, const u16* __restrict__ be2,
                                                     const u16* __restrict__ w2, u16* __restrict__ out, char* ws) {
  extern __shared__ __attribute__((aligned(256))) char smem[];
  Ctx c;
  c.smem = smem; c.ws = ws;
  c.t = threadIdx.x; c.lane = c.t & 63; c.w = __builtin_amdgcn_readfirstlane(c.t >> 6);
  const int xcd = blockIdx.x & 7, jx = blockIdx.x >> 3;
  c.b = xcd >> 2; c.ty = (xcd & 3) * 4 + (jx >> 3); c.tx = jx & 7;
  c.jb = c.ty * 8 + c.tx; c.y0 = c.ty * 8; c.x0 = c.tx * 16;
  c.rx = mk_rsrc(x, 8u << 20); c.rw = mk_rsrc(ws, 16u << 20);
  c.wb = (unsigned)c.b * WB_SIZE;

  if (c.w < 4) {
    STAMP_DECL STAMP(0);
    const int yrow = (xcd & 3) * 32 + jx;
    const unsigned epoch = __hip_atomic_load((GAS unsigned*)(ws + c.wb + WB_EPOCH), __ATOMIC_RELAXED, __HIP_MEMORY_SCOPE_AGENT);
    XRow xr; xrow_issue(c, xr, yrow);
    const int ch = c.t & 127;
    const unsigned gbA = (c.t < 128 ? g1 : be1)[ch], gbB = (c.t < 128 ? g2 : be2)[ch];
    const int px = c.lane & 15, q = c.lane >> 4;
    const rsrc_t ro = mk_rsrc(out, 8u << 20);
    SB();
    unsigned oo = (unsigned)(((c.b * 128 + 32 * c.w + 16 * (q & 1) + px) * 128 + c.y0) * 128 + c.x0 + 8 * (q >> 1)) * 2; PIN(oo);
    XT xt; xraw_init(c, xt);
    Task0 t0; task0_init(c, t0);
    unsigned baseP, baseD;
    { const int colP = px + (q & 1), colD = px + 2;
      baseP = colP * 256 + (((q >> 1) ^ swz(colP & 15)) << 4); baseD = colD * 256 + ((q ^ swz(colD & 15)) << 4); PIN(baseP); PIN(baseD); }
    unsigned wvoff = c.wb + WB_WF + (unsigned)c.w * 73728u + (unsigned)c.lane * 16u; PIN(wvoff);
    unsigned fo = c.wb + WB_FLAG + (unsigned)(c.w * 128 + (c.lane & 7) * 4); PIN(fo);
    SB();
    STAMP(2);
    c.tag = __builtin_amdgcn_readfirstlane(epoch) + 1u;
    { const f32x2 p = xrow_stats(xr); if (c.lane == 0) *(f32x2*)(smem + L_RED + c.w * 8) = p; }
    bar();
    STAMP(14);
    xrow_cross(c, xr, yrow);
    { float* gbp = (float*)(smem + L_GB) + (c.t < 128 ? 0 : 128) + ch; gbp[0] = bflo(gbA) * LOG2E; gbp[256] = bflo(gbB) * LOG2E; }
    SB();
    xraw_issue(c, xt.vo); SB();
    unsigned fl = __builtin_amdgcn_raw_buffer_load_b32(c.rw, fo, 0, SC1); SB();
    STAMP(15);
    xraw_transpose<1>(c, xt); SB();
    STAMP(3);
    for (int spin = 0; spin < SPIN_MAX; ++spin) {
      if (!__any(fl != c.tag)) break;
      __builtin_amdgcn_s_sleep(POLL_SLEEP);
      cbar();
      fl = __builtin_amdgcn_raw_buffer_load_b32(c.rw, fo, 0, SC1);
    }
    STAMP(4);
    bf16x8 wr[WD];
#pragma unroll
    for (int k = 0; k < WD; ++k) wr[k] = __builtin_bit_cast(bf16x8, bld<SC1>(c.rw, wvoff, (unsigned)k * 1024));
    bar();
    STAMP(5);
    norm_group0(c, t0, 0);
    bar();
    STAMP(6);
    f32x4 acc[8][2], ini[2];
    ini[0] = *(const f32x4*)(smem + L_EMB + (32 * c.w + 4 * q) * 4); ini[1] = *(const f32x4*)(smem + L_EMB + (32 * c.w + 16 + 4 * q) * 4);
    __builtin_amdgcn_s_setprio(1);
    mainloop<0>(c, acc, wr, wvoff, ini, baseP, baseD);
    __builtin_amdgcn_s_setprio(0);
    STAMP(7);
    {
      f32x2 s2 = {0.f, 0.f}, q2 = {0.f, 0.f};
#pragma unroll
      for (int r = 0; r < 8; ++r)
#pragma unroll
        for (int nt = 0; nt < 2; ++nt)
#pragma unroll
          for (int k = 0; k < 2; ++k) { const f32x2 v = {acc[r][nt][2 * k], acc[r][nt][2 * k + 1]}; s2 += v; q2 += v * v; }
      const float s = wave_sum(s2[0] + s2[1]), qs = wave_sum(q2[0] + q2[1]);
      if (c.lane == 0) bst_sc1(u32x4{__float_as_uint(s), c.tag, __float_as_uint(qs), c.tag}, c.rw, c.wb + WB_G2 + (unsigned)(c.w * 128 + c.jb) * 16, 0);
    }
    STAMP(1);
    {
      const int chk = 4 * c.w + (((q & 1) << 1) | (q >> 1));
      const unsigned go = WS_HB + (unsigned)(((c.b * 128 + c.y0) * 128 + c.x0 + px) * 256 + chk * 16);
      u32x4 hv[8];
#pragma unroll
      for (int r = 0; r < 8; ++r) hv[r] = pack_row(acc[r]);
      auto san = [](u32x4 v) { return __builtin_elementwise_min(v, u32x4{0xfffffffeu, 0xfffffffeu, 0xfffffffeu, 0xfffffffeu}); };
      bst_sc1(san(hv[0]), c.rw, go, 0); bst_sc1(san(hv[7]), c.rw, go, 7 * 32768);
      if (px == 0 || px == 15) {
#pragma unroll
        for (int r = 1; r < 7; ++r) bst_sc1(san(hv[r]), c.rw, go, r * 32768);
      }
      bar_nowait();
      char* lp = smem + L_T + RP + (px + 1) * 256 + ((chk ^ swz((px + 1) & 15)) << 4);
#pragma unroll
      for (int r = 0; r < 8; ++r) *(u32x4*)(lp + r * RP) = hv[r];
    }
    STAMP(8);
#pragma unroll
    for (int r = 0; r < 8; ++r) {
      const u32x4 v = *(const u32x4*)(smem + L_RAW + (r + 1) * RS + (32 * c.w + 16 * (q & 1) + px) * 64 + 16 + (q >> 1) * 16);
      auto a = __builtin_amdgcn_permlane16_swap(v[0], v[2], false, false);
      auto e = __builtin_amdgcn_permlane16_swap(v[1], v[3], false, false);
      acc[r][0] = f32x4{bflo(a[0]), bfhi(a[0]), bflo(e[0]), bfhi(e[0])};
      acc[r][1] = f32x4{bflo(a[1]), bfhi(a[1]), bflo(e[1]), bfhi(e[1])};
      asm volatile("" : "+v"(acc[r][0]), "+v"(acc[r][1]));
    }
    STAMP(9);
    bar();
    STAMP(10);
    norm_group0(c, t0, 1);
    bar();
    STAMP(11);
    __builtin_amdgcn_s_setprio(1);
    mainloop<1>(c, acc, wr, wvoff, ini, baseP, baseD);
    __builtin_amdgcn_s_setprio(0);
    STAMP(12);
#pragma unroll
    for (int r = 0; r < 8; ++r) __builtin_amdgcn_raw_buffer_store_b128(pack_row(acc[r]), ro, oo + r * 256, 0, 0);
    STAMP(13); STAMP_FLUSH;
  } else {
    STAMP_DECL STAMP(0);
    const int g = c.w - 4;
    const int yrow = (xcd & 3) * 32 + jx;
    const int ksvc = jx * 4 + (xcd & 3);
    const bool svc_w = c.w == 7 && (ksvc < 32 || (ksvc >= 64 && ksvc < 96)), svc_e = c.w == 7 && ksvc >= 32 && ksvc < 64;
    const int wu = ksvc < 32 ? ksvc : ksvc - 32;
    const unsigned epoch = __hip_atomic_load((GAS unsigned*)(ws + c.wb + WB_EPOCH), __ATOMIC_RELAXED, __HIP_MEMORY_SCOPE_AGENT);
    XRow xr; xrow_issue(c, xr, yrow);
    SB();
    u32x4 sv[11]; unsigned te[6];
    if (svc_w) {
      const int r3 = (c.lane * 57) >> 10, pc = c.lane - 18 * r3;
      const rsrc_t rws = mk_rsrc((wu >> 5) ? w2 : w1, 294912u);
      const unsigned o = (unsigned)(((32 * ((wu >> 3) & 3) + r3) * 128 + 16 * (wu & 7)) * 18 + pc * 16);
#pragma unroll
      for (int k = 0; k < 11; ++k) sv[k] = bld<AUX_SVC>(rws, o, k * 3 * 2304);
    } else if (svc_e) {
      const int j = ksvc - 32, e0 = 192 * (j >> 3) + (c.lane >> 1);
      const rsrc_t re = mk_rsrc(w_emb, 196608u), rt = mk_rsrc(t_emb, 3072u);
#pragma unroll
      for (int k = 0; k < 6; ++k) { sv[k] = bld<AUX_SVC>(re, (unsigned)(e0 * 256 + (16 * (j & 7) + 8 * (c.lane & 1)) * 2), k * 32 * 256);
                                    te[k] = __builtin_amdgcn_raw_buffer_load_b16(rt, (unsigned)(c.b * 768 + e0) * 2, k * 64, 0); }
    }
    SB();
    XT xt; xraw_init(c, xt);
    Task0 t0; task0_init(c, t0);
    HTask ht; htask_init(c, ht);
    if ((c.ty == 0) | (c.ty == 15) | (c.tx == 0) | (c.tx == 7)) {
      Halo hz; halo_init(c, hz);
#pragma unroll
      for (int r = 0; r < 4; ++r) if (hz.inr[r] & !hz.valid[r]) *(u32x4*)(smem + hz.lo[r]) = u32x4{0, 0, 0, 0};
    }
    SB();
    STAMP(4);
    c.tag = __builtin_amdgcn_readfirstlane(epoch) + 1u;
    STAMP(3);
    const f32x2 psum = xrow_stats(xr);
    asm volatile("" :: "v"(psum));
    STAMP(14);
    bar();
    { const f32x2 p = psum + *(const f32x2*)(smem + L_RED + g * 8);
      if (c.lane == 0) bst_sc1(u32x4{__float_as_uint(p[0]), c.tag, __float_as_uint(p[1]), c.tag}, c.rw, c.wb + WB_G1 + (unsigned)(g * 128 + c.jb) * 16, 0); }
    SB();
    STAMP(1);
    xrow_cross(c, xr, yrow);
    if (svc_w) {
      if (c.lane < 54) {
#pragma unroll
        for (int k = 0; k < 11; ++k) *(u32x4*)(smem + L_SVC + k * 864 + c.lane * 16) = sv[k];
      }
#pragma unroll
      for (int k = 0; k < 9; ++k) { sv[k] = *(const u32x4*)(smem + L_SVC + (c.lane & 31) * 288 + (c.lane >> 5) * 144 + k * 16); asm volatile("" : "+v"(sv[k])); }
    }
    SB();
    xraw_issue(c, xt.vo); SB();
    STAMP(5);
    if (svc_w) {
      const int cs = wu & 7, o = c.lane & 31, h = c.lane >> 5;
      const unsigned dst = c.wb + WB_WF + (unsigned)(wu >> 5) * 294912u + (unsigned)((wu >> 3) & 3) * 73728u + (unsigned)(o >> 4) * 1024u + (unsigned)(o & 15) * 16u;
      const unsigned dP = dst + (unsigned)(3 * (cs >> 1) + (cs & 1)) * 6144u + (unsigned)h * 512u;
      const unsigned dD = dst + (unsigned)(3 * (cs >> 1) + 2) * 6144u + (unsigned)(2 * (cs & 1) + h) * 256u;
#pragma unroll
      for (int k = 0; k < 9; ++k) {
        const int dy = k / 3, dx = k % 3;
        u32x4 o4;
#pragma unroll
        for (int m = 0; m < 4; ++m) {
          const int e0 = 18 * m + k, e1 = e0 + 9;
          const unsigned sel = ((e0 & 1) ? 0x0302u : 0x0100u) | ((e1 & 1) ? 0x07060000u : 0x05040000u);
          o4[m] = __builtin_amdgcn_perm(sv[e1 >> 3][(e1 >> 1) & 3], sv[e0 >> 3][(e0 >> 1) & 3], sel);
        }
        bst_sc1(o4, c.rw, dx < 2 ? dP : dD, dy * 2048 + (dx < 2 ? dx * 256 : 0));
      }
      SB();
      xraw_transpose<9>(c, xt);
      asm volatile("s_waitcnt vmcnt(0)" ::: "memory");
      if (c.lane == 0) __hip_atomic_store((GAS unsigned*)(ws + c.wb + WB_FLAG + (wu >> 3) * 128 + (wu & 7) * 4), c.tag, __ATOMIC_RELAXED, __HIP_MEMORY_SCOPE_AGENT);
    } else if (svc_e) {
      f32x2 a[4];
#pragma unroll
      for (int m = 0; m < 4; ++m) a[m] = f32x2{0.f, 0.f};
#pragma unroll
      for (int k = 0; k < 6; ++k) {
        unsigned tk = te[k]; PIN(tk);
        const float tv = bflo(tk);
        const float sl = tv * __builtin_amdgcn_rcpf(1.f + __builtin_amdgcn_exp2f(-tv * LOG2E));
#pragma unroll
        for (int m = 0; m < 4; ++m) a[m] += f32x2{bflo(sv[k][m]), bfhi(sv[k][m])} * sl;
      }
      float r[8];
#pragma unroll
      for (int m = 0; m < 8; ++m) r[m] = rows_sum(dppadd<0x128>(dppadd<0x124>(dppadd<0x4E>(a[m >> 1][m & 1]))));
      const int j = ksvc - 32;
      if (c.lane < 2) {
        const unsigned o = c.wb + WB_EG + (unsigned)((j >> 3) * 128 + 16 * (j & 7) + 8 * c.lane) * 8;
#pragma unroll
        for (int m = 0; m < 4; ++m) bst_sc1(u32x4{__float_as_uint(r[2 * m]), c.tag, __float_as_uint(r[2 * m + 1]), c.tag}, c.rw, o, m * 16);
      }
      SB();
      xraw_transpose<4>(c, xt);
    } else xraw_transpose<0>(c, xt);
    SB();
    STAMP(2);
    {
      u32x4 v0, v1, vc[2]; bool n0 = true, n1 = true, nc[2];
      const unsigned o = c.wb + WB_G1 + (unsigned)(g * 128 + c.lane) * 16;
      const bool xc = c.tx == 3 || c.tx == 4;
      unsigned co[2], cl2[2]; bool cin[2];
#pragma unroll
      for (int r = 0; r < 2; ++r) {
        const int id = c.t - 256 + 256 * r, row = id >> 5, i = id & 31, y = c.y0 - 1 + row, side = c.tx == 3 ? 1 : 0;
        cin[r] = xc & (id < 320);
        nc[r] = cin[r] & ((unsigned)y < 128u);
        co[r] = c.wb + WB_XC + (unsigned)((y * 2 + side) * 64 + 2 * i) * 8;
        cl2[r] = L_T + row * RP + side * 17 * 256 + (((i >> 1) ^ side) << 4) + (i & 1) * 8;
        vc[r] = u32x4{0, 0, 0, 0};
      }
      for (int spin = 0; spin < SPIN_MAX; ++spin) {
        cbar();
        if (n0) v0 = bld<SC1>(c.rw, o, 0);
        if (n1) v1 = bld<SC1>(c.rw, o, 1024);
        if (xc) {
#pragma unroll
          for (int r = 0; r < 2; ++r) if (nc[r]) vc[r] = bld<SC1>(c.rw, co[r], 0);
        }
        n0 = !((v0[1] == c.tag) & (v0[3] == c.tag)); n1 = !((v1[1] == c.tag) & (v1[3] == c.tag));
#pragma unroll
        for (int r = 0; r < 2; ++r) nc[r] = nc[r] & !((vc[r][1] == c.tag) & (vc[r][3] == c.tag));
        if (!__any(n0 | n1 | nc[0] | nc[1])) break;
        __builtin_amdgcn_s_sleep(POLL_SLEEP);
      }
      stats_to_lds(c, g, v0, v1);
      if (xc) {
#pragma unroll
        for (int r = 0; r < 2; ++r) if (cin[r]) *(u32x2*)(smem + cl2[r]) = u32x2{vc[r][0], vc[r][2]};
      }
    }
    u32x4 ve[4];
    if (c.w == 4) {
#pragma unroll
      for (int k = 0; k < 4; ++k) ve[k] = bld<SC1>(c.rw, c.wb + WB_EG + (unsigned)c.lane * 16, k * 1024);
    }
    STAMP(6);
    bar();
    if (c.t == 256 && c.jb == 0) __hip_atomic_store((GAS unsigned*)(ws + c.wb + WB_EPOCH), c.tag, __ATOMIC_RELAXED, __HIP_MEMORY_SCOPE_AGENT);
    norm_group0(c, t0, 0);
    if (c.w == 4) {
      for (int spin = 0; spin < SPIN_MAX; ++spin) {
        bool ok = true;
#pragma unroll
        for (int k = 0; k < 4; ++k) ok = ok & (ve[k][1] == c.tag) & (ve[k][3] == c.tag);
        if (!__any(!ok)) break;
        cbar();
#pragma unroll
        for (int k = 0; k < 4; ++k) ve[k] = bld<SC1>(c.rw, c.wb + WB_EG + (unsigned)c.lane * 16, k * 1024);
      }
      *(f32x2*)(smem + L_EMB + c.lane * 8) = f32x2{(__uint_as_float(ve[0][0]) + __uint_as_float(ve[1][0])) + (__uint_as_float(ve[2][0]) + __uint_as_float(ve[3][0])),
                                                  (__uint_as_float(ve[0][2]) + __uint_as_float(ve[1][2])) + (__uint_as_float(ve[2][2]) + __uint_as_float(ve[3][2]))};
    }
    STAMP(15);
    bar();
#pragma unroll 1
    for (int k = 1; k < 4; ++k) {
      if (k == 2) {
        Clr cl; clear_init(c, cl);
#pragma unroll
        for (int r = 0; r < 3; ++r) bst_sc1(u32x4{~0u, ~0u, ~0u, ~0u}, c.rw, cl.o[r], 0);
      }
      norm_group_h(c, ht, k);
      if (k == 2) asm volatile("s_waitcnt vmcnt(0)" ::: "memory");
      if (k == 3 && c.w == 6) {
        const unsigned fo = c.wb + WB_FLAG + (unsigned)((4 + ((c.lane >> 3) & 3)) * 128 + (c.lane & 7) * 4);
        for (int spin = 0; spin < SPIN_MAX; ++spin) {
          cbar();
          const unsigned f = __builtin_amdgcn_raw_buffer_load_b32(c.rw, fo, 0, SC1);
          if (!__any(f != c.tag)) break;
        }
      }
      bar();
      if (k == 2 && c.t == 256) __hip_atomic_store((GAS unsigned*)(ws + c.wb + WB_CF + c.jb * 4), c.tag, __ATOMIC_RELAXED, __HIP_MEMORY_SCOPE_AGENT);
    }
    STAMP(7);
    {
      Halo hl; halo_init(c, hl);
      if (c.w == 5) {
        const int l9 = c.lane < 9 ? c.lane : 4, dy = ((l9 * 11) >> 5) - 1, dx = l9 - 3 * (dy + 1) - 1;
        const int ny = c.ty + dy, nx = c.tx + dx;
        const bool chk = ((unsigned)ny < 16u) & ((unsigned)nx < 8u);
        const unsigned fo = c.wb + WB_CF + (unsigned)(chk ? ny * 8 + nx : c.jb) * 4;
        for (int spin = 0; spin < SPIN_MAX; ++spin) {
          cbar();
          const unsigned f = __builtin_amdgcn_raw_buffer_load_b32(c.rw, fo, 0, SC1);
          if (!__any(f != c.tag)) break;
          __builtin_amdgcn_s_sleep(2);
        }
      }
      u32x4 hv[4]; bool need[4];
#pragma unroll
      for (int r = 0; r < 4; ++r) { need[r] = hl.valid[r]; hv[r] = u32x4{0, 0, 0, 0}; }
      u32x4 v0, v1; bool n0 = true, n1 = true;
      const unsigned o = c.wb + WB_G2 + (unsigned)(g * 128 + c.lane) * 16;
      bar_nowait();
      STAMP(13);
      for (int spin = 0; spin < SPIN_MAX; ++spin) {
        cbar();
        if (n0) v0 = bld<SC1>(c.rw, o, 0);
        if (n1) v1 = bld<SC1>(c.rw, o, 1024);
#pragma unroll
        for (int r = 0; r < 4; ++r) if (need[r]) hv[r] = bld<SC1>(c.rw, hl.go[r], 0);
        bool any = false;
#pragma unroll
        for (int r = 0; r < 4; ++r) { need[r] = need[r] & ((hv[r][0] == ~0u) | (hv[r][1] == ~0u) | (hv[r][2] == ~0u) | (hv[r][3] == ~0u)); any |= need[r]; }
        n0 = !((v0[1] == c.tag) & (v0[3] == c.tag)); n1 = !((v1[1] == c.tag) & (v1[3] == c.tag));
        if (!__any(any | n0 | n1)) break;
        __builtin_amdgcn_s_sleep(POLL_SLEEP2);
      }
      STAMP(8);
#pragma unroll
      for (int r = 0; r < 4; ++r) if (hl.valid[r]) *(u32x4*)(smem + hl.lo[r]) = hv[r];
      stats_to_lds(c, g, v0, v1);
    }
    bar();
    norm_group0(c, t0, 1);
    bar();
#pragma unroll 1
    for (int k = 1; k < 4; ++k) { if (k == 1) STAMP(9); norm_group_h(c, ht, k); if (k == 1) STAMP(10); if (k == 2) STAMP(11); bar(); }
    STAMP(12); STAMP_FLUSH;

    if (blockIdx.x == DIAG_WG && c.w == 5) {
      unsigned pr_t[7];
      const rsrc_t rp = PBUF == 0 ? c.rx : c.rw;
      const unsigned base = PBUF == 0 ? 0x140000u : 0x900000u;
      pr_t[0] = (unsigned)__builtin_amdgcn_s_memrealtime();
#pragma unroll
      for (int i = 0; i < 6; ++i) {
        unsigned v = __builtin_amdgcn_raw_buffer_load_b32(rp, base + (i >> 1) * 4096 + c.lane * 4, 0, 16);
        asm volatile("s_waitcnt vmcnt(0)" : "+v"(v) :: "memory");
        pr_t[i + 1] = (unsigned)__builtin_amdgcn_s_memrealtime();
        asm volatile("" : "+s"(pr_t[i + 1]));
      }
      const int val_ = (int)(pr_t[PROBE + 1] - pr_t[PROBE]) * PSCALE;
      const unsigned t0_ = (unsigned)__builtin_amdgcn_s_memrealtime(); while ((int)((unsigned)__builtin_amdgcn_s_memrealtime() - t0_) < val_ + 400) __builtin_amdgcn_s_sleep(1);
    }
  }
}

extern "C" void kernel_launch(void* const* d_in, const int* in_sizes, int n_in, void* d_out, int out_size, void* d_ws, size_t ws_size, hipStream_t stream) {
  (void)hipFuncSetAttribute((const void*)k_resblock, hipFuncAttributeMaxDynamicSharedMemorySize, L_END);
  k_resblock<<<256, 512, L_END, stream>>>((const u16*)d_in[0], (const u16*)d_in[1], (const u16*)d_in[2], (const u16*)d_in[3], (const u16*)d_in[4],
                                          (const u16*)d_in[5], (const u16*)d_in[6], (const u16*)d_in[7], (const u16*)d_in[8], (u16*)d_out, (char*)d_ws);
}
```

```cpp
#define DIAG 1
#define DIAG_WG 100
#define DIAG_WAVE 4
#define DIAG_A 4
#define DIAG_B 3
#define DIAG_SCALE 10
#include <hip/hip_runtime.h>
#include <hip/hip_bf16.h>
#include <stdint.h>

typedef float f32x4 __attribute__((ext_vector_type(4)));
typedef float f32x2 __attribute__((ext_vector_type(2)));
typedef __bf16 bf16x8 __attribute__((ext_vector_type(8)));
typedef __bf16 bf16x2 __attribute__((ext_vector_type(2)));
typedef unsigned u32x4 __attribute__((ext_vector_type(4)));
typedef unsigned u32x2 __attribute__((ext_vector_type(2)));
typedef unsigned short u16;
#define GAS __attribute__((address_space(1)))
#define LAS __attribute__((address_space(3)))
#define DI __device__ __forceinline__
#define SB() __builtin_amdgcn_sched_barrier(0)
#define PIN(x) asm volatile("" : "+v"(x))

constexpr int RP = 4608;
constexpr int TILE_BYTES = 10 * RP;
constexpr int RS = 8224;
constexpr int WD = 18;
constexpr float LOG2E = 1.4426950408889634f;
constexpr int SC1 = 16, NT = 2;
constexpr int SPIN_MAX = 1 << 16;
#define POLL_SLEEP 4
#define POLL_SLEEP2 2
#define AUX_SVC 0
#define AUX_ROW 2

constexpr unsigned WB_SIZE = 1u << 20;
constexpr unsigned WB_WF = 0, WB_EPOCH = 589824, WB_FLAG = WB_EPOCH + 256  , WB_G1 = WB_FLAG + 1024, WB_G2 = WB_G1 + 8192,
                   WB_EG = WB_G2 + 8192  , WB_CF = WB_EG + 4096  , WB_XC = WB_CF + 512  ;
constexpr unsigned WS_HB = 2u << 20;
constexpr int L_T = 0, L_RAW = TILE_BYTES, L_GB = L_RAW + 10 * RS  , L_PAR = L_GB + 2048  , L_EMB = L_PAR + 1024,
              L_STAT = L_EMB + 512  , L_RED = L_STAT + 32  , L_DUMMY = (L_RED + 32 + 255) / 256 * 256  , L_SVC = L_DUMMY + 1024  ,
              L_END = L_SVC + 9504;

DI int swz(int c) { return c ^ ((c >> 1) & 4); }
DI unsigned cvtpk(float lo, float hi) { f32x2 v = {lo, hi}; bf16x2 b = __builtin_convertvector(v, bf16x2); return __builtin_bit_cast(unsigned, b); }
DI unsigned cvtpk_s(float lo, float hi) { unsigned r; asm("v_cvt_pk_bf16_f32 %0, %1, %2" : "=v"(r) : "v"(lo), "v"(hi)); return r; }
DI float bflo(unsigned d) { return __uint_as_float(d << 16); }
DI float bfhi(unsigned d) { return __uint_as_float(d & 0xffff0000u); }
DI float dot2(unsigned a, unsigned b, float c) { return __builtin_amdgcn_fdot2_f32_bf16(__builtin_bit_cast(bf16x2, a), __builtin_bit_cast(bf16x2, b), c, false); }
template <int CTRL> DI float dppadd(float v) { return v + __int_as_float(__builtin_amdgcn_update_dpp(0, __float_as_int(v), CTRL, 0xf, 0xf, true)); }
DI float rows_sum(float v) {
  auto p = __builtin_amdgcn_permlane16_swap(__float_as_uint(v), __float_as_uint(v), false, false);
  v = __uint_as_float(p[0]) + __uint_as_float(p[1]);
  auto q = __builtin_amdgcn_permlane32_swap(__float_as_uint(v), __float_as_uint(v), false, false);
  return __uint_as_float(q[0]) + __uint_as_float(q[1]);
}
DI float wave_sum(float v) { v = dppadd<0xB1>(v); v = dppadd<0x4E>(v); v = dppadd<0x141>(v); v = dppadd<0x140>(v); return rows_sum(v); }
DI void bar() { asm volatile("s_waitcnt lgkmcnt(0)\n\ts_barrier" ::: "memory"); }
DI void bar_nowait() { asm volatile("s_barrier" ::: "memory"); }
DI void cbar() { asm volatile("" ::: "memory"); }
typedef __amdgpu_buffer_rsrc_t rsrc_t;
DI rsrc_t mk_rsrc(const void* p, unsigned bytes) { return __builtin_amdgcn_make_buffer_rsrc((void*)p, 0, bytes, 0x00020000); }
template <int AUX> DI u32x4 bld(rsrc_t r, unsigned voff, unsigned soff) { return __builtin_amdgcn_raw_buffer_load_b128(r, voff, soff, AUX); }
DI void bst_sc1(u32x4 v, rsrc_t r, unsigned voff, unsigned off) { __builtin_amdgcn_raw_buffer_store_b128(v, r, voff + off, 0, SC1); }

struct Ctx {
  rsrc_t rx, rw;
  char* smem; char* ws;
  int b, ty, tx, jb, y0, x0, w, lane, t;
  unsigned wb;
  unsigned tag;
};

#define STAMP_DECL unsigned st_t[16]; _Pragma("unroll") for (int i_ = 0; i_ < 16; ++i_) st_t[i_] = 0;
#define STAMP(idx) do { if (idx == DIAG_A || idx == DIAG_B) { st_t[idx] = (unsigned)__builtin_amdgcn_s_memrealtime(); asm volatile("" : "+s"(st_t[idx])); } } while (0)
#define STAMP_FLUSH do { if (blockIdx.x == DIAG_WG && c.w == DIAG_WAVE) { const int val_ = (int)(st_t[DIAG_B] - st_t[DIAG_A]) * DIAG_SCALE; \
  const unsigned t0_ = (unsigned)__builtin_amdgcn_s_memrealtime(); while ((int)((unsigned)__builtin_amdgcn_s_memrealtime() - t0_) < val_) __builtin_amdgcn_s_sleep(1); } } while (0)

DI u32x2 act4_packed(u32x2 v, f32x4 a, f32x4 b) {
  u32x2 o;
#pragma unroll
  for (int m = 0; m < 2; ++m) {
    f32x2 xx = {bflo(v[m]), bfhi(v[m])}, aa = {a[2 * m], a[2 * m + 1]}, bb = {b[2 * m], b[2 * m + 1]};
    f32x2 u = xx * aa + bb;
    f32x2 e = {__builtin_amdgcn_exp2f(-u[0]), __builtin_amdgcn_exp2f(-u[1])};
    f32x2 d = e * LOG2E + LOG2E;
    f32x2 r = {__builtin_amdgcn_rcpf(d[0]), __builtin_amdgcn_rcpf(d[1])};
    f32x2 y = u * r;
    o[m] = cvtpk(y[0], y[1]);
  }
  return o;
}
DI float act1(float x, float a, float b) { float u = __builtin_fmaf(x, a, b); float e = __builtin_amdgcn_exp2f(-u); return u * __builtin_amdgcn_rcpf(__builtin_fmaf(e, LOG2E, LOG2E)); }
DI u32x2 act4_scalar(u32x2 v, f32x4 a, f32x4 b) {
  u32x2 o;
  o[0] = cvtpk_s(act1(bflo(v[0]), a[0], b[0]), act1(bfhi(v[0]), a[1], b[1]));
  o[1] = cvtpk_s(act1(bflo(v[1]), a[2], b[2]), act1(bfhi(v[1]), a[3], b[3]));
  return o;
}
DI bool in_image(const Ctx& c, int row, int col) { return ((unsigned)(c.y0 - 1 + row) < 128u) & ((unsigned)(c.x0 - 1 + col) < 128u); }
struct Tasks { unsigned off[3]; };
DI void tasks_init(const Ctx& c, Tasks& tk, int idx, int c4) {
  const unsigned lo = (c4 & 1) * 8, dummy = L_DUMMY + c.lane * 16 + lo;
  const int col = idx & 15, row = idx >> 4;
  const unsigned o0 = L_T + row * RP + col * 256 + (((c4 >> 1) ^ swz(col)) << 4) + lo;
  const int j = idx - 32, row2 = idx < 32 ? 8 + row : j >> 1, col2 = idx < 32 ? col : 16 + (j & 1);
  const unsigned o2 = L_T + row2 * RP + col2 * 256 + (((c4 >> 1) ^ swz(col2 & 15)) << 4) + lo;
  tk.off[0] = o0; tk.off[1] = o0 + 4 * RP; tk.off[2] = idx < 52 ? o2 : dummy;
  if ((c.ty == 0) | (c.ty == 15) | (c.tx == 0) | (c.tx == 7)) {
    tk.off[0] = in_image(c, row, col) ? tk.off[0] : dummy;
    tk.off[1] = in_image(c, row + 4, col) ? tk.off[1] : dummy;
    tk.off[2] = in_image(c, row2, col2) ? tk.off[2] : dummy;
  }
#pragma unroll
  for (int r = 0; r < 3; ++r) PIN(tk.off[r]);
}
typedef Tasks Task0;
DI void task0_init(const Ctx& c, Task0& t0) { tasks_init(c, t0, c.t >> 3, c.t & 7); }
DI void norm_group0(const Ctx& c, const Task0& t0, int gbsel) {
  u32x2 v[3];
#pragma unroll
  for (int r = 0; r < 3; ++r) v[r] = *(const u32x2*)(c.smem + t0.off[r]);
  const int ch = 4 * (c.t & 7);
  const f32x4 g = *(const f32x4*)(c.smem + L_GB + gbsel * 1024 + ch * 4), be = *(const f32x4*)(c.smem + L_GB + gbsel * 1024 + 512 + ch * 4);
  const f32x2 st = *(const f32x2*)(c.smem + L_STAT);
  const f32x4 a = g * st[1], b = be - a * st[0];
#pragma unroll
  for (int r = 0; r < 2; ++r) *(u32x2*)(c.smem + t0.off[r]) = act4_packed(v[r], a, b);
  if (c.w < 7) *(u32x2*)(c.smem + t0.off[2]) = act4_packed(v[2], a, b);
  else if (c.lane < 48) {
    const int c2 = 32 + 2 * c.lane;
    const f32x2 g2 = *(const f32x2*)(c.smem + L_GB + gbsel * 1024 + c2 * 4), b2 = *(const f32x2*)(c.smem + L_GB + gbsel * 1024 + 512 + c2 * 4);
    const f32x2 s2 = *(const f32x2*)(c.smem + L_STAT + (c2 >> 5) * 8);
    const f32x2 aa = g2 * s2[1];
    *(f32x2*)(c.smem + L_PAR + c2 * 4) = aa; *(f32x2*)(c.smem + L_PAR + 512 + c2 * 4) = b2 - aa * s2[0];
  }
}
typedef Tasks HTask;
DI void htask_init(const Ctx& c, HTask& ht) { tasks_init(c, ht, (c.t - 256) >> 2, 2 * (c.t & 3)); }
DI void norm_group_h(const Ctx& c, const HTask& ht, int k) {
  const int ch = 32 * k + 8 * (c.t & 3);
  u32x4 v[3];
#pragma unroll
  for (int r = 0; r < 3; ++r) v[r] = *(const u32x4*)(c.smem + (ht.off[r] ^ (k << 6)));
  const f32x4 a0 = *(const f32x4*)(c.smem + L_PAR + ch * 4), a1 = *(const f32x4*)(c.smem + L_PAR + ch * 4 + 16);
  const f32x4 b0 = *(const f32x4*)(c.smem + L_PAR + 512 + ch * 4), b1 = *(const f32x4*)(c.smem + L_PAR + 512 + ch * 4 + 16);
#pragma unroll
  for (int r = 0; r < 3; ++r) {
    const u32x2 lo = act4_scalar(u32x2{v[r][0], v[r][1]}, a0, b0), hi = act4_scalar(u32x2{v[r][2], v[r][3]}, a1, b1);
    *(u32x4*)(c.smem + (ht.off[r] ^ (k << 6))) = u32x4{lo[0], lo[1], hi[0], hi[1]};
  }
}

DI void xraw_issue(const Ctx& c, unsigned vo) {
#pragma unroll
  for (int row = 0; row < 10; ++row) {
    const int y = row == 0 ? (c.ty == 0 ? 0 : c.y0 - 1) : row == 9 ? (c.ty == 15 ? 127 : c.y0 + 8) : c.y0 - 1 + row;
    __builtin_amdgcn_raw_ptr_buffer_load_lds(c.rx, (LAS void*)(c.smem + L_RAW + row * RS + c.w * 1024), 16, vo, y * 256, 0, 0);
  }
}
struct XT { unsigned vo, ra, rh, wa[2], wh; bool huse, odd, hrow; };
DI void xraw_init(const Ctx& c, XT& x) {
  const unsigned lb = (unsigned)(size_t)(LAS char*)c.smem;
  int xs = c.x0 - 8 + 8 * (c.lane & 3);
  { const int lo = c.x0 & 64; xs = xs < lo ? lo : (xs > lo + 56 ? lo + 56 : xs); }
  x.vo = (unsigned)(((c.b * 128 + 16 * c.w + (c.lane >> 2)) * 128) * 128 + xs) * 2; PIN(x.vo);
  const int gsel = c.lane >> 4, i = c.lane & 15, qp = i >> 2, p = i & 3;
  x.ra = lb + L_RAW + (gsel & 1) * RS + (4 * (4 * c.w + (gsel >> 1)) + qp) * 64 + 16 + p * 8; PIN(x.ra);
#pragma unroll
  for (int a = 0; a < 2; ++a) { x.wa[a] = lb + L_T + (gsel & 1) * RP + (i + 1) * 256 + (((2 * c.w + a) ^ swz((i + 1) & 15)) << 4) + (gsel >> 1) * 8; PIN(x.wa[a]); }
  x.rh = lb + L_RAW + (p >> 1) * RS + (4 * (4 * c.w + gsel) + qp) * 64 + 8 + (p & 1) * 40; PIN(x.rh);
  const int side = qp & 1;
  x.wh = lb + L_T + (qp >> 1) * RP + side * 17 * 256 + (((2 * c.w + (gsel >> 1)) ^ side) << 4) + (gsel & 1) * 8; PIN(x.wh);
  const bool colok = side ? ((c.tx != 7) & (c.tx != 3)) : ((c.tx != 0) & (c.tx != 4));
  x.huse = (p == (side ? 0 : 3)) & colok; x.odd = gsel & 1; x.hrow = qp >> 1;
}
template <int RPAIR, int EXTRA>
DI void xraw_step(const XT& x, bool rowok, bool hok) {
  u32x2 v[3];
  constexpr int RO = (RPAIR & 1) * 2 * RS;
  const unsigned ra2 = x.ra + (RPAIR >> 1) * 4 * RS, rh2 = x.rh + (RPAIR >> 1) * 4 * RS;
  asm volatile("s_waitcnt vmcnt(%5)\n\t"
               "ds_read_b64_tr_b16 %0, %3 offset:%6\n\tds_read_b64_tr_b16 %1, %3 offset:%7\n\tds_read_b64_tr_b16 %2, %4 offset:%6\n\ts_waitcnt lgkmcnt(0)"
               : "=&v"(v[0]), "=&v"(v[1]), "=&v"(v[2])
               : "v"(ra2), "v"(rh2), "i"(8 - 2 * RPAIR + EXTRA), "i"(RO), "i"(RO + 512) : "memory");
  if (rowok) {
#pragma unroll
    for (int a = 0; a < 2; ++a) asm volatile("ds_write_b64 %0, %1 offset:%2" :: "v"(x.wa[a]), "v"(v[a]), "i"(RPAIR * 2 * RP) : "memory");
  }
  if (hok) asm volatile("ds_write_b64 %0, %1 offset:%2" :: "v"(x.wh), "v"(v[2]), "i"(RPAIR * 2 * RP) : "memory");
}
template <int EXTRA>
DI void xraw_transpose(const Ctx& c, const XT& x) {
  const bool top = c.ty == 0, bot = c.ty == 15;
  xraw_step<0, EXTRA>(x, !(top & !x.odd), x.huse & !(top & !x.hrow));
  xraw_step<1, EXTRA>(x, true, x.huse);
  xraw_step<2, EXTRA>(x, true, x.huse);
  xraw_step<3, EXTRA>(x, true, x.huse);
  xraw_step<4, EXTRA>(x, !(bot & x.odd), x.huse & !(bot & x.hrow));
}
struct XRow { u32x4 r[4]; };
DI void xrow_issue(const Ctx& c, XRow& xr, int yrow) {
  const unsigned o = (unsigned)(((c.b * 128 + 32 * (c.w & 3) + 16 * (c.w >> 2) + 4 * (c.lane >> 4)) * 128 + yrow) * 128) * 2 + (c.lane & 15) * 16;
#pragma unroll
  for (int jj = 0; jj < 4; ++jj) xr.r[jj] = bld<AUX_ROW>(c.rx, o, jj * 32768);
}
DI f32x2 xrow_stats(const XRow& xr) {
  float s = 0.f, s1 = 0.f, qs = 0.f, q1 = 0.f;
#pragma unroll
  for (int jj = 0; jj < 4; jj += 2)
#pragma unroll
    for (int k = 0; k < 4; ++k) { s = dot2(xr.r[jj][k], 0x3f803f80u, s); qs = dot2(xr.r[jj][k], xr.r[jj][k], qs); s1 = dot2(xr.r[jj + 1][k], 0x3f803f80u, s1); q1 = dot2(xr.r[jj + 1][k], xr.r[jj + 1][k], q1); }
  return f32x2{wave_sum(s + s1), wave_sum(qs + q1)};
}
DI void xrow_cross(const Ctx& c, const XRow& xr, int yrow) {
  const int k16 = c.lane & 15;
  if (k16 == 7 || k16 == 8) {
    unsigned v[2];
#pragma unroll
    for (int m = 0; m < 2; ++m) {
      const unsigned a = __builtin_amdgcn_perm(xr.r[2 * m + 1][3], xr.r[2 * m][3], 0x07060302u), b2 = __builtin_amdgcn_perm(xr.r[2 * m + 1][0], xr.r[2 * m][0], 0x05040100u);
      v[m] = k16 == 7 ? a : b2;
    }
    bst_sc1(u32x4{v[0], c.tag, v[1], c.tag}, c.rw, c.wb + WB_XC + (unsigned)((yrow * 2 + (k16 - 7)) * 64 + 16 * (c.w & 3) + 8 * (c.w >> 2) + 2 * (c.lane >> 4)) * 8, 0);
  }
}
struct Halo { unsigned go[4], lo[4]; bool inr[4], valid[4]; };
DI void halo_init(const Ctx& c, Halo& h) {
  const int tid = c.t - 256, cg = tid & 15, i = tid >> 4;
#pragma unroll
  for (int r = 0; r < 4; ++r) {
    const int row = r == 0 ? 0 : r == 1 ? 9 : r == 2 ? 1 + (i & 7) : (i >> 1) * 9;
    const int col = r < 2 ? i : r == 2 ? (i >> 3) * 17 : 16 + (i & 1);
    h.inr[r] = r < 3 ? true : i < 4;
    h.valid[r] = h.inr[r] & in_image(c, row, col);
    h.go[r] = WS_HB + (unsigned)(((c.b * 128 + c.y0 - 1 + row) * 128 + c.x0 - 1 + col) * 256 + cg * 16); PIN(h.go[r]);
    h.lo[r] = L_T + row * RP + col * 256 + ((cg ^ swz(col & 15)) << 4); PIN(h.lo[r]);
  }
}
struct Clr { unsigned o[3]; };
DI void clear_init(const Ctx& c, Clr& cl) {
  const int tid = c.t - 256, cg = tid & 15, i = tid >> 4, j = i < 12 ? i : 11;
#pragma unroll
  for (int r = 0; r < 3; ++r) {
    const int ry = r == 0 ? 0 : r == 1 ? 7 : 1 + (j >> 1), rx = r < 2 ? i : (j & 1) * 15;
    cl.o[r] = WS_HB + (unsigned)(((c.b * 128 + c.y0 + ry) * 128 + c.x0 + rx) * 256 + cg * 16); PIN(cl.o[r]);
  }
}
DI void stats_to_lds(const Ctx& c, int g, const u32x4& v0, const u32x4& v1) {
  const float s = wave_sum(__uint_as_float(v0[0]) + __uint_as_float(v1[0])), q = wave_sum(__uint_as_float(v0[2]) + __uint_as_float(v1[2]));
  const float mean = s * (1.f / 524288.f); float var = q * (1.f / 524288.f) - mean * mean; var = var < 0.f ? 0.f : var;
  if (c.lane == 0) *(f32x2*)(c.smem + L_STAT + g * 8) = f32x2{mean, __builtin_amdgcn_rsqf(var + 1e-5f)};
}

template <int CONV>
DI void mainloop(const Ctx& c, f32x4 (&acc)[8][2], bf16x8 (&wr)[WD], unsigned wvoff, const f32x4 (&ini)[2], unsigned baseP, unsigned baseD) {
  const char* T = c.smem + L_T;
  auto rd = [&](int B, int r) -> bf16x8 {
    const int sp = B / 3, ty = B % 3;
    const unsigned a = ty == 2 ? (baseD ^ (sp << 6)) : (baseP ^ ((2 * sp + ty) << 5));
    return *(const bf16x8*)(T + a + r * RP);
  };
  bf16x8 xf[4];
#pragma unroll
  for (int r = 0; r < 3; ++r) xf[r] = rd(0, r);
#pragma unroll
  for (int B = 0; B < 12; ++B) {
#pragma unroll
    for (int r = 0; r < 10; ++r) {
      const int G = B * 10 + r;
      if (r == 7 && (B % 3) == 2 && B < 11) { bar_nowait(); SB(); }
      { const int Gn = G + 3; if (Gn < 120) xf[Gn & 3] = rd(Gn / 10, Gn % 10); }
#pragma unroll
      for (int dy = 0; dy < 3; ++dy) {
        const int row = r - dy;
        if (row >= 0 && row < 8) {
#pragma unroll
          for (int nt = 0; nt < 2; ++nt) {
            const int ts = 6 * B + 2 * dy + nt;
            if (CONV == 1) asm volatile("v_mfma_f32_16x16x32_bf16 %0, %1, %2, %0" : "+v"(acc[row][nt]) : "v"(xf[G & 3]), "v"(wr[ts % WD]));
            else if (B == 0 && dy == 0) asm volatile("v_mfma_f32_16x16x32_bf16 %0, %1, %2, %3" : "=&v"(acc[row][nt]) : "v"(wr[ts % WD]), "v"(xf[G & 3]), "v"(ini[nt]));
            else           asm volatile("v_mfma_f32_16x16x32_bf16 %0, %1, %2, %0" : "+v"(acc[row][nt]) : "v"(wr[ts % WD]), "v"(xf[G & 3]));
            if (row == 7) { const int tn = CONV * 72 + ts + WD; if (tn < 144) wr[ts % WD] = __builtin_bit_cast(bf16x8, bld<SC1>(c.rw, wvoff, (unsigned)tn * 1024 + (tn >= 72 ? 294912u - 72u * 1024u : 0u))); }
            SB();
          }
        }
      }
    }
  }
  asm volatile("s_nop 15" ::: "memory");
  SB();
}
DI u32x4 pack_row(const f32x4 (&a)[2]) {
  const unsigned d0 = cvtpk(a[0][0], a[0][1]), d1 = cvtpk(a[0][2], a[0][3]), d2 = cvtpk(a[1][0], a[1][1]), d3 = cvtpk(a[1][2], a[1][3]);
  auto p = __builtin_amdgcn_permlane16_swap(d0, d2, false, false);
  auto q = __builtin_amdgcn_permlane16_swap(d1, d3, false, false);
  return u32x4{p[0], q[0], p[1], q[1]};
}

__global__ __launch_bounds__(512, 2) void k_resblock(const u16* __restrict__ x, const u16* __restrict__ t_emb, const u16* __restrict__ g1, const u16* __restrict__ be1,
                                                     const u16* __restrict__ w1, const u16* __restrict__ w_emb, const u16* __restrict__ g2, const u16* __restrict__ be2,
                                                     const u16* __restrict__ w2, u16* __restrict__ out, char* ws) {
  extern __shared__ __attribute__((aligned(256))) char smem[];
  Ctx c;
  c.smem = smem; c.ws = ws;
  c.t = threadIdx.x; c.lane = c.t & 63; c.w = __builtin_amdgcn_readfirstlane(c.t >> 6);
  const int xcd = blockIdx.x & 7, jx = blockIdx.x >> 3;
  c.b = xcd >> 2; c.ty = (xcd & 3) * 4 + (jx >> 3); c.tx = jx & 7;
  c.jb = c.ty * 8 + c.tx; c.y0 = c.ty * 8; c.x0 = c.tx * 16;
  c.rx = mk_rsrc(x, 8u << 20); c.rw = mk_rsrc(ws, 16u << 20);
  c.wb = (unsigned)c.b * WB_SIZE;

  if (c.w < 4) {
    STAMP_DECL STAMP(0);
    const int yrow = (xcd & 3) * 32 + jx;
    const unsigned epoch = __hip_atomic_load((GAS unsigned*)(ws + c.wb + WB_EPOCH), __ATOMIC_RELAXED, __HIP_MEMORY_SCOPE_AGENT);
    XRow xr; xrow_issue(c, xr, yrow);
    const int ch = c.t & 127;
    const unsigned gbA = (c.t < 128 ? g1 : be1)[ch], gbB = (c.t < 128 ? g2 : be2)[ch];
    const int px = c.lane & 15, q = c.lane >> 4;
    const rsrc_t ro = mk_rsrc(out, 8u << 20);
    SB();
    unsigned oo = (unsigned)(((c.b * 128 + 32 * c.w + 16 * (q & 1) + px) * 128 + c.y0) * 128 + c.x0 + 8 * (q >> 1)) * 2; PIN(oo);
    XT xt; xraw_init(c, xt);
    Task0 t0; task0_init(c, t0);
    unsigned baseP, baseD;
    { const int colP = px + (q & 1), colD = px + 2;
      baseP = colP * 256 + (((q >> 1) ^ swz(colP & 15)) << 4); baseD = colD * 256 + ((q ^ swz(colD & 15)) << 4); PIN(baseP); PIN(baseD); }
    unsigned wvoff = c.wb + WB_WF + (unsigned)c.w * 73728u + (unsigned)c.lane * 16u; PIN(wvoff);
    unsigned fo = c.wb + WB_FLAG + (unsigned)(c.w * 128 + (c.lane & 7) * 4); PIN(fo);
    SB();
    STAMP(2);
    c.tag = __builtin_amdgcn_readfirstlane(epoch) + 1u;
    { const f32x2 p = xrow_stats(xr); if (c.lane == 0) *(f32x2*)(smem + L_RED + c.w * 8) = p; }
    bar();
    STAMP(14);
    xrow_cross(c, xr, yrow);
    { float* gbp = (float*)(smem + L_GB) + (c.t < 128 ? 0 : 128) + ch; gbp[0] = bflo(gbA) * LOG2E; gbp[256] = bflo(gbB) * LOG2E; }
    SB();
    xraw_issue(c, xt.vo); SB();
    unsigned fl = __builtin_amdgcn_raw_buffer_load_b32(c.rw, fo, 0, SC1); SB();
    STAMP(15);
    xraw_transpose<1>(c, xt); SB();
    STAMP(3);
    for (int spin = 0; spin < SPIN_MAX; ++spin) {
      if (!__any(fl != c.tag)) break;
      __builtin_amdgcn_s_sleep(POLL_SLEEP);
      cbar();
      fl = __builtin_amdgcn_raw_buffer_load_b32(c.rw, fo, 0, SC1);
    }
    STAMP(4);
    bf16x8 wr[WD];
#pragma unroll
    for (int k = 0; k < WD; ++k) wr[k] = __builtin_bit_cast(bf16x8, bld<SC1>(c.rw, wvoff, (unsigned)k * 1024));
    bar();
    STAMP(5);
    norm_group0(c, t0, 0);
    bar();
    STAMP(6);
    f32x4 acc[8][2], ini[2];
    ini[0] = *(const f32x4*)(smem + L_EMB + (32 * c.w + 4 * q) * 4); ini[1] = *(const f32x4*)(smem + L_EMB + (32 * c.w + 16 + 4 * q) * 4);
    __builtin_amdgcn_s_setprio(1);
    mainloop<0>(c, acc, wr, wvoff, ini, baseP, baseD);
    __builtin_amdgcn_s_setprio(0);
    STAMP(7);
    {
      f32x2 s2 = {0.f, 0.f}, q2 = {0.f, 0.f};
#pragma unroll
      for (int r = 0; r < 8; ++r)
#pragma unroll
        for (int nt = 0; nt < 2; ++nt)
#pragma unroll
          for (int k = 0; k < 2; ++k) { const f32x2 v = {acc[r][nt][2 * k], acc[r][nt][2 * k + 1]}; s2 += v; q2 += v * v; }
      const float s = wave_sum(s2[0] + s2[1]), qs = wave_sum(q2[0] + q2[1]);
      if (c.lane == 0) bst_sc1(u32x4{__float_as_uint(s), c.tag, __float_as_uint(qs), c.tag}, c.rw, c.wb + WB_G2 + (unsigned)(c.w * 128 + c.jb) * 16, 0);
    }
    STAMP(1);
    {
      const int chk = 4 * c.w + (((q & 1) << 1) | (q >> 1));
      const unsigned go = WS_HB + (unsigned)(((c.b * 128 + c.y0) * 128 + c.x0 + px) * 256 + chk * 16);
      u32x4 hv[8];
#pragma unroll
      for (int r = 0; r < 8; ++r) hv[r] = pack_row(acc[r]);
      auto san = [](u32x4 v) { return __builtin_elementwise_min(v, u32x4{0xfffffffeu, 0xfffffffeu, 0xfffffffeu, 0xfffffffeu}); };
      bst_sc1(san(hv[0]), c.rw, go, 0); bst_sc1(san(hv[7]), c.rw, go, 7 * 32768);
      if (px == 0 || px == 15) {
#pragma unroll
        for (int r = 1; r < 7; ++r) bst_sc1(san(hv[r]), c.rw, go, r * 32768);
      }
      bar_nowait();
      char* lp = smem + L_T + RP + (px + 1) * 256 + ((chk ^ swz((px + 1) & 15)) << 4);
#pragma unroll
      for (int r = 0; r < 8; ++r) *(u32x4*)(lp + r * RP) = hv[r];
    }
    STAMP(8);
#pragma unroll
    for (int r = 0; r < 8; ++r) {
      const u32x4 v = *(const u32x4*)(smem + L_RAW + (r + 1) * RS + (32 * c.w + 16 * (q & 1) + px) * 64 + 16 + (q >> 1) * 16);
      auto a = __builtin_amdgcn_permlane16_swap(v[0], v[2], false, false);
      auto e = __builtin_amdgcn_permlane16_swap(v[1], v[3], false, false);
      acc[r][0] = f32x4{bflo(a[0]), bfhi(a[0]), bflo(e[0]), bfhi(e[0])};
      acc[r][1] = f32x4{bflo(a[1]), bfhi(a[1]), bflo(e[1]), bfhi(e[1])};
      asm volatile("" : "+v"(acc[r][0]), "+v"(acc[r][1]));
    }
    STAMP(9);
    bar();
    STAMP(10);
    norm_group0(c, t0, 1);
    bar();
    STAMP(11);
    __builtin_amdgcn_s_setprio(1);
    mainloop<1>(c, acc, wr, wvoff, ini, baseP, baseD);
    __builtin_amdgcn_s_setprio(0);
    STAMP(12);
#pragma unroll
    for (int r = 0; r < 8; ++r) __builtin_amdgcn_raw_buffer_store_b128(pack_row(acc[r]), ro, oo + r * 256, 0, 0);
    STAMP(13); STAMP_FLUSH;
  } else {
    STAMP_DECL STAMP(0);
    const int g = c.w - 4;
    const int yrow = (xcd & 3) * 32 + jx;
    const int ksvc = jx * 4 + (xcd & 3);
    const bool svc_w = c.w == 7 && (ksvc < 32 || (ksvc >= 64 && ksvc < 96)), svc_e = c.w == 7 && ksvc >= 32 && ksvc < 64;
    const int wu = ksvc < 32 ? ksvc : ksvc - 32;
    const unsigned epoch = __hip_atomic_load((GAS unsigned*)(ws + c.wb + WB_EPOCH), __ATOMIC_RELAXED, __HIP_MEMORY_SCOPE_AGENT);
    XRow xr; xrow_issue(c, xr, yrow);
    SB();
    STAMP(9);
    u32x4 sv[11]; unsigned te[6];
    if (svc_w) {
      const int r3 = (c.lane * 57) >> 10, pc = c.lane - 18 * r3;
      const rsrc_t rws = mk_rsrc((wu >> 5) ? w2 : w1, 294912u);
      const unsigned o = (unsigned)(((32 * ((wu >> 3) & 3) + r3) * 128 + 16 * (wu & 7)) * 18 + pc * 16);
#pragma unroll
      for (int k = 0; k < 11; ++k) sv[k] = bld<AUX_SVC>(rws, o, k * 3 * 2304);
    } else if (svc_e) {
      const int j = ksvc - 32, e0 = 192 * (j >> 3) + (c.lane >> 1);
      const rsrc_t re = mk_rsrc(w_emb, 196608u), rt = mk_rsrc(t_emb, 3072u);
#pragma unroll
      for (int k = 0; k < 6; ++k) { sv[k] = bld<AUX_SVC>(re, (unsigned)(e0 * 256 + (16 * (j & 7) + 8 * (c.lane & 1)) * 2), k * 32 * 256);
                                    te[k] = __builtin_amdgcn_raw_buffer_load_b16(rt, (unsigned)(c.b * 768 + e0) * 2, k * 64, 0); }
    }
    SB();
    XT xt; xraw_init(c, xt);
    Task0 t0; task0_init(c, t0);
    HTask ht; htask_init(c, ht);
    if ((c.ty == 0) | (c.ty == 15) | (c.tx == 0) | (c.tx == 7)) {
      Halo hz; halo_init(c, hz);
#pragma unroll
      for (int r = 0; r < 4; ++r) if (hz.inr[r] & !hz.valid[r]) *(u32x4*)(smem + hz.lo[r]) = u32x4{0, 0, 0, 0};
    }
    SB();
    STAMP(4);
    c.tag = __builtin_amdgcn_readfirstlane(epoch) + 1u;
    STAMP(3);
    const f32x2 psum = xrow_stats(xr);
    asm volatile("" :: "v"(psum));
    STAMP(14);
    bar();
    { const f32x2 p = psum + *(const f32x2*)(smem + L_RED + g * 8);
      if (c.lane == 0) bst_sc1(u32x4{__float_as_uint(p[0]), c.tag, __float_as_uint(p[1]), c.tag}, c.rw, c.wb + WB_G1 + (unsigned)(g * 128 + c.jb) * 16, 0); }
    SB();
    STAMP(1);
    xrow_cross(c, xr, yrow);
    if (svc_w) {
      if (c.lane < 54) {
#pragma unroll
        for (int k = 0; k < 11; ++k) *(u32x4*)(smem + L_SVC + k * 864 + c.lane * 16) = sv[k];
      }
#pragma unroll
      for (int k = 0; k < 9; ++k) { sv[k] = *(const u32x4*)(smem + L_SVC + (c.lane & 31) * 288 + (c.lane >> 5) * 144 + k * 16); asm volatile("" : "+v"(sv[k])); }
    }
    SB();
    xraw_issue(c, xt.vo); SB();
    STAMP(5);
    if (svc_w) {
      const int cs = wu & 7, o = c.lane & 31, h = c.lane >> 5;
      const unsigned dst = c.wb + WB_WF + (unsigned)(wu >> 5) * 294912u + (unsigned)((wu >> 3) & 3) * 73728u + (unsigned)(o >> 4) * 1024u + (unsigned)(o & 15) * 16u;
      const unsigned dP = dst + (unsigned)(3 * (cs >> 1) + (cs & 1)) * 6144u + (unsigned)h * 512u;
      const unsigned dD = dst + (unsigned)(3 * (cs >> 1) + 2) * 6144u + (unsigned)(2 * (cs & 1) + h) * 256u;
#pragma unroll
      for (int k = 0; k < 9; ++k) {
        const int dy = k / 3, dx = k % 3;
        u32x4 o4;
#pragma unroll
        for (int m = 0; m < 4; ++m) {
          const int e0 = 18 * m + k, e1 = e0 + 9;
          const unsigned sel = ((e0 & 1) ? 0x0302u : 0x0100u) | ((e1 & 1) ? 0x07060000u : 0x05040000u);
          o4[m] = __builtin_amdgcn_perm(sv[e1 >> 3][(e1 >> 1) & 3], sv[e0 >> 3][(e0 >> 1) & 3], sel);
        }
        bst_sc1(o4, c.rw, dx < 2 ? dP : dD, dy * 2048 + (dx < 2 ? dx * 256 : 0));
      }
      SB();
      xraw_transpose<9>(c, xt);
      asm volatile("s_waitcnt vmcnt(0)" ::: "memory");
      if (c.lane == 0) __hip_atomic_store((GAS unsigned*)(ws + c.wb + WB_FLAG + (wu >> 3) * 128 + (wu & 7) * 4), c.tag, __ATOMIC_RELAXED, __HIP_MEMORY_SCOPE_AGENT);
    } else if (svc_e) {
      f32x2 a[4];
#pragma unroll
      for (int m = 0; m < 4; ++m) a[m] = f32x2{0.f, 0.f};
#pragma unroll
      for (int k = 0; k < 6; ++k) {
        unsigned tk = te[k]; PIN(tk);
        const float tv = bflo(tk);
        const float sl = tv * __builtin_amdgcn_rcpf(1.f + __builtin_amdgcn_exp2f(-tv * LOG2E));
#pragma unroll
        for (int m = 0; m < 4; ++m) a[m] += f32x2{bflo(sv[k][m]), bfhi(sv[k][m])} * sl;
      }
      float r[8];
#pragma unroll
      for (int m = 0; m < 8; ++m) r[m] = rows_sum(dppadd<0x128>(dppadd<0x124>(dppadd<0x4E>(a[m >> 1][m & 1]))));
      const int j = ksvc - 32;
      if (c.lane < 2) {
        const unsigned o = c.wb + WB_EG + (unsigned)((j >> 3) * 128 + 16 * (j & 7) + 8 * c.lane) * 8;
#pragma unroll
        for (int m = 0; m < 4; ++m) bst_sc1(u32x4{__float_as_uint(r[2 * m]), c.tag, __float_as_uint(r[2 * m + 1]), c.tag}, c.rw, o, m * 16);
      }
      SB();
      xraw_transpose<4>(c, xt);
    } else xraw_transpose<0>(c, xt);
    SB();
    STAMP(2);
    {
      u32x4 v0, v1, vc[2]; bool n0 = true, n1 = true, nc[2];
      const unsigned o = c.wb + WB_G1 + (unsigned)(g * 128 + c.lane) * 16;
      const bool xc = c.tx == 3 || c.tx == 4;
      unsigned co[2], cl2[2]; bool cin[2];
#pragma unroll
      for (int r = 0; r < 2; ++r) {
        const int id = c.t - 256 + 256 * r, row = id >> 5, i = id & 31, y = c.y0 - 1 + row, side = c.tx == 3 ? 1 : 0;
        cin[r] = xc & (id < 320);
        nc[r] = cin[r] & ((unsigned)y < 128u);
        co[r] = c.wb + WB_XC + (unsigned)((y * 2 + side) * 64 + 2 * i) * 8;
        cl2[r] = L_T + row * RP + side * 17 * 256 + (((i >> 1) ^ side) << 4) + (i & 1) * 8;
        vc[r] = u32x4{0, 0, 0, 0};
      }
      for (int spin = 0; spin < SPIN_MAX; ++spin) {
        cbar();
        if (n0) v0 = bld<SC1>(c.rw, o, 0);
        if (n1) v1 = bld<SC1>(c.rw, o, 1024);
        if (xc) {
#pragma unroll
          for (int r = 0; r < 2; ++r) if (nc[r]) vc[r] = bld<SC1>(c.rw, co[r], 0);
        }
        n0 = !((v0[1] == c.tag) & (v0[3] == c.tag)); n1 = !((v1[1] == c.tag) & (v1[3] == c.tag));
#pragma unroll
        for (int r = 0; r < 2; ++r) nc[r] = nc[r] & !((vc[r][1] == c.tag) & (vc[r][3] == c.tag));
        if (!__any(n0 | n1 | nc[0] | nc[1])) break;
        __builtin_amdgcn_s_sleep(POLL_SLEEP);
      }
      stats_to_lds(c, g, v0, v1);
      if (xc) {
#pragma unroll
        for (int r = 0; r < 2; ++r) if (cin[r]) *(u32x2*)(smem + cl2[r]) = u32x2{vc[r][0], vc[r][2]};
      }
    }
    u32x4 ve[4];
    if (c.w == 4) {
#pragma unroll
      for (int k = 0; k < 4; ++k) ve[k] = bld<SC1>(c.rw, c.wb + WB_EG + (unsigned)c.lane * 16, k * 1024);
    }
    STAMP(6);
    bar();
    if (c.t == 256 && c.jb == 0) __hip_atomic_store((GAS unsigned*)(ws + c.wb + WB_EPOCH), c.tag, __ATOMIC_RELAXED, __HIP_MEMORY_SCOPE_AGENT);
    norm_group0(c, t0, 0);
    if (c.w == 4) {
      for (int spin = 0; spin < SPIN_MAX; ++spin) {
        bool ok = true;
#pragma unroll
        for (int k = 0; k < 4; ++k) ok = ok & (ve[k][1] == c.tag) & (ve[k][3] == c.tag);
        if (!__any(!ok)) break;
        cbar();
#pragma unroll
        for (int k = 0; k < 4; ++k) ve[k] = bld<SC1>(c.rw, c.wb + WB_EG + (unsigned)c.lane * 16, k * 1024);
      }
      *(f32x2*)(smem + L_EMB + c.lane * 8) = f32x2{(__uint_as_float(ve[0][0]) + __uint_as_float(ve[1][0])) + (__uint_as_float(ve[2][0]) + __uint_as_float(ve[3][0])),
                                                  (__uint_as_float(ve[0][2]) + __uint_as_float(ve[1][2])) + (__uint_as_float(ve[2][2]) + __uint_as_float(ve[3][2]))};
    }
    STAMP(15);
    bar();
#pragma unroll 1
    for (int k = 1; k < 4; ++k) {
      if (k == 2) {
        Clr cl; clear_init(c, cl);
#pragma unroll
        for (int r = 0; r < 3; ++r) bst_sc1(u32x4{~0u, ~0u, ~0u, ~0u}, c.rw, cl.o[r], 0);
      }
      norm_group_h(c, ht, k);
      if (k == 2) asm volatile("s_waitcnt vmcnt(0)" ::: "memory");
      if (k == 3 && c.w == 6) {
        const unsigned fo = c.wb + WB_FLAG + (unsigned)((4 + ((c.lane >> 3) & 3)) * 128 + (c.lane & 7) * 4);
        for (int spin = 0; spin < SPIN_MAX; ++spin) {
          cbar();
          const unsigned f = __builtin_amdgcn_raw_buffer_load_b32(c.rw, fo, 0, SC1);
          if (!__any(f != c.tag)) break;
        }
      }
      bar();
      if (k == 2 && c.t == 256) __hip_atomic_store((GAS unsigned*)(ws + c.wb + WB_CF + c.jb * 4), c.tag, __ATOMIC_RELAXED, __HIP_MEMORY_SCOPE_AGENT);
    }
    STAMP(7);
    {
      Halo hl; halo_init(c, hl);
      if (c.w == 5) {
        const int l9 = c.lane < 9 ? c.lane : 4, dy = ((l9 * 11) >> 5) - 1, dx = l9 - 3 * (dy + 1) - 1;
        const int ny = c.ty + dy, nx = c.tx + dx;
        const bool chk = ((unsigned)ny < 16u) & ((unsigned)nx < 8u);
        const unsigned fo = c.wb + WB_CF + (unsigned)(chk ? ny * 8 + nx : c.jb) * 4;
        for (int spin = 0; spin < SPIN_MAX; ++spin) {
          cbar();
          const unsigned f = __builtin_amdgcn_raw_buffer_load_b32(c.rw, fo, 0, SC1);
          if (!__any(f != c.tag)) break;
          __builtin_amdgcn_s_sleep(2);
        }
      }
      u32x4 hv[4]; bool need[4];
#pragma unroll
      for (int r = 0; r < 4; ++r) { need[r] = hl.valid[r]; hv[r] = u32x4{0, 0, 0, 0}; }
      u32x4 v0, v1; bool n0 = true, n1 = true;
      const unsigned o = c.wb + WB_G2 + (unsigned)(g * 128 + c.lane) * 16;
      bar_nowait();
      STAMP(13);
      for (int spin = 0; spin < SPIN_MAX; ++spin) {
        cbar();
        if (n0) v0 = bld<SC1>(c.rw, o, 0);
        if (n1) v1 = bld<SC1>(c.rw, o, 1024);
#pragma unroll
        for (int r = 0; r < 4; ++r) if (need[r]) hv[r] = bld<SC1>(c.rw, hl.go[r], 0);
        bool any = false;
#pragma unroll
        for (int r = 0; r < 4; ++r) { need[r] = need[r] & ((hv[r][0] == ~0u) | (hv[r][1] == ~0u) | (hv[r][2] == ~0u) | (hv[r][3] == ~0u)); any |= need[r]; }
        n0 = !((v0[1] == c.tag) & (v0[3] == c.tag)); n1 = !((v1[1] == c.tag) & (v1[3] == c.tag));
        if (!__any(any | n0 | n1)) break;
        __builtin_amdgcn_s_sleep(POLL_SLEEP2);
      }
      STAMP(8);
#pragma unroll
      for (int r = 0; r < 4; ++r) if (hl.valid[r]) *(u32x4*)(smem + hl.lo[r]) = hv[r];
      stats_to_lds(c, g, v0, v1);
    }
    bar();
    norm_group0(c, t0, 1);
    bar();
#pragma unroll 1
    for (int k = 1; k < 4; ++k) { if (k == 1) STAMP(10); norm_group_h(c, ht, k); if (k == 1) STAMP(11); bar(); }
    STAMP(12); STAMP_FLUSH;
  }
}

extern "C" void kernel_launch(void* const* d_in, const int* in_sizes, int n_in, void* d_out, int out_size, void* d_ws, size_t ws_size, hipStream_t stream) {
  (void)hipFuncSetAttribute((const void*)k_resblock, hipFuncAttributeMaxDynamicSharedMemorySize, L_END);
  k_resblock<<<256, 512, L_END, stream>>>((const u16*)d_in[0], (const u16*)d_in[1], (const u16*)d_in[2], (const u16*)d_in[3], (const u16*)d_in[4],
                                          (const u16*)d_in[5], (const u16*)d_in[6], (const u16*)d_in[7], (const u16*)d_in[8], (u16*)d_out, (char*)d_ws);
}
```

```cpp
#include <hip/hip_runtime.h>
#include <hip/hip_bf16.h>
#include <stdint.h>

typedef float f32x4 __attribute__((ext_vector_type(4)));
typedef float f32x2 __attribute__((ext_vector_type(2)));
typedef __bf16 bf16x8 __attribute__((ext_vector_type(8)));
typedef __bf16 bf16x2 __attribute__((ext_vector_type(2)));
typedef unsigned u32x4 __attribute__((ext_vector_type(4)));
typedef unsigned u32x2 __attribute__((ext_vector_type(2)));
typedef unsigned short u16;
#define GAS __attribute__((address_space(1)))
#define DI __device__ __forceinline__
#define SB() __builtin_amdgcn_sched_barrier(0)

constexpr int RP = 4608;
constexpr int TILE_BYTES = 10 * RP;
constexpr int WD = 18;
constexpr float LOG2E = 1.4426950408889634f;
constexpr int SC1 = 16;
constexpr int SPIN_MAX = 1 << 16;

constexpr unsigned WB_SIZE = 1u << 20;
constexpr unsigned WB_WF = 0, WB_EPOCH = 589824, WB_FLAG = WB_EPOCH + 256  , WB_G1 = WB_FLAG + 1024, WB_G2 = WB_G1 + 8192,
                   WB_EG = WB_G2 + 8192  , WB_CF = WB_EG + 4096  , WB_XC = WB_CF + 512  ;
constexpr unsigned WS_HB = 2u << 20;
constexpr int RS = 8224;
constexpr int L_T = 0, L_RAW = TILE_BYTES, L_GB = L_RAW + 10 * RS  , L_PAR = L_GB + 2048  , L_EMB = L_PAR + 1024,
              L_STAT = L_EMB + 512  , L_DUMMY = (L_STAT + 64 + 255) / 256 * 256  , L_END = L_DUMMY + 1024;
#define LAS __attribute__((address_space(3)))

DI int swz(int c) { return c ^ ((c >> 1) & 4); }
DI unsigned cvtpk(float lo, float hi) { f32x2 v = {lo, hi}; bf16x2 b = __builtin_convertvector(v, bf16x2); return __builtin_bit_cast(unsigned, b); }
DI unsigned cvtpk_s(float lo, float hi) { unsigned r; asm("v_cvt_pk_bf16_f32 %0, %1, %2" : "=v"(r) : "v"(lo), "v"(hi)); return r; }
DI float bflo(unsigned d) { return __uint_as_float(d << 16); }
DI float bfhi(unsigned d) { return __uint_as_float(d & 0xffff0000u); }
DI float dot2(unsigned a, unsigned b, float c) { return __builtin_amdgcn_fdot2_f32_bf16(__builtin_bit_cast(bf16x2, a), __builtin_bit_cast(bf16x2, b), c, false); }
template <int CTRL> DI float dppadd(float v) { return v + __int_as_float(__builtin_amdgcn_update_dpp(0, __float_as_int(v), CTRL, 0xf, 0xf, true)); }
DI float wave_sum(float v) {
  v = dppadd<0xB1>(v); v = dppadd<0x4E>(v); v = dppadd<0x141>(v); v = dppadd<0x140>(v);
  auto p = __builtin_amdgcn_permlane16_swap(__float_as_uint(v), __float_as_uint(v), false, false);
  v = __uint_as_float(p[0]) + __uint_as_float(p[1]);
  auto q = __builtin_amdgcn_permlane32_swap(__float_as_uint(v), __float_as_uint(v), false, false);
  return __uint_as_float(q[0]) + __uint_as_float(q[1]);
}
DI void bar() { asm volatile("s_waitcnt lgkmcnt(0)\n\ts_barrier" ::: "memory"); }
DI void bar_nowait() { asm volatile("s_barrier" ::: "memory"); }
DI void cbar() { asm volatile("" ::: "memory"); }
typedef __amdgpu_buffer_rsrc_t rsrc_t;
DI rsrc_t mk_rsrc(const void* p, unsigned bytes) { return __builtin_amdgcn_make_buffer_rsrc((void*)p, 0, bytes, 0x00020000); }
#define AUX_ROW 0
#define AUX_DMA 0
#define NT_SVC 0
DI u32x4 bld(rsrc_t r, unsigned voff, unsigned soff) { return __builtin_amdgcn_raw_buffer_load_b128(r, voff, soff, 0); }
DI u32x4 bld_row(rsrc_t r, unsigned voff, unsigned soff) { return __builtin_amdgcn_raw_buffer_load_b128(r, voff, soff, AUX_ROW); }
DI u32x4 gld16(const void* p) { if (NT_SVC) return __builtin_nontemporal_load((const u32x4*)p); else return *(const u32x4*)p; }
DI u32x4 bld_sc1(rsrc_t r, unsigned voff, unsigned soff) { return __builtin_amdgcn_raw_buffer_load_b128(r, voff, soff, SC1); }
DI void bst_sc1(u32x4 v, rsrc_t r, unsigned voff, unsigned soff) { __builtin_amdgcn_raw_buffer_store_b128(v, r, voff, soff, SC1); }

struct Ctx {
  rsrc_t rx, rw;
  char* smem; char* ws;
  int b, ty, tx, jb, y0, x0, w, lane, t;
  unsigned wb;
  unsigned tag;
};

#define STAMP_DECL
#define STAMP(idx) do {} while (0)
#define STAMP_FLUSH do {} while (0)

DI void stat_par(const char* smem, int gbsel, int grp, int ch, f32x4& a, f32x4& b) {
  const f32x4 g = *(const f32x4*)(smem + L_GB + gbsel * 1024 + ch * 4), be = *(const f32x4*)(smem + L_GB + gbsel * 1024 + 512 + ch * 4);
  const f32x2 st = *(const f32x2*)(smem + L_STAT + grp * 8);
  a = g * (st[1] * LOG2E); b = be * LOG2E - a * st[0];
}
DI u32x2 act4_packed(u32x2 v, f32x4 a, f32x4 b) {
  u32x2 o;
#pragma unroll
  for (int m = 0; m < 2; ++m) {
    f32x2 xx = {bflo(v[m]), bfhi(v[m])}, aa = {a[2 * m], a[2 * m + 1]}, bb = {b[2 * m], b[2 * m + 1]};
    f32x2 u = xx * aa + bb;
    f32x2 e = {__builtin_amdgcn_exp2f(-u[0]), __builtin_amdgcn_exp2f(-u[1])};
    f32x2 d = e * LOG2E + LOG2E;
    f32x2 r = {__builtin_amdgcn_rcpf(d[0]), __builtin_amdgcn_rcpf(d[1])};
    f32x2 y = u * r;
    o[m] = cvtpk(y[0], y[1]);
  }
  return o;
}
DI float act1(float x, float a, float b) { float u = __builtin_fmaf(x, a, b); float e = __builtin_amdgcn_exp2f(-u); return u * __builtin_amdgcn_rcpf(__builtin_fmaf(e, LOG2E, LOG2E)); }
DI u32x2 act4_scalar(u32x2 v, f32x4 a, f32x4 b) {
  u32x2 o;
  o[0] = cvtpk_s(act1(bflo(v[0]), a[0], b[0]), act1(bfhi(v[0]), a[1], b[1]));
  o[1] = cvtpk_s(act1(bflo(v[1]), a[2], b[2]), act1(bfhi(v[1]), a[3], b[3]));
  return o;
}
DI unsigned ntask(const Ctx& c, unsigned px, int c4) {
  const unsigned row = px / 18, col = px - row * 18;
  const unsigned off = L_T + row * RP + col * 256 + (((c4 >> 1) ^ swz(col & 15)) << 4) + (c4 & 1) * 8;
  const bool valid = px < 180 && (unsigned)(c.y0 - 1 + (int)row) < 128u && (unsigned)(c.x0 - 1 + (int)col) < 128u;
  unsigned r = valid ? off : (unsigned)(L_DUMMY + c.lane * 16 + (c4 & 1) * 8);
  asm volatile("" : "+v"(r));
  return r;
}
struct Task0 { unsigned off[3]; };
DI void task0_init(const Ctx& c, Task0& t0) {
#pragma unroll
  for (int r = 0; r < 3; ++r) t0.off[r] = ntask(c, (c.t >> 3) + 64 * r, c.t & 7);
}
DI void norm_group0(const Ctx& c, const Task0& t0, int gbsel) {
  u32x2 v[3];
#pragma unroll
  for (int r = 0; r < 3; ++r) v[r] = *(const u32x2*)(c.smem + t0.off[r]);
  f32x4 a, b; stat_par(c.smem, gbsel, 0, 4 * (c.t & 7), a, b);
#pragma unroll
  for (int r = 0; r < 2; ++r) *(u32x2*)(c.smem + t0.off[r]) = act4_packed(v[r], a, b);
  if (c.w < 7) *(u32x2*)(c.smem + t0.off[2]) = act4_packed(v[2], a, b);
  else {
    if (c.lane < 48) {
      const int ch = 32 + 2 * c.lane;
      const f32x2 g = *(const f32x2*)(c.smem + L_GB + gbsel * 1024 + ch * 4), be = *(const f32x2*)(c.smem + L_GB + gbsel * 1024 + 512 + ch * 4);
      const f32x2 st = *(const f32x2*)(c.smem + L_STAT + (ch >> 5) * 8);
      const f32x2 aa = g * (st[1] * LOG2E);
      *(f32x2*)(c.smem + L_PAR + ch * 4) = aa; *(f32x2*)(c.smem + L_PAR + 512 + ch * 4) = be * LOG2E - aa * st[0];
    }
  }
}
struct HTask { unsigned off[3]; };
DI void htask_init(const Ctx& c, HTask& ht) {
#pragma unroll
  for (int r = 0; r < 3; ++r) ht.off[r] = ntask(c, ((c.t - 256) >> 2) + 64 * r, 2 * (c.t & 3));
}
DI void norm_group_h(const Ctx& c, const HTask& ht, int k) {
  const int ch = 32 * k + 8 * (c.t & 3);
  u32x4 v[3];
#pragma unroll
  for (int r = 0; r < 3; ++r) v[r] = *(const u32x4*)(c.smem + (ht.off[r] ^ (k << 6)));
  const f32x4 a0 = *(const f32x4*)(c.smem + L_PAR + ch * 4), a1 = *(const f32x4*)(c.smem + L_PAR + ch * 4 + 16);
  const f32x4 b0 = *(const f32x4*)(c.smem + L_PAR + 512 + ch * 4), b1 = *(const f32x4*)(c.smem + L_PAR + 512 + ch * 4 + 16);
#pragma unroll
  for (int r = 0; r < 3; ++r) {
    const u32x2 lo = act4_scalar(u32x2{v[r][0], v[r][1]}, a0, b0), hi = act4_scalar(u32x2{v[r][2], v[r][3]}, a1, b1);
    *(u32x4*)(c.smem + (ht.off[r] ^ (k << 6))) = u32x4{lo[0], lo[1], hi[0], hi[1]};
  }
}

DI void xraw_issue(const Ctx& c) {
  int xs = c.x0 - 8 + 8 * (c.lane & 3);
  { const int lo = c.x0 & 64; xs = xs < lo ? lo : (xs > lo + 56 ? lo + 56 : xs); }
  const unsigned vo = (unsigned)(((c.b * 128 + 32 * c.w + (c.lane >> 2)) * 128) * 128 + xs) * 2;
#pragma unroll
  for (int row = 0; row < 10; ++row) {
    const int y = row == 0 ? (c.ty == 0 ? 0 : c.y0 - 1) : row == 9 ? (c.ty == 15 ? 127 : c.y0 + 8) : c.y0 - 1 + row;
#pragma unroll
    for (int cbl = 0; cbl < 2; ++cbl)
      __builtin_amdgcn_raw_ptr_buffer_load_lds(c.rx, (LAS void*)(c.smem + L_RAW + row * RS + (2 * c.w + cbl) * 1024), 16, vo, y * 256 + cbl * 16 * 32768, 0, AUX_DMA);
  }
}
template <int RPAIR>
DI void xraw_step(const Ctx& c, unsigned ra, unsigned rh, const unsigned (&wa)[4], const unsigned (&wh)[2], bool rowok, bool hok) {
  u32x2 v[6];
  constexpr int RO = (RPAIR & 1) * 2 * RS;
  const unsigned ra2 = ra + (RPAIR >> 1) * 4 * RS, rh2 = rh + (RPAIR >> 1) * 4 * RS;
  asm volatile("s_waitcnt vmcnt(%8)\n\t"
               "ds_read_b64_tr_b16 %0, %6 offset:%9\n\tds_read_b64_tr_b16 %1, %6 offset:%10\n\tds_read_b64_tr_b16 %2, %6 offset:%11\n\tds_read_b64_tr_b16 %3, %6 offset:%12\n\t"
               "ds_read_b64_tr_b16 %4, %7 offset:%9\n\tds_read_b64_tr_b16 %5, %7 offset:%13\n\ts_waitcnt lgkmcnt(0)"
               : "=&v"(v[0]), "=&v"(v[1]), "=&v"(v[2]), "=&v"(v[3]), "=&v"(v[4]), "=&v"(v[5])
               : "v"(ra2), "v"(rh2), "i"(16 - 4 * RPAIR), "i"(RO), "i"(RO + 512), "i"(RO + 1024), "i"(RO + 1536), "i"(RO + 1024) : "memory");
  if (rowok) {
#pragma unroll
    for (int a = 0; a < 4; ++a) asm volatile("ds_write_b64 %0, %1 offset:%2" :: "v"(wa[a]), "v"(v[a]), "i"(RPAIR * 2 * RP) : "memory");
  }
  if (hok) {
#pragma unroll
    for (int hb = 0; hb < 2; ++hb) asm volatile("ds_write_b64 %0, %1 offset:%2" :: "v"(wh[hb]), "v"(v[4 + hb]), "i"(RPAIR * 2 * RP) : "memory");
  }
}
DI void xraw_transpose(const Ctx& c) {
  const unsigned lb = (unsigned)(size_t)(LAS char*)c.smem;
  const int gsel = c.lane >> 4, i = c.lane & 15, qp = i >> 2, p = i & 3;
  const unsigned ra = lb + L_RAW + (gsel & 1) * RS + (4 * (8 * c.w + (gsel >> 1)) + qp) * 64 + 16 + p * 8;
  unsigned wa[4];
#pragma unroll
  for (int a = 0; a < 4; ++a) wa[a] = lb + L_T + (gsel & 1) * RP + (i + 1) * 256 + (((4 * c.w + a) ^ swz((i + 1) & 15)) << 4) + (gsel >> 1) * 8;
  const unsigned rh = lb + L_RAW + (p >> 1) * RS + (4 * (8 * c.w + gsel) + qp) * 64 + ((p & 1) ? 48 : 8);
  const int side = qp & 1, hrow = qp >> 1, hcol = side ? 17 : 0;
  unsigned wh[2];
#pragma unroll
  for (int hb = 0; hb < 2; ++hb) wh[hb] = lb + L_T + hrow * RP + hcol * 256 + (((4 * c.w + 2 * hb + (gsel >> 1)) ^ swz(hcol & 15)) << 4) + (gsel & 1) * 8;
  const bool huse = (side ? p == 0 : p == 3) && (side ? (c.tx != 7 && c.tx != 3) : (c.tx != 0 && c.tx != 4));
  const bool top = c.ty == 0, bot = c.ty == 15;
  xraw_step<0>(c, ra, rh, wa, wh, !(top && (gsel & 1) == 0), huse && !(top && hrow == 0));
  xraw_step<1>(c, ra, rh, wa, wh, true, huse);
  xraw_step<2>(c, ra, rh, wa, wh, true, huse);
  xraw_step<3>(c, ra, rh, wa, wh, true, huse);
  xraw_step<4>(c, ra, rh, wa, wh, !(bot && (gsel & 1) == 1), huse && !(bot && hrow == 1));
}
struct Halo { unsigned go[4], lo[4]; bool inr[4], valid[4]; };
DI void halo_init(const Ctx& c, Halo& h) {
  const int tid = c.t - 256, cg = tid & 15;
#pragma unroll
  for (int r = 0; r < 4; ++r) {
    const int hp = (tid >> 4) + 16 * r;
    int row, col;
    if (hp < 18) { row = 0; col = hp; } else if (hp < 36) { row = 9; col = hp - 18; } else { const int j = hp - 36; row = 1 + (j >> 1); col = (j & 1) ? 17 : 0; }
    h.inr[r] = hp < 52;
    h.valid[r] = h.inr[r] && (unsigned)(c.y0 - 1 + row) < 128u && (unsigned)(c.x0 - 1 + col) < 128u;
    h.go[r] = WS_HB + (unsigned)(((c.b * 128 + c.y0 - 1 + row) * 128 + c.x0 - 1 + col) * 256 + cg * 16);
    h.lo[r] = L_T + row * RP + col * 256 + ((cg ^ swz(col & 15)) << 4);
  }
}
struct Clr { unsigned o[3]; };
DI void clear_init(const Ctx& c, Clr& cl) {
#pragma unroll
  for (int r = 0; r < 3; ++r) {
    int id = c.t - 256 + 256 * r; id = id < 704 ? id : id - 256;
    const int i = id >> 4;
    int ry, rx;
    if (i < 16) { ry = 0; rx = i; } else if (i < 32) { ry = 7; rx = i - 16; } else { const int j = i - 32; ry = 1 + (j >> 1); rx = (j & 1) ? 15 : 0; }
    cl.o[r] = WS_HB + (unsigned)(((c.b * 128 + c.y0 + ry) * 128 + c.x0 + rx) * 256 + (id & 15) * 16);
    asm volatile("" : "+v"(cl.o[r]));
  }
}
DI void clear_borders(const Ctx& c, const Clr& cl) {
  const u32x4 s = {~0u, ~0u, ~0u, ~0u};
#pragma unroll
  for (int r = 0; r < 3; ++r) bst_sc1(s, c.rw, cl.o[r], 0);
}
DI void stats_to_lds(const Ctx& c, int g, const u32x4& v0, const u32x4& v1) {
  const float s = wave_sum(__uint_as_float(v0[0]) + __uint_as_float(v1[0])), q = wave_sum(__uint_as_float(v0[2]) + __uint_as_float(v1[2]));
  const float mean = s * (1.f / 524288.f); float var = q * (1.f / 524288.f) - mean * mean; var = var < 0.f ? 0.f : var;
  if (c.lane == 0) *(f32x2*)(c.smem + L_STAT + g * 8) = f32x2{mean, __builtin_amdgcn_rsqf(var + 1e-5f)};
}

#define WLD bld_sc1
template <int CONV>
DI void mainloop(const Ctx& c, f32x4 (&acc)[8][2], bf16x8 (&wr)[WD], unsigned wvoff, const f32x4 (&ini)[2]) {
  const char* T = c.smem + L_T;
  const int px = c.lane & 15, q = c.lane >> 4;
  const int colP = px + (q & 1), colD = px + 2;
  const unsigned baseP = colP * 256 + (((q >> 1) ^ swz(colP & 15)) << 4), baseD = colD * 256 + ((q ^ swz(colD & 15)) << 4);
  auto rd = [&](int B, int r) -> bf16x8 {
    const int sp = B / 3, ty = B % 3;
    const unsigned a = ty == 2 ? (baseD ^ (sp << 6)) : (baseP ^ ((2 * sp + ty) << 5));
    return *(const bf16x8*)(T + a + r * RP);
  };
  bf16x8 xf[4];
#pragma unroll
  for (int r = 0; r < 3; ++r) xf[r] = rd(0, r);
#pragma unroll
  for (int B = 0; B < 12; ++B) {
#pragma unroll
    for (int r = 0; r < 10; ++r) {
      const int G = B * 10 + r;
      if (r == 7 && (B % 3) == 2 && B < 11) { bar_nowait(); SB(); }
      { const int Gn = G + 3; if (Gn < 120) xf[Gn & 3] = rd(Gn / 10, Gn % 10); }
#pragma unroll
      for (int dy = 0; dy < 3; ++dy) {
        const int row = r - dy;
        if (row >= 0 && row < 8) {
#pragma unroll
          for (int nt = 0; nt < 2; ++nt) {
            const int ts = 6 * B + 2 * dy + nt;
            if (CONV == 1) asm volatile("v_mfma_f32_16x16x32_bf16 %0, %1, %2, %0" : "+v"(acc[row][nt]) : "v"(xf[G & 3]), "v"(wr[ts % WD]));
            else if (B == 0 && dy == 0) asm volatile("v_mfma_f32_16x16x32_bf16 %0, %1, %2, %3" : "=&v"(acc[row][nt]) : "v"(wr[ts % WD]), "v"(xf[G & 3]), "v"(ini[nt]));
            else           asm volatile("v_mfma_f32_16x16x32_bf16 %0, %1, %2, %0" : "+v"(acc[row][nt]) : "v"(wr[ts % WD]), "v"(xf[G & 3]));
            if (row == 7) { const int tn = CONV * 72 + ts + WD; if (tn < 144) wr[ts % WD] = __builtin_bit_cast(bf16x8, WLD(c.rw, wvoff, (unsigned)tn * 1024 + (tn >= 72 ? 294912u - 72u * 1024u : 0u))); }
            SB();
          }
        }
      }
    }
  }
  asm volatile("s_nop 15" ::: "memory");
  SB();
}

DI void pin_acc(f32x4 (&acc)[8][2]) {
#pragma unroll
  for (int r = 0; r < 8; ++r) asm volatile("" : "+v"(acc[r][0]), "+v"(acc[r][1]));
}
__global__ __launch_bounds__(512, 2) void k_resblock(const u16* __restrict__ x, const u16* __restrict__ t_emb, const u16* __restrict__ g1, const u16* __restrict__ be1,
                                                     const u16* __restrict__ w1, const u16* __restrict__ w_emb, const u16* __restrict__ g2, const u16* __restrict__ be2,
                                                     const u16* __restrict__ w2, u16* __restrict__ out, char* __restrict__ ws) {
  extern __shared__ __attribute__((aligned(256))) char smem[];
  Ctx c;
  c.smem = smem; c.ws = ws;
  c.t = threadIdx.x; c.lane = c.t & 63; c.w = __builtin_amdgcn_readfirstlane(c.t >> 6);
  const int xcd = blockIdx.x & 7, jx = blockIdx.x >> 3;
  c.b = xcd >> 2; c.ty = (xcd & 3) * 4 + (jx >> 3); c.tx = jx & 7;
  c.jb = c.ty * 8 + c.tx; c.y0 = c.ty * 8; c.x0 = c.tx * 16;
  c.rx = mk_rsrc(x, 8u << 20); c.rw = mk_rsrc(ws, 16u << 20);
  c.wb = (unsigned)c.b * WB_SIZE;
  const unsigned epoch = __hip_atomic_load((GAS unsigned*)(ws + c.wb + WB_EPOCH), __ATOMIC_RELAXED, __HIP_MEMORY_SCOPE_AGENT);

  if (c.w < 4) {
    STAMP_DECL STAMP(0);
    const int ch = c.t & 127;
    const unsigned gbA = (c.t < 128 ? g1 : be1)[ch], gbB = (c.t < 128 ? g2 : be2)[ch];
    SB();
    bar_nowait(); SB();
    STAMP(14);
    xraw_issue(c); SB();
    STAMP(15);
    Task0 t0; task0_init(c, t0);
    xraw_transpose(c); SB();
    c.tag = __builtin_amdgcn_readfirstlane(epoch) + 1u;
    { float* gbp = (float*)(smem + L_GB) + (c.t < 128 ? 0 : 128) + ch; gbp[0] = bflo(gbA); gbp[256] = bflo(gbB); }
    STAMP(3);
    {
      const unsigned fo = c.wb + WB_FLAG + (unsigned)((((c.lane >> 3) & 1) * 4 + c.w) * 128 + (c.lane & 7) * 4);
      for (int spin = 0; spin < SPIN_MAX; ++spin) {
        cbar();
        const unsigned f = __builtin_amdgcn_raw_buffer_load_b32(c.rw, fo, 0, SC1);
        if (!__any(f != c.tag)) break;
        __builtin_amdgcn_s_sleep(2);
      }
    }
    STAMP(4);
    const unsigned wvoff = c.wb + WB_WF + (unsigned)c.w * 73728u + (unsigned)c.lane * 16u;
    bf16x8 wr[WD];
#pragma unroll
    for (int k = 0; k < WD; ++k) wr[k] = __builtin_bit_cast(bf16x8, bld_sc1(c.rw, wvoff, (unsigned)k * 1024));
    const int px = c.lane & 15, q = c.lane >> 4;
    bar();
    STAMP(5);
    f32x4 acc[8][2], ini[2];
    ini[0] = *(const f32x4*)(smem + L_EMB + (32 * c.w + 4 * q) * 4); ini[1] = *(const f32x4*)(smem + L_EMB + (32 * c.w + 16 + 4 * q) * 4);
    norm_group0(c, t0, 0);
    bar();
    STAMP(6);
    __builtin_amdgcn_s_setprio(1);
    mainloop<0>(c, acc, wr, wvoff, ini);
    __builtin_amdgcn_s_setprio(0);
    STAMP(7);
    {
      f32x2 s2 = {0.f, 0.f}, q2 = {0.f, 0.f};
#pragma unroll
      for (int r = 0; r < 8; ++r)
#pragma unroll
        for (int nt = 0; nt < 2; ++nt)
#pragma unroll
          for (int k = 0; k < 2; ++k) { const f32x2 v = {acc[r][nt][2 * k], acc[r][nt][2 * k + 1]}; s2 += v; q2 += v * v; }
      const float s = wave_sum(s2[0] + s2[1]), qs = wave_sum(q2[0] + q2[1]);
      if (c.lane == 0) bst_sc1(u32x4{__float_as_uint(s), c.tag, __float_as_uint(qs), c.tag}, c.rw, c.wb + WB_G2 + (unsigned)(c.w * 128 + c.jb) * 16, 0);
    }
    STAMP(1);
    bar_nowait();
    {
      const int chk = 4 * c.w + (((q & 1) << 1) | (q >> 1));
      const unsigned go = WS_HB + (unsigned)(((c.b * 128 + c.y0) * 128 + c.x0 + px) * 256 + chk * 16);
      char* lp = smem + L_T + RP + (px + 1) * 256 + ((chk ^ swz((px + 1) & 15)) << 4);
      const bool edge = px == 0 || px == 15;
#pragma unroll
      for (int rr = 0; rr < 8; ++rr) {
        const int r = rr == 0 ? 0 : (rr == 1 ? 7 : rr - 1);
        const unsigned d0 = cvtpk(acc[r][0][0], acc[r][0][1]), d1 = cvtpk(acc[r][0][2], acc[r][0][3]);
        const unsigned d2 = cvtpk(acc[r][1][0], acc[r][1][1]), d3 = cvtpk(acc[r][1][2], acc[r][1][3]);
        auto a = __builtin_amdgcn_permlane16_swap(d0, d2, false, false);
        auto e = __builtin_amdgcn_permlane16_swap(d1, d3, false, false);
        const u32x4 v = {a[0], e[0], a[1], e[1]};
        if (r == 0 || r == 7 || edge) bst_sc1(u32x4{min(v[0], 0xfffffffeu), min(v[1], 0xfffffffeu), min(v[2], 0xfffffffeu), min(v[3], 0xfffffffeu)}, c.rw, go, r * 32768);
        *(u32x4*)(lp + r * RP) = v;
      }
    }
    bar();
    STAMP(8);
#pragma unroll
    for (int r = 0; r < 8; ++r) {
      const u32x4 v = *(const u32x4*)(smem + L_RAW + (r + 1) * RS + (32 * c.w + 16 * (q & 1) + px) * 64 + 16 + (q >> 1) * 16);
      auto a = __builtin_amdgcn_permlane16_swap(v[0], v[2], false, false);
      auto e = __builtin_amdgcn_permlane16_swap(v[1], v[3], false, false);
      acc[r][0] = f32x4{bflo(a[0]), bfhi(a[0]), bflo(e[0]), bfhi(e[0])};
      acc[r][1] = f32x4{bflo(a[1]), bfhi(a[1]), bflo(e[1]), bfhi(e[1])};
    }
    pin_acc(acc);
    STAMP(9);
    bar();
    STAMP(10);
    norm_group0(c, t0, 1);
    bar();
    STAMP(11);
    __builtin_amdgcn_s_setprio(1);
    mainloop<1>(c, acc, wr, wvoff, ini);
    __builtin_amdgcn_s_setprio(0);
    STAMP(12);
    u16* op = out + ((size_t)(c.b * 128 + 32 * c.w + 16 * (q & 1) + px) * 128 + c.y0) * 128 + c.x0 + 8 * (q >> 1);
#pragma unroll
    for (int r = 0; r < 8; ++r) {
      const unsigned d0 = cvtpk(acc[r][0][0], acc[r][0][1]), d1 = cvtpk(acc[r][0][2], acc[r][0][3]);
      const unsigned d2 = cvtpk(acc[r][1][0], acc[r][1][1]), d3 = cvtpk(acc[r][1][2], acc[r][1][3]);
      auto a = __builtin_amdgcn_permlane16_swap(d0, d2, false, false);
      auto e = __builtin_amdgcn_permlane16_swap(d1, d3, false, false);
      *(u32x4*)(op + r * 128) = u32x4{a[0], e[0], a[1], e[1]};
    }
    STAMP(13); STAMP_FLUSH;
  } else {
    STAMP_DECL STAMP(0);
    const int g = c.w - 4;
    const int yrow = (xcd & 3) * 32 + jx;
    __builtin_amdgcn_s_setprio(2);
    const int ksvc = jx * 4 + (xcd & 3);
    const bool svc_w = c.w == 7 && (ksvc < 32 || (ksvc >= 64 && ksvc < 96)), svc_e = c.w == 7 && ksvc >= 32 && ksvc < 64;
    const int wu = ksvc < 32 ? ksvc : ksvc - 32;
    u32x4 sv[9]; unsigned te[6];
    if (svc_w) {
      const u32x4* src = (const u32x4*)(((wu >> 5) ? w2 : w1) + (size_t)((32 * ((wu >> 3) & 3) + (c.lane & 31)) * 128 + 16 * (wu & 7) + 8 * (c.lane >> 5)) * 9);
#pragma unroll
      for (int k = 0; k < 9; ++k) sv[k] = gld16(src + k);
    }
    if (svc_e) {
      const int j = ksvc - 32, e0 = 192 * (j >> 3) + (c.lane >> 1);
#pragma unroll
      for (int k = 0; k < 6; ++k) { sv[k] = gld16(w_emb + (size_t)(e0 + 32 * k) * 128 + 16 * (j & 7) + 8 * (c.lane & 1)); te[k] = t_emb[c.b * 768 + e0 + 32 * k]; }
    }
    SB();
    u32x4 xr[8];
    { const unsigned o = (unsigned)(((c.b * 128 + 32 * g + 8 * (c.lane >> 4)) * 128 + yrow) * 128) * 2 + (c.lane & 15) * 16;
#pragma unroll
      for (int jj = 0; jj < 8; ++jj) xr[jj] = bld_row(c.rx, o, jj * 32768); }
    SB();
    bar_nowait(); SB();
    STAMP(4);
    c.tag = __builtin_amdgcn_readfirstlane(epoch) + 1u;
    {
      float s = 0.f, qs = 0.f;
#pragma unroll
      for (int jj = 0; jj < 8; ++jj)
#pragma unroll
        for (int k = 0; k < 4; ++k) { s = dot2(xr[jj][k], 0x3f803f80u, s); qs = dot2(xr[jj][k], xr[jj][k], qs); }
      s = wave_sum(s); qs = wave_sum(qs);
      if (c.lane == 0) bst_sc1(u32x4{__float_as_uint(s), c.tag, __float_as_uint(qs), c.tag}, c.rw, c.wb + WB_G1 + (unsigned)(g * 128 + c.jb) * 16, 0);
    }
    {
      const int k16 = c.lane & 15;
      if (k16 == 7 || k16 == 8) {
        unsigned v[4];
#pragma unroll
        for (int m = 0; m < 4; ++m) {
          const unsigned a = __builtin_amdgcn_perm(xr[2 * m + 1][3], xr[2 * m][3], 0x07060302u), b2 = __builtin_amdgcn_perm(xr[2 * m + 1][0], xr[2 * m][0], 0x05040100u);
          v[m] = k16 == 7 ? a : b2;
        }
        const unsigned o = c.wb + WB_XC + (unsigned)((yrow * 2 + (k16 - 7)) * 64 + 16 * g + 4 * (c.lane >> 4)) * 8;
        bst_sc1(u32x4{v[0], c.tag, v[1], c.tag}, c.rw, o, 0); bst_sc1(u32x4{v[2], c.tag, v[3], c.tag}, c.rw, o, 16);
      }
    }
    STAMP(1);
    if (svc_w) {
      const int cs = wu & 7, o = c.lane & 31, h = c.lane >> 5;
      const unsigned dst = c.wb + WB_WF + (unsigned)(wu >> 5) * 294912u + (unsigned)((wu >> 3) & 3) * 73728u + (unsigned)(o >> 4) * 1024u + (unsigned)(o & 15) * 16u;
      const unsigned dP = dst + (unsigned)(3 * (cs >> 1) + (cs & 1)) * 6144u + (unsigned)h * 512u;
      const unsigned dD = dst + (unsigned)(3 * (cs >> 1) + 2) * 6144u + (unsigned)(2 * (cs & 1) + h) * 256u;
#pragma unroll
      for (int k = 0; k < 9; ++k) {
        const int dy = k / 3, dx = k % 3;
        u32x4 o4;
#pragma unroll
        for (int m = 0; m < 4; ++m) {
          const int e0 = 18 * m + k, e1 = e0 + 9;
          const unsigned sel = ((e0 & 1) ? 0x0302u : 0x0100u) | ((e1 & 1) ? 0x07060000u : 0x05040000u);
          o4[m] = __builtin_amdgcn_perm(sv[e1 >> 3][(e1 >> 1) & 3], sv[e0 >> 3][(e0 >> 1) & 3], sel);
        }
        bst_sc1(o4, c.rw, dx < 2 ? dP : dD, dy * 2048 + (dx < 2 ? dx * 256 : 0));
      }
      asm volatile("s_waitcnt vmcnt(0)" ::: "memory");
      if (c.lane == 0) __hip_atomic_store((GAS unsigned*)(ws + c.wb + WB_FLAG + (wu >> 3) * 128 + (wu & 7) * 4), c.tag, __ATOMIC_RELAXED, __HIP_MEMORY_SCOPE_AGENT);
    }
    if (svc_e) {
      f32x2 a[4];
#pragma unroll
      for (int m = 0; m < 4; ++m) a[m] = f32x2{0.f, 0.f};
#pragma unroll
      for (int k = 0; k < 6; ++k) {
        const float tv = bflo(te[k]);
        const float sl = tv * __builtin_amdgcn_rcpf(1.f + __builtin_amdgcn_exp2f(-tv * LOG2E));
#pragma unroll
        for (int m = 0; m < 4; ++m) a[m] += f32x2{bflo(sv[k][m]), bfhi(sv[k][m])} * sl;
      }
      float r[8];
#pragma unroll
      for (int m = 0; m < 8; ++m) {
        float v = a[m >> 1][m & 1];
        v = dppadd<0x4E>(v); v = dppadd<0x124>(v); v = dppadd<0x128>(v);
        auto p = __builtin_amdgcn_permlane16_swap(__float_as_uint(v), __float_as_uint(v), false, false);
        v = __uint_as_float(p[0]) + __uint_as_float(p[1]);
        auto p2 = __builtin_amdgcn_permlane32_swap(__float_as_uint(v), __float_as_uint(v), false, false);
        r[m] = __uint_as_float(p2[0]) + __uint_as_float(p2[1]);
      }
      const int j = ksvc - 32;
      if (c.lane < 2) {
        const unsigned o = c.wb + WB_EG + (unsigned)((j >> 3) * 128 + 16 * (j & 7) + 8 * c.lane) * 8;
#pragma unroll
        for (int m = 0; m < 4; ++m) bst_sc1(u32x4{__float_as_uint(r[2 * m]), c.tag, __float_as_uint(r[2 * m + 1]), c.tag}, c.rw, o, m * 16);
      }
    }
    __builtin_amdgcn_s_setprio(0);
    STAMP(2);
    Halo hl; halo_init(c, hl);
#pragma unroll
    for (int r = 0; r < 4; ++r) if (hl.inr[r] && !hl.valid[r]) *(u32x4*)(smem + hl.lo[r]) = u32x4{0, 0, 0, 0};
    Task0 t0; task0_init(c, t0);
    HTask ht; htask_init(c, ht);
    Clr cl; clear_init(c, cl);
    STAMP(3);
    {
      u32x4 v0, v1, ve[4], vc[2]; bool n0 = true, n1 = true, ne = c.w == 4, nc[2];
      const unsigned o = c.wb + WB_G1 + (unsigned)(g * 128 + c.lane) * 16;
      const bool xc = c.tx == 3 || c.tx == 4;
      unsigned co[2], cl[2]; bool cin[2];
#pragma unroll
      for (int r = 0; r < 2; ++r) {
        const int id = c.t - 256 + 256 * r, row = id >> 5, i = id & 31, y = c.y0 - 1 + row, col = c.tx == 3 ? 17 : 0;
        cin[r] = xc && id < 320;
        nc[r] = cin[r] && (unsigned)y < 128u;
        co[r] = c.wb + WB_XC + (unsigned)((y * 2 + (c.tx == 3 ? 1 : 0)) * 64 + 2 * i) * 8;
        cl[r] = L_T + row * RP + col * 256 + (((i >> 1) ^ swz(col & 15)) << 4) + (i & 1) * 8;
        vc[r] = u32x4{0, 0, 0, 0};
      }
      for (int spin = 0; spin < SPIN_MAX; ++spin) {
        cbar();
        if (n0) v0 = bld_sc1(c.rw, o, 0);
        if (n1) v1 = bld_sc1(c.rw, o, 1024);
        if (ne) {
#pragma unroll
          for (int k = 0; k < 4; ++k) ve[k] = bld_sc1(c.rw, c.wb + WB_EG + (unsigned)c.lane * 16, k * 1024);
        }
#pragma unroll
        for (int r = 0; r < 2; ++r) if (nc[r]) vc[r] = bld_sc1(c.rw, co[r], 0);
        n0 = !(v0[1] == c.tag && v0[3] == c.tag); n1 = !(v1[1] == c.tag && v1[3] == c.tag);
        if (ne) { bool ok = true;
#pragma unroll
          for (int k = 0; k < 4; ++k) ok = ok && ve[k][1] == c.tag && ve[k][3] == c.tag;
          ne = !ok; }
#pragma unroll
        for (int r = 0; r < 2; ++r) nc[r] = nc[r] && !(vc[r][1] == c.tag && vc[r][3] == c.tag);
        if (!__any(n0 || n1 || ne || nc[0] || nc[1])) break;
        __builtin_amdgcn_s_sleep(2);
      }
      stats_to_lds(c, g, v0, v1);
      if (c.w == 4) *(f32x2*)(smem + L_EMB + c.lane * 8) = f32x2{(__uint_as_float(ve[0][0]) + __uint_as_float(ve[1][0])) + (__uint_as_float(ve[2][0]) + __uint_as_float(ve[3][0])),
                                                               (__uint_as_float(ve[0][2]) + __uint_as_float(ve[1][2])) + (__uint_as_float(ve[2][2]) + __uint_as_float(ve[3][2]))};
#pragma unroll
      for (int r = 0; r < 2; ++r) if (cin[r]) *(u32x2*)(smem + cl[r]) = u32x2{vc[r][0], vc[r][2]};
    }
    STAMP(6);
    bar();
    if (c.t == 256 && c.jb == 0) __hip_atomic_store((GAS unsigned*)(ws + c.wb + WB_EPOCH), c.tag, __ATOMIC_RELAXED, __HIP_MEMORY_SCOPE_AGENT);
    norm_group0(c, t0, 0);
    bar();
#pragma unroll 1
    for (int k = 1; k < 4; ++k) {
      if (k == 2) clear_borders(c, cl);
      norm_group_h(c, ht, k);
      if (k == 2) asm volatile("s_waitcnt vmcnt(0)" ::: "memory");
      bar();
      if (k == 2 && c.t == 256) __hip_atomic_store((GAS unsigned*)(ws + c.wb + WB_CF + c.jb * 4), c.tag, __ATOMIC_RELAXED, __HIP_MEMORY_SCOPE_AGENT);
    }
    STAMP(7);
    {
      if (c.w == 5) {
        const int dy = (c.lane & 15) / 3 - 1, dx = (c.lane & 15) % 3 - 1;
        const int ny = c.ty + dy, nx = c.tx + dx;
        const bool chk = c.lane < 9 && (unsigned)ny < 16u && (unsigned)nx < 8u;
        for (int spin = 0; spin < SPIN_MAX; ++spin) {
          cbar();
          unsigned f = c.tag;
          if (chk) f = __builtin_amdgcn_raw_buffer_load_b32(c.rw, c.wb + WB_CF + (unsigned)(ny * 8 + nx) * 4, 0, SC1);
          if (!__any(f != c.tag)) break;
          __builtin_amdgcn_s_sleep(2);
        }
      }
      u32x4 hv[4]; bool need[4];
#pragma unroll
      for (int r = 0; r < 4; ++r) { need[r] = hl.valid[r]; hv[r] = u32x4{0, 0, 0, 0}; }
      u32x4 v0, v1; bool n0 = true, n1 = true;
      const unsigned o = c.wb + WB_G2 + (unsigned)(g * 128 + c.lane) * 16;
      bar_nowait();
      bar();
      STAMP(13);
      for (int spin = 0; spin < SPIN_MAX; ++spin) {
        cbar();
#pragma unroll
        for (int r = 0; r < 4; ++r) if (need[r]) hv[r] = bld_sc1(c.rw, hl.go[r], 0);
        if (n0) v0 = bld_sc1(c.rw, o, 0);
        if (n1) v1 = bld_sc1(c.rw, o, 1024);
        bool any = false;
#pragma unroll
        for (int r = 0; r < 4; ++r) { need[r] = need[r] && (hv[r][0] == ~0u || hv[r][1] == ~0u || hv[r][2] == ~0u || hv[r][3] == ~0u); any |= need[r]; }
        n0 = !(v0[1] == c.tag && v0[3] == c.tag); n1 = !(v1[1] == c.tag && v1[3] == c.tag);
        if (!__any(any || n0 || n1)) break;
        __builtin_amdgcn_s_sleep(1);
      }
      STAMP(8);
#pragma unroll
      for (int r = 0; r < 4; ++r) if (hl.valid[r]) *(u32x4*)(smem + hl.lo[r]) = hv[r];
      stats_to_lds(c, g, v0, v1);
    }
    bar();
    norm_group0(c, t0, 1);
    bar();
#pragma unroll 1
    for (int k = 1; k < 4; ++k) { if (k == 1) STAMP(9); norm_group_h(c, ht, k); if (k == 1) STAMP(10); if (k == 2) STAMP(11); bar(); }
    STAMP(12); STAMP_FLUSH;
  }
}

extern "C" void kernel_launch(void* const* d_in, const int* in_sizes, int n_in, void* d_out, int out_size, void* d_ws, size_t ws_size, hipStream_t stream) {
  (void)hipFuncSetAttribute((const void*)k_resblock, hipFuncAttributeMaxDynamicSharedMemorySize, L_END);
  k_resblock<<<256, 512, L_END, stream>>>((const u16*)d_in[0], (const u16*)d_in[1], (const u16*)d_in[2], (const u16*)d_in[3], (const u16*)d_in[4],
                                          (const u16*)d_in[5], (const u16*)d_in[6], (const u16*)d_in[7], (const u16*)d_in[8], (u16*)d_out, (char*)d_ws);
}
```

```cpp
#define DIAG 1
#define DIAG_WG 100
#define DIAG_WAVE 0
#define DIAG_A 0
#define DIAG_B 13
#define DIAG_SCALE 0
#define DIAG_CYC 0
#define NO_CODE_PF
#include <hip/hip_runtime.h>
#include <hip/hip_bf16.h>
#include <stdint.h>

typedef float f32x4 __attribute__((ext_vector_type(4)));
typedef float f32x2 __attribute__((ext_vector_type(2)));
typedef __bf16 bf16x8 __attribute__((ext_vector_type(8)));
typedef __bf16 bf16x2 __attribute__((ext_vector_type(2)));
typedef unsigned u32x4 __attribute__((ext_vector_type(4)));
typedef unsigned u32x2 __attribute__((ext_vector_type(2)));
typedef unsigned short u16;
#define GAS __attribute__((address_space(1)))
#define LAS __attribute__((address_space(3)))
#define DI __device__ __forceinline__
#define SB() __builtin_amdgcn_sched_barrier(0)
#define PIN(x) asm volatile("" : "+v"(x))

constexpr int RP = 4608;
constexpr int TILE_BYTES = 10 * RP;
constexpr int RS = 8224;
constexpr int WD = 18;
constexpr float LOG2E = 1.4426950408889634f;
constexpr int SC1 = 16, NT = 2;
constexpr int SPIN_MAX = 1 << 16;
constexpr unsigned CODE_PF_BYTES = 35u * 1024u;
#define POLL_SLEEP 4
#define POLL_SLEEP2 2
#define AUX_SVC 0
#define AUX_ROW 2

constexpr unsigned WB_SIZE = 1u << 20;
constexpr unsigned WB_WF = 0, WB_EPOCH = 589824, WB_FLAG = WB_EPOCH + 256  , WB_G1 = WB_FLAG + 1024, WB_G2 = WB_G1 + 8192,
                   WB_EG = WB_G2 + 8192  , WB_CF = WB_EG + 4096  , WB_XC = WB_CF + 512  ;
constexpr unsigned WS_HB = 2u << 20;
constexpr int L_T = 0, L_RAW = TILE_BYTES, L_GB = L_RAW + 10 * RS  , L_PAR = L_GB + 2048  , L_EMB = L_PAR + 1024,
              L_STAT = L_EMB + 512  , L_RED = L_STAT + 32  , L_DUMMY = (L_RED + 32 + 255) / 256 * 256  , L_SVC = L_DUMMY + 1024  ,
              L_END = L_SVC + 9504;

DI int swz(int c) { return c ^ ((c >> 1) & 4); }
DI unsigned cvtpk(float lo, float hi) { f32x2 v = {lo, hi}; bf16x2 b = __builtin_convertvector(v, bf16x2); return __builtin_bit_cast(unsigned, b); }
DI unsigned cvtpk_s(float lo, float hi) { unsigned r; asm("v_cvt_pk_bf16_f32 %0, %1, %2" : "=v"(r) : "v"(lo), "v"(hi)); return r; }
DI float bflo(unsigned d) { return __uint_as_float(d << 16); }
DI float bfhi(unsigned d) { return __uint_as_float(d & 0xffff0000u); }
DI float dot2(unsigned a, unsigned b, float c) { return __builtin_amdgcn_fdot2_f32_bf16(__builtin_bit_cast(bf16x2, a), __builtin_bit_cast(bf16x2, b), c, false); }
template <int CTRL> DI float dppadd(float v) { return v + __int_as_float(__builtin_amdgcn_update_dpp(0, __float_as_int(v), CTRL, 0xf, 0xf, true)); }
DI float rows_sum(float v) {
  auto p = __builtin_amdgcn_permlane16_swap(__float_as_uint(v), __float_as_uint(v), false, false);
  v = __uint_as_float(p[0]) + __uint_as_float(p[1]);
  auto q = __builtin_amdgcn_permlane32_swap(__float_as_uint(v), __float_as_uint(v), false, false);
  return __uint_as_float(q[0]) + __uint_as_float(q[1]);
}
DI float wave_sum(float v) { v = dppadd<0xB1>(v); v = dppadd<0x4E>(v); v = dppadd<0x141>(v); v = dppadd<0x140>(v); return rows_sum(v); }
DI void bar() { asm volatile("s_waitcnt lgkmcnt(0)\n\ts_barrier" ::: "memory"); }
DI void bar_nowait() { asm volatile("s_barrier" ::: "memory"); }
DI void cbar() { asm volatile("" ::: "memory"); }
typedef __amdgpu_buffer_rsrc_t rsrc_t;
DI rsrc_t mk_rsrc(const void* p, unsigned bytes) { return __builtin_amdgcn_make_buffer_rsrc((void*)p, 0, bytes, 0x00020000); }
template <int AUX> DI u32x4 bld(rsrc_t r, unsigned voff, unsigned soff) { return __builtin_amdgcn_raw_buffer_load_b128(r, voff, soff, AUX); }
DI void bst_sc1(u32x4 v, rsrc_t r, unsigned voff, unsigned off) { __builtin_amdgcn_raw_buffer_store_b128(v, r, voff + off, 0, SC1); }

struct Ctx {
  rsrc_t rx, rw;
  char* smem; char* ws;
  int b, ty, tx, jb, y0, x0, w, lane, t;
  unsigned wb;
  unsigned tag;
};

#define STAMP_DECL unsigned st_t[16]; _Pragma("unroll") for (int i_ = 0; i_ < 16; ++i_) st_t[i_] = 0;
#define STAMP(idx) do { if (idx == DIAG_A || idx == DIAG_B) { st_t[idx] = DIAG_CYC ? (unsigned)__builtin_amdgcn_s_memtime() : (unsigned)__builtin_amdgcn_s_memrealtime(); asm volatile("" : "+s"(st_t[idx])); } } while (0)
#define STAMP_FLUSH do { if (blockIdx.x == DIAG_WG && c.w == DIAG_WAVE) { const int val_ = (int)(st_t[DIAG_B] - st_t[DIAG_A]) * DIAG_SCALE / (DIAG_CYC ? 20 : 1); \
  const unsigned t0_ = (unsigned)__builtin_amdgcn_s_memrealtime(); while ((int)((unsigned)__builtin_amdgcn_s_memrealtime() - t0_) < val_) __builtin_amdgcn_s_sleep(1); } } while (0)

DI u32x2 act4_packed(u32x2 v, f32x4 a, f32x4 b) {
  u32x2 o;
#pragma unroll
  for (int m = 0; m < 2; ++m) {
    f32x2 xx = {bflo(v[m]), bfhi(v[m])}, aa = {a[2 * m], a[2 * m + 1]}, bb = {b[2 * m], b[2 * m + 1]};
    f32x2 u = xx * aa + bb;
    f32x2 e = {__builtin_amdgcn_exp2f(-u[0]), __builtin_amdgcn_exp2f(-u[1])};
    f32x2 d = e * LOG2E + LOG2E;
    f32x2 r = {__builtin_amdgcn_rcpf(d[0]), __builtin_amdgcn_rcpf(d[1])};
    f32x2 y = u * r;
    o[m] = cvtpk(y[0], y[1]);
  }
  return o;
}
DI float act1(float x, float a, float b) { float u = __builtin_fmaf(x, a, b); float e = __builtin_amdgcn_exp2f(-u); return u * __builtin_amdgcn_rcpf(__builtin_fmaf(e, LOG2E, LOG2E)); }
DI u32x2 act4_scalar(u32x2 v, f32x4 a, f32x4 b) {
  u32x2 o;
  o[0] = cvtpk_s(act1(bflo(v[0]), a[0], b[0]), act1(bfhi(v[0]), a[1], b[1]));
  o[1] = cvtpk_s(act1(bflo(v[1]), a[2], b[2]), act1(bfhi(v[1]), a[3], b[3]));
  return o;
}
DI bool in_image(const Ctx& c, int row, int col) { return ((unsigned)(c.y0 - 1 + row) < 128u) & ((unsigned)(c.x0 - 1 + col) < 128u); }
struct Tasks { unsigned off[3]; };
DI void tasks_init(const Ctx& c, Tasks& tk, int idx, int c4) {
  const unsigned lo = (c4 & 1) * 8, dummy = L_DUMMY + c.lane * 16 + lo;
  const int col = idx & 15, row = idx >> 4;
  const unsigned o0 = L_T + row * RP + col * 256 + (((c4 >> 1) ^ swz(col)) << 4) + lo;
  const int j = idx - 32, row2 = idx < 32 ? 8 + row : j >> 1, col2 = idx < 32 ? col : 16 + (j & 1);
  const unsigned o2 = L_T + row2 * RP + col2 * 256 + (((c4 >> 1) ^ swz(col2 & 15)) << 4) + lo;
  tk.off[0] = o0; tk.off[1] = o0 + 4 * RP; tk.off[2] = idx < 52 ? o2 : dummy;
  if ((c.ty == 0) | (c.ty == 15) | (c.tx == 0) | (c.tx == 7)) {
    tk.off[0] = in_image(c, row, col) ? tk.off[0] : dummy;
    tk.off[1] = in_image(c, row + 4, col) ? tk.off[1] : dummy;
    tk.off[2] = in_image(c, row2, col2) ? tk.off[2] : dummy;
  }
#pragma unroll
  for (int r = 0; r < 3; ++r) PIN(tk.off[r]);
}
typedef Tasks Task0;
DI void task0_init(const Ctx& c, Task0& t0) { tasks_init(c, t0, c.t >> 3, c.t & 7); }
DI void norm_group0(const Ctx& c, const Task0& t0, int gbsel) {
  u32x2 v[3];
#pragma unroll
  for (int r = 0; r < 3; ++r) v[r] = *(const u32x2*)(c.smem + t0.off[r]);
  const int ch = 4 * (c.t & 7);
  const f32x4 g = *(const f32x4*)(c.smem + L_GB + gbsel * 1024 + ch * 4), be = *(const f32x4*)(c.smem + L_GB + gbsel * 1024 + 512 + ch * 4);
  const f32x2 st = *(const f32x2*)(c.smem + L_STAT);
  const f32x4 a = g * st[1], b = be - a * st[0];
#pragma unroll
  for (int r = 0; r < 2; ++r) *(u32x2*)(c.smem + t0.off[r]) = act4_packed(v[r], a, b);
  if (c.w < 7) *(u32x2*)(c.smem + t0.off[2]) = act4_packed(v[2], a, b);
  else if (c.lane < 48) {
    const int c2 = 32 + 2 * c.lane;
    const f32x2 g2 = *(const f32x2*)(c.smem + L_GB + gbsel * 1024 + c2 * 4), b2 = *(const f32x2*)(c.smem + L_GB + gbsel * 1024 + 512 + c2 * 4);
    const f32x2 s2 = *(const f32x2*)(c.smem + L_STAT + (c2 >> 5) * 8);
    const f32x2 aa = g2 * s2[1];
    *(f32x2*)(c.smem + L_PAR + c2 * 4) = aa; *(f32x2*)(c.smem + L_PAR + 512 + c2 * 4) = b2 - aa * s2[0];
  }
}
typedef Tasks HTask;
DI void htask_init(const Ctx& c, HTask& ht) { tasks_init(c, ht, (c.t - 256) >> 2, 2 * (c.t & 3)); }
DI void norm_group_h(const Ctx& c, const HTask& ht, int k) {
  const int ch = 32 * k + 8 * (c.t & 3);
  u32x4 v[3];
#pragma unroll
  for (int r = 0; r < 3; ++r) v[r] = *(const u32x4*)(c.smem + (ht.off[r] ^ (k << 6)));
  const f32x4 a0 = *(const f32x4*)(c.smem + L_PAR + ch * 4), a1 = *(const f32x4*)(c.smem + L_PAR + ch * 4 + 16);
  const f32x4 b0 = *(const f32x4*)(c.smem + L_PAR + 512 + ch * 4), b1 = *(const f32x4*)(c.smem + L_PAR + 512 + ch * 4 + 16);
#pragma unroll
  for (int r = 0; r < 3; ++r) {
    const u32x2 lo = act4_scalar(u32x2{v[r][0], v[r][1]}, a0, b0), hi = act4_scalar(u32x2{v[r][2], v[r][3]}, a1, b1);
    *(u32x4*)(c.smem + (ht.off[r] ^ (k << 6))) = u32x4{lo[0], lo[1], hi[0], hi[1]};
  }
}

DI void xraw_issue(const Ctx& c, unsigned vo) {
#pragma unroll
  for (int row = 0; row < 10; ++row) {
    const int y = row == 0 ? (c.ty == 0 ? 0 : c.y0 - 1) : row == 9 ? (c.ty == 15 ? 127 : c.y0 + 8) : c.y0 - 1 + row;
    __builtin_amdgcn_raw_ptr_buffer_load_lds(c.rx, (LAS void*)(c.smem + L_RAW + row * RS + c.w * 1024), 16, vo, y * 256, 0, 0);
  }
}
struct XT { unsigned vo, ra, rh, wa[2], wh; bool huse, odd, hrow; };
DI void xraw_init(const Ctx& c, XT& x) {
  const unsigned lb = (unsigned)(size_t)(LAS char*)c.smem;
  int xs = c.x0 - 8 + 8 * (c.lane & 3);
  { const int lo = c.x0 & 64; xs = xs < lo ? lo : (xs > lo + 56 ? lo + 56 : xs); }
  x.vo = (unsigned)(((c.b * 128 + 16 * c.w + (c.lane >> 2)) * 128) * 128 + xs) * 2; PIN(x.vo);
  const int gsel = c.lane >> 4, i = c.lane & 15, qp = i >> 2, p = i & 3;
  x.ra = lb + L_RAW + (gsel & 1) * RS + (4 * (4 * c.w + (gsel >> 1)) + qp) * 64 + 16 + p * 8; PIN(x.ra);
#pragma unroll
  for (int a = 0; a < 2; ++a) { x.wa[a] = lb + L_T + (gsel & 1) * RP + (i + 1) * 256 + (((2 * c.w + a) ^ swz((i + 1) & 15)) << 4) + (gsel >> 1) * 8; PIN(x.wa[a]); }
  x.rh = lb + L_RAW + (p >> 1) * RS + (4 * (4 * c.w + gsel) + qp) * 64 + 8 + (p & 1) * 40; PIN(x.rh);
  const int side = qp & 1;
  x.wh = lb + L_T + (qp >> 1) * RP + side * 17 * 256 + (((2 * c.w + (gsel >> 1)) ^ side) << 4) + (gsel & 1) * 8; PIN(x.wh);
  const bool colok = side ? ((c.tx != 7) & (c.tx != 3)) : ((c.tx != 0) & (c.tx != 4));
  x.huse = (p == (side ? 0 : 3)) & colok; x.odd = gsel & 1; x.hrow = qp >> 1;
}
template <int RPAIR, int EXTRA>
DI void xraw_step(const XT& x, bool rowok, bool hok) {
  u32x2 v[3];
  constexpr int RO = (RPAIR & 1) * 2 * RS;
  const unsigned ra2 = x.ra + (RPAIR >> 1) * 4 * RS, rh2 = x.rh + (RPAIR >> 1) * 4 * RS;
  asm volatile("s_waitcnt vmcnt(%5)\n\t"
               "ds_read_b64_tr_b16 %0, %3 offset:%6\n\tds_read_b64_tr_b16 %1, %3 offset:%7\n\tds_read_b64_tr_b16 %2, %4 offset:%6\n\ts_waitcnt lgkmcnt(0)"
               : "=&v"(v[0]), "=&v"(v[1]), "=&v"(v[2])
               : "v"(ra2), "v"(rh2), "i"(8 - 2 * RPAIR + EXTRA), "i"(RO), "i"(RO + 512) : "memory");
  if (rowok) {
#pragma unroll
    for (int a = 0; a < 2; ++a) asm volatile("ds_write_b64 %0, %1 offset:%2" :: "v"(x.wa[a]), "v"(v[a]), "i"(RPAIR * 2 * RP) : "memory");
  }
  if (hok) asm volatile("ds_write_b64 %0, %1 offset:%2" :: "v"(x.wh), "v"(v[2]), "i"(RPAIR * 2 * RP) : "memory");
}
template <int EXTRA>
DI void xraw_transpose(const Ctx& c, const XT& x) {
  const bool top = c.ty == 0, bot = c.ty == 15;
  xraw_step<0, EXTRA>(x, !(top & !x.odd), x.huse & !(top & !x.hrow));
  xraw_step<1, EXTRA>(x, true, x.huse);
  xraw_step<2, EXTRA>(x, true, x.huse);
  xraw_step<3, EXTRA>(x, true, x.huse);
  xraw_step<4, EXTRA>(x, !(bot & x.odd), x.huse & !(bot & x.hrow));
}
struct XRow { u32x4 r[4]; };
DI void xrow_issue(const Ctx& c, XRow& xr, int yrow) {
  const unsigned o = (unsigned)(((c.b * 128 + 32 * (c.w & 3) + 16 * (c.w >> 2) + 4 * (c.lane >> 4)) * 128 + yrow) * 128) * 2 + (c.lane & 15) * 16;
#pragma unroll
  for (int jj = 0; jj < 4; ++jj) xr.r[jj] = bld<AUX_ROW>(c.rx, o, jj * 32768);
}
DI f32x2 xrow_stats(const XRow& xr) {
  float s = 0.f, s1 = 0.f, qs = 0.f, q1 = 0.f;
#pragma unroll
  for (int jj = 0; jj < 4; jj += 2)
#pragma unroll
    for (int k = 0; k < 4; ++k) { s = dot2(xr.r[jj][k], 0x3f803f80u, s); qs = dot2(xr.r[jj][k], xr.r[jj][k], qs); s1 = dot2(xr.r[jj + 1][k], 0x3f803f80u, s1); q1 = dot2(xr.r[jj + 1][k], xr.r[jj + 1][k], q1); }
  return f32x2{wave_sum(s + s1), wave_sum(qs + q1)};
}
DI void xrow_cross(const Ctx& c, const XRow& xr, int yrow) {
  const int k16 = c.lane & 15;
  if (k16 == 7 || k16 == 8) {
    unsigned v[2];
#pragma unroll
    for (int m = 0; m < 2; ++m) {
      const unsigned a = __builtin_amdgcn_perm(xr.r[2 * m + 1][3], xr.r[2 * m][3], 0x07060302u), b2 = __builtin_amdgcn_perm(xr.r[2 * m + 1][0], xr.r[2 * m][0], 0x05040100u);
      v[m] = k16 == 7 ? a : b2;
    }
    bst_sc1(u32x4{v[0], c.tag, v[1], c.tag}, c.rw, c.wb + WB_XC + (unsigned)((yrow * 2 + (k16 - 7)) * 64 + 16 * (c.w & 3) + 8 * (c.w >> 2) + 2 * (c.lane >> 4)) * 8, 0);
  }
}
struct Halo { unsigned go[4], lo[4]; bool inr[4], valid[4]; };
DI void halo_init(const Ctx& c, Halo& h) {
  const int tid = c.t - 256, cg = tid & 15, i = tid >> 4;
#pragma unroll
  for (int r = 0; r < 4; ++r) {
    const int row = r == 0 ? 0 : r == 1 ? 9 : r == 2 ? 1 + (i & 7) : (i >> 1) * 9;
    const int col = r < 2 ? i : r == 2 ? (i >> 3) * 17 : 16 + (i & 1);
    h.inr[r] = r < 3 ? true : i < 4;
    h.valid[r] = h.inr[r] & in_image(c, row, col);
    h.go[r] = WS_HB + (unsigned)(((c.b * 128 + c.y0 - 1 + row) * 128 + c.x0 - 1 + col) * 256 + cg * 16); PIN(h.go[r]);
    h.lo[r] = L_T + row * RP + col * 256 + ((cg ^ swz(col & 15)) << 4); PIN(h.lo[r]);
  }
}
struct Clr { unsigned o[3]; };
DI void clear_init(const Ctx& c, Clr& cl) {
  const int tid = c.t - 256, cg = tid & 15, i = tid >> 4, j = i < 12 ? i : 11;
#pragma unroll
  for (int r = 0; r < 3; ++r) {
    const int ry = r == 0 ? 0 : r == 1 ? 7 : 1 + (j >> 1), rx = r < 2 ? i : (j & 1) * 15;
    cl.o[r] = WS_HB + (unsigned)(((c.b * 128 + c.y0 + ry) * 128 + c.x0 + rx) * 256 + cg * 16); PIN(cl.o[r]);
  }
}
DI void stats_to_lds(const Ctx& c, int g, const u32x4& v0, const u32x4& v1) {
  const float s = wave_sum(__uint_as_float(v0[0]) + __uint_as_float(v1[0])), q = wave_sum(__uint_as_float(v0[2]) + __uint_as_float(v1[2]));
  const float mean = s * (1.f / 524288.f); float var = q * (1.f / 524288.f) - mean * mean; var = var < 0.f ? 0.f : var;
  if (c.lane == 0) *(f32x2*)(c.smem + L_STAT + g * 8) = f32x2{mean, __builtin_amdgcn_rsqf(var + 1e-5f)};
}

template <int CONV>
DI void mainloop(const Ctx& c, f32x4 (&acc)[8][2], bf16x8 (&wr)[WD], unsigned wvoff, const f32x4 (&ini)[2], unsigned baseP, unsigned baseD) {
  const char* T = c.smem + L_T;
  auto rd = [&](int B, int r) -> bf16x8 {
    const int sp = B / 3, ty = B % 3;
    const unsigned a = ty == 2 ? (baseD ^ (sp << 6)) : (baseP ^ ((2 * sp + ty) << 5));
    return *(const bf16x8*)(T + a + r * RP);
  };
  bf16x8 xf[4];
#pragma unroll
  for (int r = 0; r < 3; ++r) xf[r] = rd(0, r);
#pragma unroll
  for (int B = 0; B < 12; ++B) {
#pragma unroll
    for (int r = 0; r < 10; ++r) {
      const int G = B * 10 + r;
      if (r == 7 && (B % 3) == 2 && B < 11) { bar_nowait(); SB(); }
      { const int Gn = G + 3; if (Gn < 120) xf[Gn & 3] = rd(Gn / 10, Gn % 10); }
#pragma unroll
      for (int dy = 0; dy < 3; ++dy) {
        const int row = r - dy;
        if (row >= 0 && row < 8) {
#pragma unroll
          for (int nt = 0; nt < 2; ++nt) {
            const int ts = 6 * B + 2 * dy + nt;
            if (CONV == 1) asm volatile("v_mfma_f32_16x16x32_bf16 %0, %1, %2, %0" : "+v"(acc[row][nt]) : "v"(xf[G & 3]), "v"(wr[ts % WD]));
            else if (B == 0 && dy == 0) asm volatile("v_mfma_f32_16x16x32_bf16 %0, %1, %2, %3" : "=&v"(acc[row][nt]) : "v"(wr[ts % WD]), "v"(xf[G & 3]), "v"(ini[nt]));
            else           asm volatile("v_mfma_f32_16x16x32_bf16 %0, %1, %2, %0" : "+v"(acc[row][nt]) : "v"(wr[ts % WD]), "v"(xf[G & 3]));
            if (row == 7) { const int tn = CONV * 72 + ts + WD; if (tn < 144) wr[ts % WD] = __builtin_bit_cast(bf16x8, bld<SC1>(c.rw, wvoff, (unsigned)tn * 1024 + (tn >= 72 ? 294912u - 72u * 1024u : 0u))); }
            SB();
          }
        }
      }
    }
  }
  asm volatile("s_nop 15" ::: "memory");
  SB();
}
DI u32x4 pack_row(const f32x4 (&a)[2]) {
  const unsigned d0 = cvtpk(a[0][0], a[0][1]), d1 = cvtpk(a[0][2], a[0][3]), d2 = cvtpk(a[1][0], a[1][1]), d3 = cvtpk(a[1][2], a[1][3]);
  auto p = __builtin_amdgcn_permlane16_swap(d0, d2, false, false);
  auto q = __builtin_amdgcn_permlane16_swap(d1, d3, false, false);
  return u32x4{p[0], q[0], p[1], q[1]};
}

__global__ __launch_bounds__(512, 2) void k_resblock(const u16* __restrict__ x, const u16* __restrict__ t_emb, const u16* __restrict__ g1, const u16* __restrict__ be1,
                                                     const u16* __restrict__ w1, const u16* __restrict__ w_emb, const u16* __restrict__ g2, const u16* __restrict__ be2,
                                                     const u16* __restrict__ w2, u16* __restrict__ out, char* ws) {
  extern __shared__ __attribute__((aligned(256))) char smem[];
  Ctx c;
  c.smem = smem; c.ws = ws;
  c.t = threadIdx.x; c.lane = c.t & 63; c.w = __builtin_amdgcn_readfirstlane(c.t >> 6);
  const int xcd = blockIdx.x & 7, jx = blockIdx.x >> 3;
  c.b = xcd >> 2; c.ty = (xcd & 3) * 4 + (jx >> 3); c.tx = jx & 7;
  c.jb = c.ty * 8 + c.tx; c.y0 = c.ty * 8; c.x0 = c.tx * 16;
  c.rx = mk_rsrc(x, 8u << 20); c.rw = mk_rsrc(ws, 16u << 20);
  c.wb = (unsigned)c.b * WB_SIZE;

  if (c.w < 4) {
    STAMP_DECL STAMP(0);
    const int yrow = (xcd & 3) * 32 + jx;
    const unsigned epoch = __hip_atomic_load((GAS unsigned*)(ws + c.wb + WB_EPOCH), __ATOMIC_RELAXED, __HIP_MEMORY_SCOPE_AGENT);
    XRow xr; xrow_issue(c, xr, yrow);
    const int ch = c.t & 127;
    const unsigned gbA = (c.t < 128 ? g1 : be1)[ch], gbB = (c.t < 128 ? g2 : be2)[ch];
    const int px = c.lane & 15, q = c.lane >> 4;
    const rsrc_t ro = mk_rsrc(out, 8u << 20);
    SB();
    unsigned oo = (unsigned)(((c.b * 128 + 32 * c.w + 16 * (q & 1) + px) * 128 + c.y0) * 128 + c.x0 + 8 * (q >> 1)) * 2; PIN(oo);
    XT xt; xraw_init(c, xt);
    Task0 t0; task0_init(c, t0);
    unsigned baseP, baseD;
    { const int colP = px + (q & 1), colD = px + 2;
      baseP = colP * 256 + (((q >> 1) ^ swz(colP & 15)) << 4); baseD = colD * 256 + ((q ^ swz(colD & 15)) << 4); PIN(baseP); PIN(baseD); }
    unsigned wvoff = c.wb + WB_WF + (unsigned)c.w * 73728u + (unsigned)c.lane * 16u; PIN(wvoff);
    unsigned fo = c.wb + WB_FLAG + (unsigned)(c.w * 128 + (c.lane & 7) * 4); PIN(fo);
    SB();
    STAMP(2);
    c.tag = __builtin_amdgcn_readfirstlane(epoch) + 1u;
    { const f32x2 p = xrow_stats(xr); if (c.lane == 0) *(f32x2*)(smem + L_RED + c.w * 8) = p; }
    bar();
    STAMP(14);
    xrow_cross(c, xr, yrow);
    { float* gbp = (float*)(smem + L_GB) + (c.t < 128 ? 0 : 128) + ch; gbp[0] = bflo(gbA) * LOG2E; gbp[256] = bflo(gbB) * LOG2E; }
    SB();
    xraw_issue(c, xt.vo); SB();
    unsigned fl = __builtin_amdgcn_raw_buffer_load_b32(c.rw, fo, 0, SC1); SB();
    STAMP(15);
    xraw_transpose<1>(c, xt); SB();
    STAMP(3);
    for (int spin = 0; spin < SPIN_MAX; ++spin) {
      if (!__any(fl != c.tag)) break;
      __builtin_amdgcn_s_sleep(POLL_SLEEP);
      cbar();
      fl = __builtin_amdgcn_raw_buffer_load_b32(c.rw, fo, 0, SC1);
    }
    STAMP(4);
    bf16x8 wr[WD];
#pragma unroll
    for (int k = 0; k < WD; ++k) wr[k] = __builtin_bit_cast(bf16x8, bld<SC1>(c.rw, wvoff, (unsigned)k * 1024));
    bar();
    STAMP(5);
    norm_group0(c, t0, 0);
    bar();
    STAMP(6);
    f32x4 acc[8][2], ini[2];
    ini[0] = *(const f32x4*)(smem + L_EMB + (32 * c.w + 4 * q) * 4); ini[1] = *(const f32x4*)(smem + L_EMB + (32 * c.w + 16 + 4 * q) * 4);
    __builtin_amdgcn_s_setprio(1);
    mainloop<0>(c, acc, wr, wvoff, ini, baseP, baseD);
    __builtin_amdgcn_s_setprio(0);
    STAMP(7);
    {
      f32x2 s2 = {0.f, 0.f}, q2 = {0.f, 0.f};
#pragma unroll
      for (int r = 0; r < 8; ++r)
#pragma unroll
        for (int nt = 0; nt < 2; ++nt)
#pragma unroll
          for (int k = 0; k < 2; ++k) { const f32x2 v = {acc[r][nt][2 * k], acc[r][nt][2 * k + 1]}; s2 += v; q2 += v * v; }
      const float s = wave_sum(s2[0] + s2[1]), qs = wave_sum(q2[0] + q2[1]);
      if (c.lane == 0) bst_sc1(u32x4{__float_as_uint(s), c.tag, __float_as_uint(qs), c.tag}, c.rw, c.wb + WB_G2 + (unsigned)(c.w * 128 + c.jb) * 16, 0);
    }
    STAMP(1);
    {
      const int chk = 4 * c.w + (((q & 1) << 1) | (q >> 1));
      const unsigned go = WS_HB + (unsigned)(((c.b * 128 + c.y0) * 128 + c.x0 + px) * 256 + chk * 16);
      u32x4 hv[8];
#pragma unroll
      for (int r = 0; r < 8; ++r) hv[r] = pack_row(acc[r]);
      auto san = [](u32x4 v) { return __builtin_elementwise_min(v, u32x4{0xfffffffeu, 0xfffffffeu, 0xfffffffeu, 0xfffffffeu}); };
      bst_sc1(san(hv[0]), c.rw, go, 0); bst_sc1(san(hv[7]), c.rw, go, 7 * 32768);
      if (px == 0 || px == 15) {
#pragma unroll
        for (int r = 1; r < 7; ++r) bst_sc1(san(hv[r]), c.rw, go, r * 32768);
      }
      bar_nowait();
      char* lp = smem + L_T + RP + (px + 1) * 256 + ((chk ^ swz((px + 1) & 15)) << 4);
#pragma unroll
      for (int r = 0; r < 8; ++r) *(u32x4*)(lp + r * RP) = hv[r];
    }
    STAMP(8);
#pragma unroll
    for (int r = 0; r < 8; ++r) {
      const u32x4 v = *(const u32x4*)(smem + L_RAW + (r + 1) * RS + (32 * c.w + 16 * (q & 1) + px) * 64 + 16 + (q >> 1) * 16);
      auto a = __builtin_amdgcn_permlane16_swap(v[0], v[2], false, false);
      auto e = __builtin_amdgcn_permlane16_swap(v[1], v[3], false, false);
      acc[r][0] = f32x4{bflo(a[0]), bfhi(a[0]), bflo(e[0]), bfhi(e[0])};
      acc[r][1] = f32x4{bflo(a[1]), bfhi(a[1]), bflo(e[1]), bfhi(e[1])};
      asm volatile("" : "+v"(acc[r][0]), "+v"(acc[r][1]));
    }
    STAMP(9);
    bar();
    STAMP(10);
    norm_group0(c, t0, 1);
    bar();
    STAMP(11);
    __builtin_amdgcn_s_setprio(1);
    mainloop<1>(c, acc, wr, wvoff, ini, baseP, baseD);
    __builtin_amdgcn_s_setprio(0);
    STAMP(12);
#pragma unroll
    for (int r = 0; r < 8; ++r) __builtin_amdgcn_raw_buffer_store_b128(pack_row(acc[r]), ro, oo + r * 256, 0, 0);
    STAMP(13); STAMP_FLUSH;
  } else {
    STAMP_DECL STAMP(0);
    const int g = c.w - 4;
    const int yrow = (xcd & 3) * 32 + jx;
    const int ksvc = jx * 4 + (xcd & 3);
    const bool svc_w = c.w == 7 && (ksvc < 32 || (ksvc >= 64 && ksvc < 96)), svc_e = c.w == 7 && ksvc >= 32 && ksvc < 64;
    const int wu = ksvc < 32 ? ksvc : ksvc - 32;
    const unsigned epoch = __hip_atomic_load((GAS unsigned*)(ws + c.wb + WB_EPOCH), __ATOMIC_RELAXED, __HIP_MEMORY_SCOPE_AGENT);
    XRow xr; xrow_issue(c, xr, yrow);
    SB();
    STAMP(9);
    u32x4 sv[11]; unsigned te[6];
    if (svc_w) {
      const int r3 = (c.lane * 57) >> 10, pc = c.lane - 18 * r3;
      const rsrc_t rws = mk_rsrc((wu >> 5) ? w2 : w1, 294912u);
      const unsigned o = (unsigned)(((32 * ((wu >> 3) & 3) + r3) * 128 + 16 * (wu & 7)) * 18 + pc * 16);
#pragma unroll
      for (int k = 0; k < 11; ++k) sv[k] = bld<AUX_SVC>(rws, o, k * 3 * 2304);
    } else if (svc_e) {
      const int j = ksvc - 32, e0 = 192 * (j >> 3) + (c.lane >> 1);
      const rsrc_t re = mk_rsrc(w_emb, 196608u), rt = mk_rsrc(t_emb, 3072u);
#pragma unroll
      for (int k = 0; k < 6; ++k) { sv[k] = bld<AUX_SVC>(re, (unsigned)(e0 * 256 + (16 * (j & 7) + 8 * (c.lane & 1)) * 2), k * 32 * 256);
                                    te[k] = __builtin_amdgcn_raw_buffer_load_b16(rt, (unsigned)(c.b * 768 + e0) * 2, k * 64, 0); }
    }
    SB();
    XT xt; xraw_init(c, xt);
    Task0 t0; task0_init(c, t0);
    HTask ht; htask_init(c, ht);
    if ((c.ty == 0) | (c.ty == 15) | (c.tx == 0) | (c.tx == 7)) {
      Halo hz; halo_init(c, hz);
#pragma unroll
      for (int r = 0; r < 4; ++r) if (hz.inr[r] & !hz.valid[r]) *(u32x4*)(smem + hz.lo[r]) = u32x4{0, 0, 0, 0};
    }
    SB();
    STAMP(4);
    c.tag = __builtin_amdgcn_readfirstlane(epoch) + 1u;
    STAMP(3);
    const f32x2 psum = xrow_stats(xr);
    asm volatile("" :: "v"(psum));
    STAMP(14);
    bar();
    { const f32x2 p = psum + *(const f32x2*)(smem + L_RED + g * 8);
      if (c.lane == 0) bst_sc1(u32x4{__float_as_uint(p[0]), c.tag, __float_as_uint(p[1]), c.tag}, c.rw, c.wb + WB_G1 + (unsigned)(g * 128 + c.jb) * 16, 0); }
    SB();
    STAMP(1);
    xrow_cross(c, xr, yrow);
    if (svc_w) {
      if (c.lane < 54) {
#pragma unroll
        for (int k = 0; k < 11; ++k) *(u32x4*)(smem + L_SVC + k * 864 + c.lane * 16) = sv[k];
      }
#pragma unroll
      for (int k = 0; k < 9; ++k) { sv[k] = *(const u32x4*)(smem + L_SVC + (c.lane & 31) * 288 + (c.lane >> 5) * 144 + k * 16); asm volatile("" : "+v"(sv[k])); }
    }
    SB();
    xraw_issue(c, xt.vo); SB();
    STAMP(5);
    if (svc_w) {
      const int cs = wu & 7, o = c.lane & 31, h = c.lane >> 5;
      const unsigned dst = c.wb + WB_WF + (unsigned)(wu >> 5) * 294912u + (unsigned)((wu >> 3) & 3) * 73728u + (unsigned)(o >> 4) * 1024u + (unsigned)(o & 15) * 16u;
      const unsigned dP = dst + (unsigned)(3 * (cs >> 1) + (cs & 1)) * 6144u + (unsigned)h * 512u;
      const unsigned dD = dst + (unsigned)(3 * (cs >> 1) + 2) * 6144u + (unsigned)(2 * (cs & 1) + h) * 256u;
#pragma unroll
      for (int k = 0; k < 9; ++k) {
        const int dy = k / 3, dx = k % 3;
        u32x4 o4;
#pragma unroll
        for (int m = 0; m < 4; ++m) {
          const int e0 = 18 * m + k, e1 = e0 + 9;
          const unsigned sel = ((e0 & 1) ? 0x0302u : 0x0100u) | ((e1 & 1) ? 0x07060000u : 0x05040000u);
          o4[m] = __builtin_amdgcn_perm(sv[e1 >> 3][(e1 >> 1) & 3], sv[e0 >> 3][(e0 >> 1) & 3], sel);
        }
        bst_sc1(o4, c.rw, dx < 2 ? dP : dD, dy * 2048 + (dx < 2 ? dx * 256 : 0));
      }
      SB();
      xraw_transpose<9>(c, xt);
      asm volatile("s_waitcnt vmcnt(0)" ::: "memory");
      if (c.lane == 0) __hip_atomic_store((GAS unsigned*)(ws + c.wb + WB_FLAG + (wu >> 3) * 128 + (wu & 7) * 4), c.tag, __ATOMIC_RELAXED, __HIP_MEMORY_SCOPE_AGENT);
    } else if (svc_e) {
      f32x2 a[4];
#pragma unroll
      for (int m = 0; m < 4; ++m) a[m] = f32x2{0.f, 0.f};
#pragma unroll
      for (int k = 0; k < 6; ++k) {
        unsigned tk = te[k]; PIN(tk);
        const float tv = bflo(tk);
        const float sl = tv * __builtin_amdgcn_rcpf(1.f + __builtin_amdgcn_exp2f(-tv * LOG2E));
#pragma unroll
        for (int m = 0; m < 4; ++m) a[m] += f32x2{bflo(sv[k][m]), bfhi(sv[k][m])} * sl;
      }
      float r[8];
#pragma unroll
      for (int m = 0; m < 8; ++m) r[m] = rows_sum(dppadd<0x128>(dppadd<0x124>(dppadd<0x4E>(a[m >> 1][m & 1]))));
      const int j = ksvc - 32;
      if (c.lane < 2) {
        const unsigned o = c.wb + WB_EG + (unsigned)((j >> 3) * 128 + 16 * (j & 7) + 8 * c.lane) * 8;
#pragma unroll
        for (int m = 0; m < 4; ++m) bst_sc1(u32x4{__float_as_uint(r[2 * m]), c.tag, __float_as_uint(r[2 * m + 1]), c.tag}, c.rw, o, m * 16);
      }
      SB();
      xraw_transpose<4>(c, xt);
    } else xraw_transpose<0>(c, xt);
    SB();
    STAMP(2);
    {
      u32x4 v0, v1, vc[2]; bool n0 = true, n1 = true, nc[2];
      const unsigned o = c.wb + WB_G1 + (unsigned)(g * 128 + c.lane) * 16;
      const bool xc = c.tx == 3 || c.tx == 4;
      unsigned co[2], cl2[2]; bool cin[2];
#pragma unroll
      for (int r = 0; r < 2; ++r) {
        const int id = c.t - 256 + 256 * r, row = id >> 5, i = id & 31, y = c.y0 - 1 + row, side = c.tx == 3 ? 1 : 0;
        cin[r] = xc & (id < 320);
        nc[r] = cin[r] & ((unsigned)y < 128u);
        co[r] = c.wb + WB_XC + (unsigned)((y * 2 + side) * 64 + 2 * i) * 8;
        cl2[r] = L_T + row * RP + side * 17 * 256 + (((i >> 1) ^ side) << 4) + (i & 1) * 8;
        vc[r] = u32x4{0, 0, 0, 0};
      }
      for (int spin = 0; spin < SPIN_MAX; ++spin) {
        cbar();
        if (n0) v0 = bld<SC1>(c.rw, o, 0);
        if (n1) v1 = bld<SC1>(c.rw, o, 1024);
        if (xc) {
#pragma unroll
          for (int r = 0; r < 2; ++r) if (nc[r]) vc[r] = bld<SC1>(c.rw, co[r], 0);
        }
        n0 = !((v0[1] == c.tag) & (v0[3] == c.tag)); n1 = !((v1[1] == c.tag) & (v1[3] == c.tag));
#pragma unroll
        for (int r = 0; r < 2; ++r) nc[r] = nc[r] & !((vc[r][1] == c.tag) & (vc[r][3] == c.tag));
        if (!__any(n0 | n1 | nc[0] | nc[1])) break;
        __builtin_amdgcn_s_sleep(POLL_SLEEP);
      }
      stats_to_lds(c, g, v0, v1);
      if (xc) {
#pragma unroll
        for (int r = 0; r < 2; ++r) if (cin[r]) *(u32x2*)(smem + cl2[r]) = u32x2{vc[r][0], vc[r][2]};
      }
    }
    u32x4 ve[4];
    if (c.w == 4) {
#pragma unroll
      for (int k = 0; k < 4; ++k) ve[k] = bld<SC1>(c.rw, c.wb + WB_EG + (unsigned)c.lane * 16, k * 1024);
    }
    STAMP(6);
    bar();
    if (c.t == 256 && c.jb == 0) __hip_atomic_store((GAS unsigned*)(ws + c.wb + WB_EPOCH), c.tag, __ATOMIC_RELAXED, __HIP_MEMORY_SCOPE_AGENT);
    norm_group0(c, t0, 0);
    if (c.w == 4) {
      for (int spin = 0; spin < SPIN_MAX; ++spin) {
        bool ok = true;
#pragma unroll
        for (int k = 0; k < 4; ++k) ok = ok & (ve[k][1] == c.tag) & (ve[k][3] == c.tag);
        if (!__any(!ok)) break;
        cbar();
#pragma unroll
        for (int k = 0; k < 4; ++k) ve[k] = bld<SC1>(c.rw, c.wb + WB_EG + (unsigned)c.lane * 16, k * 1024);
      }
      *(f32x2*)(smem + L_EMB + c.lane * 8) = f32x2{(__uint_as_float(ve[0][0]) + __uint_as_float(ve[1][0])) + (__uint_as_float(ve[2][0]) + __uint_as_float(ve[3][0])),
                                                  (__uint_as_float(ve[0][2]) + __uint_as_float(ve[1][2])) + (__uint_as_float(ve[2][2]) + __uint_as_float(ve[3][2]))};
    }
    STAMP(15);
    bar();
#pragma unroll 1
    for (int k = 1; k < 4; ++k) {
      if (k == 2) {
        Clr cl; clear_init(c, cl);
#pragma unroll
        for (int r = 0; r < 3; ++r) bst_sc1(u32x4{~0u, ~0u, ~0u, ~0u}, c.rw, cl.o[r], 0);
      }
      norm_group_h(c, ht, k);
      if (k == 2) asm volatile("s_waitcnt vmcnt(0)" ::: "memory");
      if (k == 3 && c.w == 6) {
        const unsigned fo = c.wb + WB_FLAG + (unsigned)((4 + ((c.lane >> 3) & 3)) * 128 + (c.lane & 7) * 4);
        for (int spin = 0; spin < SPIN_MAX; ++spin) {
          cbar();
          const unsigned f = __builtin_amdgcn_raw_buffer_load_b32(c.rw, fo, 0, SC1);
          if (!__any(f != c.tag)) break;
        }
      }
      bar();
      if (k == 2 && c.t == 256) __hip_atomic_store((GAS unsigned*)(ws + c.wb + WB_CF + c.jb * 4), c.tag, __ATOMIC_RELAXED, __HIP_MEMORY_SCOPE_AGENT);
    }
    STAMP(7);
    {
      Halo hl; halo_init(c, hl);
      if (c.w == 5) {
        const int l9 = c.lane < 9 ? c.lane : 4, dy = ((l9 * 11) >> 5) - 1, dx = l9 - 3 * (dy + 1) - 1;
        const int ny = c.ty + dy, nx = c.tx + dx;
        const bool chk = ((unsigned)ny < 16u) & ((unsigned)nx < 8u);
        const unsigned fo = c.wb + WB_CF + (unsigned)(chk ? ny * 8 + nx : c.jb) * 4;
        for (int spin = 0; spin < SPIN_MAX; ++spin) {
          cbar();
          const unsigned f = __builtin_amdgcn_raw_buffer_load_b32(c.rw, fo, 0, SC1);
          if (!__any(f != c.tag)) break;
          __builtin_amdgcn_s_sleep(2);
        }
      }
      u32x4 hv[4]; bool need[4];
#pragma unroll
      for (int r = 0; r < 4; ++r) { need[r] = hl.valid[r]; hv[r] = u32x4{0, 0, 0, 0}; }
      u32x4 v0, v1; bool n0 = true, n1 = true;
      const unsigned o = c.wb + WB_G2 + (unsigned)(g * 128 + c.lane) * 16;
      bar_nowait();
      STAMP(13);
      for (int spin = 0; spin < SPIN_MAX; ++spin) {
        cbar();
        if (n0) v0 = bld<SC1>(c.rw, o, 0);
        if (n1) v1 = bld<SC1>(c.rw, o, 1024);
#pragma unroll
        for (int r = 0; r < 4; ++r) if (need[r]) hv[r] = bld<SC1>(c.rw, hl.go[r], 0);
        bool any = false;
#pragma unroll
        for (int r = 0; r < 4; ++r) { need[r] = need[r] & ((hv[r][0] == ~0u) | (hv[r][1] == ~0u) | (hv[r][2] == ~0u) | (hv[r][3] == ~0u)); any |= need[r]; }
        n0 = !((v0[1] == c.tag) & (v0[3] == c.tag)); n1 = !((v1[1] == c.tag) & (v1[3] == c.tag));
        if (!__any(any | n0 | n1)) break;
        __builtin_amdgcn_s_sleep(POLL_SLEEP2);
      }
      STAMP(8);
#pragma unroll
      for (int r = 0; r < 4; ++r) if (hl.valid[r]) *(u32x4*)(smem + hl.lo[r]) = hv[r];
      stats_to_lds(c, g, v0, v1);
    }
    bar();
    norm_group0(c, t0, 1);
    bar();
#pragma unroll 1
    for (int k = 1; k < 4; ++k) { if (k == 1) STAMP(10); norm_group_h(c, ht, k); if (k == 1) STAMP(11); bar(); }
    STAMP(12); STAMP_FLUSH;
  }
}

extern "C" void kernel_launch(void* const* d_in, const int* in_sizes, int n_in, void* d_out, int out_size, void* d_ws, size_t ws_size, hipStream_t stream) {
  (void)hipFuncSetAttribute((const void*)k_resblock, hipFuncAttributeMaxDynamicSharedMemorySize, L_END);
  k_resblock<<<256, 512, L_END, stream>>>((const u16*)d_in[0], (const u16*)d_in[1], (const u16*)d_in[2], (const u16*)d_in[3], (const u16*)d_in[4],
                                          (const u16*)d_in[5], (const u16*)d_in[6], (const u16*)d_in[7], (const u16*)d_in[8], (u16*)d_out, (char*)d_ws);
}
```

```cpp
#include <hip/hip_runtime.h>
#include <hip/hip_bf16.h>
#include <stdint.h>

typedef float f32x4 __attribute__((ext_vector_type(4)));
typedef float f32x2 __attribute__((ext_vector_type(2)));
typedef __bf16 bf16x8 __attribute__((ext_vector_type(8)));
typedef __bf16 bf16x2 __attribute__((ext_vector_type(2)));
typedef unsigned u32x4 __attribute__((ext_vector_type(4)));
typedef unsigned u32x2 __attribute__((ext_vector_type(2)));
typedef unsigned short u16;
#define GAS __attribute__((address_space(1)))
#define LAS __attribute__((address_space(3)))
#define DI __device__ __forceinline__
#define SB() __builtin_amdgcn_sched_barrier(0)
#define PIN(x) asm volatile("" : "+v"(x))

constexpr int RP = 4608;
constexpr int TILE_BYTES = 10 * RP;
constexpr int RS = 8224;
constexpr int WD = 18;
constexpr float LOG2E = 1.4426950408889634f;
constexpr int SC1 = 16;
constexpr int SPIN_MAX = 1 << 16;
constexpr unsigned CODE_PF_BYTES = 32u * 1024u;

constexpr unsigned WB_SIZE = 1u << 20;
constexpr unsigned WB_WF = 0  , WB_EPOCH = 589824, WB_FLAG = WB_EPOCH + 256  ,
                   WB_G1 = WB_FLAG + 4096  , WB_G2 = WB_G1 + 8192, WB_EG = WB_G2 + 8192  ,
                   WB_CF = WB_EG + 4096  , WB_XC = WB_CF + 8192  ;
constexpr unsigned WS_HB = 2u << 20;
constexpr int L_T = 0, L_RAW = TILE_BYTES, L_GB = L_RAW + 10 * RS  , L_PAR = L_GB + 2048  , L_EMB = L_PAR + 1024,
              L_STAT = L_EMB + 512  , L_RED = L_STAT + 128  , L_CNT = L_RED + 32  ,
              L_DUMMY = (L_CNT + 16 + 255) / 256 * 256  , L_END = L_DUMMY + 1024;

DI int swz(int c) { return c ^ ((c >> 1) & 4); }
DI unsigned cvtpk(float lo, float hi) { f32x2 v = {lo, hi}; bf16x2 b = __builtin_convertvector(v, bf16x2); return __builtin_bit_cast(unsigned, b); }
DI unsigned cvtpk_s(float lo, float hi) { unsigned r; asm("v_cvt_pk_bf16_f32 %0, %1, %2" : "=v"(r) : "v"(lo), "v"(hi)); return r; }
DI float bflo(unsigned d) { return __uint_as_float(d << 16); }
DI float bfhi(unsigned d) { return __uint_as_float(d & 0xffff0000u); }
DI float dot2(unsigned a, unsigned b, float c) { return __builtin_amdgcn_fdot2_f32_bf16(__builtin_bit_cast(bf16x2, a), __builtin_bit_cast(bf16x2, b), c, false); }
template <int CTRL> DI float dppadd(float v) { return v + __int_as_float(__builtin_amdgcn_update_dpp(0, __float_as_int(v), CTRL, 0xf, 0xf, true)); }
DI float rows_sum(float v) {
  auto p = __builtin_amdgcn_permlane16_swap(__float_as_uint(v), __float_as_uint(v), false, false);
  v = __uint_as_float(p[0]) + __uint_as_float(p[1]);
  auto q = __builtin_amdgcn_permlane32_swap(__float_as_uint(v), __float_as_uint(v), false, false);
  return __uint_as_float(q[0]) + __uint_as_float(q[1]);
}
DI float wave_sum(float v) { v = dppadd<0xB1>(v); v = dppadd<0x4E>(v); v = dppadd<0x141>(v); v = dppadd<0x140>(v); return rows_sum(v); }
DI float quad_lane_sum(float v) { return rows_sum(dppadd<0x128>(dppadd<0x124>(v))); }
DI void bar() { asm volatile("s_waitcnt lgkmcnt(0)\n\ts_barrier" ::: "memory"); }
DI void bar_nowait() { asm volatile("s_barrier" ::: "memory"); }
DI void cbar() { asm volatile("" ::: "memory"); }
typedef __amdgpu_buffer_rsrc_t rsrc_t;
DI rsrc_t mk_rsrc(const void* p, unsigned bytes) { return __builtin_amdgcn_make_buffer_rsrc((void*)p, 0, bytes, 0x00020000); }
DI u32x4 bld(rsrc_t r, unsigned voff, unsigned soff) { return __builtin_amdgcn_raw_buffer_load_b128(r, voff, soff, 0); }
DI u32x4 bld_sc1(rsrc_t r, unsigned voff, unsigned soff) { return __builtin_amdgcn_raw_buffer_load_b128(r, voff, soff, SC1); }
DI void bst_sc1(u32x4 v, rsrc_t r, unsigned voff, unsigned off) { __builtin_amdgcn_raw_buffer_store_b128(v, r, voff + off, 0, SC1); }
DI void flag_store(char* p, unsigned v) { __hip_atomic_store((GAS unsigned*)p, v, __ATOMIC_RELAXED, __HIP_MEMORY_SCOPE_AGENT); }
DI bool tagged(const u32x4& v, unsigned tag) { return v[1] == tag && v[3] == tag; }

struct Ctx {
  rsrc_t rx, rw;
  char* smem; char* ws;
  int b, ty, tx, jb, y0, x0, w, lane, t;
  unsigned wb;
  unsigned tag;
  unsigned baseP, baseD;
  rsrc_t ro; unsigned oo;
};

DI f32x2 group_stat(const char* smem, int g) {
  const f32x2 p0 = *(const f32x2*)(smem + L_STAT + g * 8), p1 = *(const f32x2*)(smem + L_STAT + 32 + g * 8);
  const f32x2 p2 = *(const f32x2*)(smem + L_STAT + 64 + g * 8), p3 = *(const f32x2*)(smem + L_STAT + 96 + g * 8);
  const f32x2 p = ((p0 + p1) + (p2 + p3)) * (1.f / 524288.f);
  float var = p[1] - p[0] * p[0]; var = var < 0.f ? 0.f : var;
  return f32x2{p[0], __builtin_amdgcn_rsqf(var + 1e-5f)};
}
DI u32x2 act4_packed(u32x2 v, f32x4 a, f32x4 b) {
  u32x2 o;
#pragma unroll
  for (int m = 0; m < 2; ++m) {
    f32x2 xx = {bflo(v[m]), bfhi(v[m])}, aa = {a[2 * m], a[2 * m + 1]}, bb = {b[2 * m], b[2 * m + 1]};
    f32x2 u = xx * aa + bb;
    f32x2 e = {__builtin_amdgcn_exp2f(-u[0]), __builtin_amdgcn_exp2f(-u[1])};
    f32x2 d = e * LOG2E + LOG2E;
    f32x2 r = {__builtin_amdgcn_rcpf(d[0]), __builtin_amdgcn_rcpf(d[1])};
    f32x2 y = u * r;
    o[m] = cvtpk(y[0], y[1]);
  }
  return o;
}
DI float act1(float x, float a, float b) { float u = __builtin_fmaf(x, a, b); float e = __builtin_amdgcn_exp2f(-u); return u * __builtin_amdgcn_rcpf(__builtin_fmaf(e, LOG2E, LOG2E)); }
DI u32x2 act4_scalar(u32x2 v, f32x4 a, f32x4 b) {
  u32x2 o;
  o[0] = cvtpk_s(act1(bflo(v[0]), a[0], b[0]), act1(bfhi(v[0]), a[1], b[1]));
  o[1] = cvtpk_s(act1(bflo(v[1]), a[2], b[2]), act1(bfhi(v[1]), a[3], b[3]));
  return o;
}
DI bool in_image(const Ctx& c, int row, int col) { return ((unsigned)(c.y0 - 1 + row) < 128u) & ((unsigned)(c.x0 - 1 + col) < 128u); }
struct Tasks { unsigned off[3]; };
DI void tasks_init(const Ctx& c, Tasks& tk, int idx, int c4) {
  const unsigned lo = (c4 & 1) * 8, dummy = L_DUMMY + c.lane * 16 + lo;
  const int col = idx & 15, row = idx >> 4;
  const unsigned o0 = L_T + row * RP + col * 256 + (((c4 >> 1) ^ swz(col)) << 4) + lo;
  const int j = idx - 32, row2 = idx < 32 ? 8 + row : j >> 1, col2 = idx < 32 ? col : 16 + (j & 1);
  const unsigned o2 = L_T + row2 * RP + col2 * 256 + (((c4 >> 1) ^ swz(col2 & 15)) << 4) + lo;
  tk.off[0] = o0; tk.off[1] = o0 + 4 * RP; tk.off[2] = idx < 52 ? o2 : dummy;
  if ((c.ty == 0) | (c.ty == 15) | (c.tx == 0) | (c.tx == 7)) {
    tk.off[0] = in_image(c, row, col) ? tk.off[0] : dummy;
    tk.off[1] = in_image(c, row + 4, col) ? tk.off[1] : dummy;
    tk.off[2] = in_image(c, row2, col2) ? tk.off[2] : dummy;
  }
#pragma unroll
  for (int r = 0; r < 3; ++r) PIN(tk.off[r]);
}
DI void norm_group0(const Ctx& c, const Tasks& t0, int gbsel) {
  u32x2 v[3];
  const int ch0 = 4 * (c.t & 7);
  f32x4 gg = *(const f32x4*)(c.smem + L_GB + gbsel * 1024 + ch0 * 4), bb = *(const f32x4*)(c.smem + L_GB + gbsel * 1024 + 512 + ch0 * 4);
  f32x2 p0 = *(const f32x2*)(c.smem + L_STAT), p1 = *(const f32x2*)(c.smem + L_STAT + 32), p2 = *(const f32x2*)(c.smem + L_STAT + 64), p3 = *(const f32x2*)(c.smem + L_STAT + 96);
#pragma unroll
  for (int r = 0; r < 3; ++r) v[r] = *(const u32x2*)(c.smem + t0.off[r]);
  SB();
  const f32x2 p = ((p0 + p1) + (p2 + p3)) * (1.f / 524288.f);
  float var = p[1] - p[0] * p[0]; var = var < 0.f ? 0.f : var;
  const float sc = __builtin_amdgcn_rsqf(var + 1e-5f) * LOG2E;
  const f32x4 a = gg * sc, b = bb * LOG2E - a * p[0];
#pragma unroll
  for (int r = 0; r < 2; ++r) *(u32x2*)(c.smem + t0.off[r]) = act4_packed(v[r], a, b);
  if (c.w < 7) *(u32x2*)(c.smem + t0.off[2]) = act4_packed(v[2], a, b);
  else if (c.lane < 48) {
    const int ch = 32 + 2 * c.lane;
    const f32x2 g = *(const f32x2*)(c.smem + L_GB + gbsel * 1024 + ch * 4), be = *(const f32x2*)(c.smem + L_GB + gbsel * 1024 + 512 + ch * 4);
    const f32x2 st = group_stat(c.smem, ch >> 5);
    const f32x2 aa = g * (st[1] * LOG2E);
    *(f32x2*)(c.smem + L_PAR + ch * 4) = aa; *(f32x2*)(c.smem + L_PAR + 512 + ch * 4) = be * LOG2E - aa * st[0];
  }
}
DI void norm_group_h(const Ctx& c, const Tasks& ht, int k) {
  const int ch = 32 * k + 8 * (c.t & 3);
  u32x4 v[3];
#pragma unroll
  for (int r = 0; r < 3; ++r) v[r] = *(const u32x4*)(c.smem + (ht.off[r] ^ (k << 6)));
  const f32x4 a0 = *(const f32x4*)(c.smem + L_PAR + ch * 4), a1 = *(const f32x4*)(c.smem + L_PAR + ch * 4 + 16);
  const f32x4 b0 = *(const f32x4*)(c.smem + L_PAR + 512 + ch * 4), b1 = *(const f32x4*)(c.smem + L_PAR + 512 + ch * 4 + 16);
#pragma unroll
  for (int r = 0; r < 3; ++r) {
    const u32x2 lo = act4_scalar(u32x2{v[r][0], v[r][1]}, a0, b0), hi = act4_scalar(u32x2{v[r][2], v[r][3]}, a1, b1);
    *(u32x4*)(c.smem + (ht.off[r] ^ (k << 6))) = u32x4{lo[0], lo[1], hi[0], hi[1]};
  }
}

DI void xraw_issue(const Ctx& c) {
  int xs = c.x0 - 8 + 8 * (c.lane & 3);
  { const int lo = c.x0 & 64; xs = xs < lo ? lo : (xs > lo + 56 ? lo + 56 : xs); }
  const unsigned vo = (unsigned)(((c.b * 128 + 32 * c.w + (c.lane >> 2)) * 128) * 128 + xs) * 2;
#pragma unroll
  for (int row = 0; row < 10; ++row) {
    const int y = row == 0 ? (c.ty == 0 ? 0 : c.y0 - 1) : row == 9 ? (c.ty == 15 ? 127 : c.y0 + 8) : c.y0 - 1 + row;
#pragma unroll
    for (int cbl = 0; cbl < 2; ++cbl)
      __builtin_amdgcn_raw_ptr_buffer_load_lds(c.rx, (LAS void*)(c.smem + L_RAW + row * RS + (2 * c.w + cbl) * 1024), 16, vo, y * 256 + cbl * 16 * 32768, 0, 0);
  }
}
template <int RPAIR>
DI void xraw_step(unsigned ra, unsigned rh, const unsigned (&wa)[4], const unsigned (&wh)[2], bool rowok, bool hok) {
  u32x2 v[6];
  constexpr int RO = (RPAIR & 1) * 2 * RS;
  const unsigned ra2 = ra + (RPAIR >> 1) * 4 * RS, rh2 = rh + (RPAIR >> 1) * 4 * RS;
  asm volatile("s_waitcnt vmcnt(%8)\n\t"
               "ds_read_b64_tr_b16 %0, %6 offset:%9\n\tds_read_b64_tr_b16 %1, %6 offset:%10\n\tds_read_b64_tr_b16 %2, %6 offset:%11\n\tds_read_b64_tr_b16 %3, %6 offset:%12\n\t"
               "ds_read_b64_tr_b16 %4, %7 offset:%9\n\tds_read_b64_tr_b16 %5, %7 offset:%13\n\ts_waitcnt lgkmcnt(0)"
               : "=&v"(v[0]), "=&v"(v[1]), "=&v"(v[2]), "=&v"(v[3]), "=&v"(v[4]), "=&v"(v[5])
               : "v"(ra2), "v"(rh2), "i"(16 - 4 * RPAIR), "i"(RO), "i"(RO + 512), "i"(RO + 1024), "i"(RO + 1536), "i"(RO + 1024) : "memory");
  if (rowok) {
#pragma unroll
    for (int a = 0; a < 4; ++a) asm volatile("ds_write_b64 %0, %1 offset:%2" :: "v"(wa[a]), "v"(v[a]), "i"(RPAIR * 2 * RP) : "memory");
  }
  if (hok) {
#pragma unroll
    for (int hb = 0; hb < 2; ++hb) asm volatile("ds_write_b64 %0, %1 offset:%2" :: "v"(wh[hb]), "v"(v[4 + hb]), "i"(RPAIR * 2 * RP) : "memory");
  }
}
DI void xraw_transpose(const Ctx& c) {
  const unsigned lb = (unsigned)(size_t)(LAS char*)c.smem;
  const int gsel = c.lane >> 4, i = c.lane & 15, qp = i >> 2, p = i & 3;
  const unsigned ra = lb + L_RAW + (gsel & 1) * RS + (4 * (8 * c.w + (gsel >> 1)) + qp) * 64 + 16 + p * 8;
  unsigned wa[4];
#pragma unroll
  for (int a = 0; a < 4; ++a) wa[a] = lb + L_T + (gsel & 1) * RP + (i + 1) * 256 + (((4 * c.w + a) ^ swz((i + 1) & 15)) << 4) + (gsel >> 1) * 8;
  const unsigned rh = lb + L_RAW + (p >> 1) * RS + (4 * (8 * c.w + gsel) + qp) * 64 + ((p & 1) ? 48 : 8);
  const int side = qp & 1, hrow = qp >> 1, hcol = side ? 17 : 0;
  unsigned wh[2];
#pragma unroll
  for (int hb = 0; hb < 2; ++hb) wh[hb] = lb + L_T + hrow * RP + hcol * 256 + (((4 * c.w + 2 * hb + (gsel >> 1)) ^ swz(hcol & 15)) << 4) + (gsel & 1) * 8;
  const bool huse = (side ? p == 0 : p == 3) && (side ? (c.tx != 7 && c.tx != 3) : (c.tx != 0 && c.tx != 4));
  const bool top = c.ty == 0, bot = c.ty == 15;
  xraw_step<0>(ra, rh, wa, wh, !(top && (gsel & 1) == 0), huse && !(top && hrow == 0));
  xraw_step<1>(ra, rh, wa, wh, true, huse);
  xraw_step<2>(ra, rh, wa, wh, true, huse);
  xraw_step<3>(ra, rh, wa, wh, true, huse);
  xraw_step<4>(ra, rh, wa, wh, !(bot && (gsel & 1) == 1), huse && !(bot && hrow == 1));
}
struct Halo { unsigned go[4], lo[4]; bool inr[4], valid[4]; };
DI void halo_init(const Ctx& c, Halo& h) {
  const int tid = c.t - 256, cg = tid & 15, i = tid >> 4;
#pragma unroll
  for (int r = 0; r < 4; ++r) {
    const int row = r == 0 ? 0 : r == 1 ? 9 : r == 2 ? 1 + (i & 7) : (i >> 1) * 9;
    const int col = r < 2 ? i : r == 2 ? (i >> 3) * 17 : 16 + (i & 1);
    h.inr[r] = r < 3 ? true : i < 4;
    h.valid[r] = h.inr[r] & in_image(c, row, col);
    h.go[r] = WS_HB + (unsigned)(((c.b * 128 + c.y0 - 1 + row) * 128 + c.x0 - 1 + col) * 256 + cg * 16); PIN(h.go[r]);
    h.lo[r] = L_T + row * RP + col * 256 + ((cg ^ swz(col & 15)) << 4); PIN(h.lo[r]);
  }
}
DI void mark_borders(const Ctx& c) {
  const int tid = c.t - 256, cg = tid & 15, i = tid >> 4, j = i < 12 ? i : 11;
  unsigned o[3];
#pragma unroll
  for (int r = 0; r < 3; ++r) {
    const int ry = r == 0 ? 0 : r == 1 ? 7 : 1 + (j >> 1), rx = r < 2 ? i : (j & 1) * 15;
    o[r] = WS_HB + (unsigned)(((c.b * 128 + c.y0 + ry) * 128 + c.x0 + rx) * 256 + cg * 16); PIN(o[r]);
  }
#pragma unroll
  for (int r = 0; r < 3; ++r) bst_sc1(u32x4{~0u, ~0u, ~0u, ~0u}, c.rw, o[r], 0);
}
DI void stats_to_lds(const Ctx& c, int pw, const u32x4& v0, const u32x4& v1) {
  const float s = quad_lane_sum(__uint_as_float(v0[0]) + __uint_as_float(v1[0])), q = quad_lane_sum(__uint_as_float(v0[2]) + __uint_as_float(v1[2]));
  if (c.lane < 4) *(f32x2*)(c.smem + L_STAT + pw * 32 + c.lane * 8) = f32x2{s, q};
}

DI u32x4 pack_row(const f32x4 (&a)[2]) {
  const unsigned d0 = cvtpk(a[0][0], a[0][1]), d1 = cvtpk(a[0][2], a[0][3]), d2 = cvtpk(a[1][0], a[1][1]), d3 = cvtpk(a[1][2], a[1][3]);
  auto p = __builtin_amdgcn_permlane16_swap(d0, d2, false, false);
  auto q = __builtin_amdgcn_permlane16_swap(d1, d3, false, false);
  return u32x4{p[0], q[0], p[1], q[1]};
}
DI void out_row(const Ctx& c, const f32x4 (&a)[2], int r) { __builtin_amdgcn_raw_buffer_store_b128(pack_row(a), c.ro, c.oo + r * 256, 0, 0); }
template <int CONV>
DI void mainloop(const Ctx& c, f32x4 (&acc)[8][2], bf16x8 (&wr)[WD], unsigned wvoff, f32x4 (&emb)[2], unsigned eo) {
  const char* T = c.smem + L_T;
  auto rd = [&](int B, int r) -> bf16x8 {
    const int sp = B / 3, ty = B % 3;
    const unsigned a = ty == 2 ? (c.baseD ^ (sp << 6)) : (c.baseP ^ ((2 * sp + ty) << 5));
    return *(const bf16x8*)(T + a + r * RP);
  };
  bf16x8 xf[4];
#pragma unroll
  for (int r = 0; r < 3; ++r) xf[r] = rd(0, r);
#pragma unroll
  for (int B = 0; B < 12; ++B) {
#pragma unroll
    for (int r = 0; r < 10; ++r) {
      const int G = B * 10 + r;
      if (r == 7 && (B % 3) == 2 && B < 11) { bar_nowait(); SB(); }
      { const int Gn = G + 3; if (Gn < 120) xf[Gn & 3] = rd(Gn / 10, Gn % 10); }
      if (CONV == 0 && B == 9 && r == 0) { emb[0] = *(const f32x4*)(c.smem + eo); emb[1] = *(const f32x4*)(c.smem + eo + 64); }
      if (CONV == 0 && B == 0 && r >= 1 && r < 7) {
        wr[4 + 2 * r] = __builtin_bit_cast(bf16x8, bld_sc1(c.rw, wvoff, (unsigned)(4 + 2 * r) * 1024));
        wr[5 + 2 * r] = __builtin_bit_cast(bf16x8, bld_sc1(c.rw, wvoff, (unsigned)(5 + 2 * r) * 1024)); SB();
      }
#pragma unroll
      for (int dy = 0; dy < 3; ++dy) {
        const int row = r - dy;
        if (row >= 0 && row < 8) {
#pragma unroll
          for (int nt = 0; nt < 2; ++nt) {
            const int ts = 6 * B + 2 * dy + nt;
            if (CONV == 1) asm volatile("v_mfma_f32_16x16x32_bf16 %0, %1, %2, %0" : "+v"(acc[row][nt]) : "v"(xf[G & 3]), "v"(wr[ts % WD]));
            else if (B == 0 && dy == 0) asm volatile("v_mfma_f32_16x16x32_bf16 %0, %1, %2, 0" : "=&v"(acc[row][nt]) : "v"(wr[ts % WD]), "v"(xf[G & 3]));
            else           asm volatile("v_mfma_f32_16x16x32_bf16 %0, %1, %2, %0" : "+v"(acc[row][nt]) : "v"(wr[ts % WD]), "v"(xf[G & 3]));
            if (row == 7) {
              const int tn = CONV * 72 + ts + WD;
              if (tn < 144) wr[ts % WD] = __builtin_bit_cast(bf16x8, bld_sc1(c.rw, wvoff, (unsigned)tn * 1024 + (tn >= 72 ? 294912u - 72u * 1024u : 0u)));
            }
            SB();
          }
        }
      }
      if (CONV == 1 && B == 11 && r >= 4) { out_row(c, acc[r - 4], r - 4); SB(); }
    }
  }
  asm volatile("s_nop 15" ::: "memory");
  SB();
}

__global__ __launch_bounds__(512, 2) void k_resblock(const u16* __restrict__ x, const u16* __restrict__ t_emb, const u16* __restrict__ g1, const u16* __restrict__ be1,
                                                     const u16* __restrict__ w1, const u16* __restrict__ w_emb, const u16* __restrict__ g2, const u16* __restrict__ be2,
                                                     const u16* __restrict__ w2, u16* __restrict__ out, char* ws) {
  extern __shared__ __attribute__((aligned(256))) char smem[];
  Ctx c;
  c.smem = smem; c.ws = ws;
  c.t = threadIdx.x; c.lane = c.t & 63; c.w = __builtin_amdgcn_readfirstlane(c.t >> 6);
  const int xcd = blockIdx.x & 7, jx = blockIdx.x >> 3;
  c.b = xcd >> 2; c.ty = (xcd & 3) * 4 + (jx >> 3); c.tx = jx & 7;
  c.jb = c.ty * 8 + c.tx; c.y0 = c.ty * 8; c.x0 = c.tx * 16;
  c.rx = mk_rsrc(x, 8u << 20); c.rw = mk_rsrc(ws, 16u << 20);
  c.wb = (unsigned)c.b * WB_SIZE;
  unsigned code_pf;
  { unsigned long long pc; asm volatile("s_getpc_b64 %0" : "=s"(pc));
    code_pf = __builtin_amdgcn_raw_buffer_load_b32(mk_rsrc((const void*)(pc & ~255ull), CODE_PF_BYTES), (unsigned)(jx * 10 + (c.lane < 10 ? c.lane : 0)) * 128u, 0, 0); }

  if (c.w < 4) {
    const int ch = c.t & 127;
    const unsigned gbA = (c.t < 128 ? g1 : be1)[ch], gbB = (c.t < 128 ? g2 : be2)[ch];
    SB();
    bar_nowait(); SB();
    xraw_issue(c); SB();
    Tasks t0; tasks_init(c, t0, c.t >> 3, c.t & 7);
    xraw_transpose(c); SB();
    asm volatile("" :: "v"(code_pf));
    bar_nowait(); SB();
    c.tag = __builtin_amdgcn_readfirstlane(*(const unsigned*)(smem + L_CNT + 8));
    { float* gbp = (float*)(smem + L_GB) + (c.t < 128 ? 0 : 128) + ch; gbp[0] = bflo(gbA); gbp[256] = bflo(gbB); }
    {
      const unsigned fo = c.wb + WB_FLAG + (unsigned)(((c.lane >> 3) & 1) * 32 + c.w * 8 + (c.lane & 7)) * 64;
      for (int spin = 0; spin < SPIN_MAX; ++spin) {
        cbar();
        const unsigned f = __builtin_amdgcn_raw_buffer_load_b32(c.rw, fo, 0, SC1);
        if (!__any(f != c.tag)) break;
        __builtin_amdgcn_s_sleep(2);
      }
    }
    const unsigned wvoff = c.wb + WB_WF + (unsigned)c.w * 73728u + (unsigned)c.lane * 16u;
    bf16x8 wr[WD];
#pragma unroll
    for (int k = 0; k < 6; ++k) wr[k] = __builtin_bit_cast(bf16x8, bld_sc1(c.rw, wvoff, (unsigned)k * 1024));
    const int px = c.lane & 15, q = c.lane >> 4;
    { const int colP = px + (q & 1), colD = px + 2;
      c.baseP = colP * 256 + (((q >> 1) ^ swz(colP & 15)) << 4); c.baseD = colD * 256 + ((q ^ swz(colD & 15)) << 4); PIN(c.baseP); PIN(c.baseD); }
    f32x4 acc[8][2], emb[2];
    bar();
    norm_group0(c, t0, 0);
    bar();
    __builtin_amdgcn_s_setprio(1);
    mainloop<0>(c, acc, wr, wvoff, emb, L_EMB + (32 * c.w + 4 * q) * 4);
    __builtin_amdgcn_s_setprio(0);
    {
      f32x2 s2 = {0.f, 0.f}, q2 = {0.f, 0.f};
#pragma unroll
      for (int r = 0; r < 8; ++r) { acc[r][0] += emb[0]; acc[r][1] += emb[1]; }
#pragma unroll
      for (int r = 0; r < 8; ++r)
#pragma unroll
        for (int nt = 0; nt < 2; ++nt)
#pragma unroll
          for (int k = 0; k < 2; ++k) { const f32x2 v = {acc[r][nt][2 * k], acc[r][nt][2 * k + 1]}; s2 += v; q2 += v * v; }
      const float s = wave_sum(s2[0] + s2[1]), qs = wave_sum(q2[0] + q2[1]);
      if (c.lane == 0) *(f32x2*)(smem + L_RED + c.w * 8) = f32x2{s, qs};
    }
    bar();
    if (c.t < 4) { const f32x2 p = *(const f32x2*)(smem + L_RED + c.lane * 8);
      bst_sc1(u32x4{__float_as_uint(p[0]), c.tag, __float_as_uint(p[1]), c.tag}, c.rw, c.wb + WB_G2 + (unsigned)(c.jb * 4 + c.lane) * 16, 0); }
    {
      const int chk = 4 * c.w + (((q & 1) << 1) | (q >> 1));
      const unsigned go = WS_HB + (unsigned)(((c.b * 128 + c.y0) * 128 + c.x0 + px) * 256 + chk * 16);
      char* lp = smem + L_T + RP + (px + 1) * 256 + ((chk ^ swz((px + 1) & 15)) << 4);
      const bool edge = px == 0 || px == 15;
#pragma unroll
      for (int rr = 0; rr < 8; ++rr) {
        const int r = rr == 0 ? 0 : (rr == 1 ? 7 : rr - 1);
        const u32x4 v = pack_row(acc[r]);
        if (r == 0 || r == 7 || edge) bst_sc1(__builtin_elementwise_min(v, u32x4{0xfffffffeu, 0xfffffffeu, 0xfffffffeu, 0xfffffffeu}), c.rw, go, r * 32768);
        *(u32x4*)(lp + r * RP) = v;
      }
    }
    bar();
#pragma unroll
    for (int r = 0; r < 8; ++r) {
      const u32x4 v = *(const u32x4*)(smem + L_RAW + (r + 1) * RS + (32 * c.w + 16 * (q & 1) + px) * 64 + 16 + (q >> 1) * 16);
      auto a = __builtin_amdgcn_permlane16_swap(v[0], v[2], false, false);
      auto e = __builtin_amdgcn_permlane16_swap(v[1], v[3], false, false);
      acc[r][0] = f32x4{bflo(a[0]), bfhi(a[0]), bflo(e[0]), bfhi(e[0])};
      acc[r][1] = f32x4{bflo(a[1]), bfhi(a[1]), bflo(e[1]), bfhi(e[1])};
    }
#pragma unroll
    for (int r = 0; r < 8; ++r) asm volatile("" : "+v"(acc[r][0]), "+v"(acc[r][1]));
    bar();
    norm_group0(c, t0, 1);
    bar();
    c.ro = mk_rsrc(out, 8u << 20);
    c.oo = (unsigned)(((c.b * 128 + 32 * c.w + 16 * (q & 1) + px) * 128 + c.y0) * 128 + c.x0 + 8 * (q >> 1)) * 2; PIN(c.oo);
    __builtin_amdgcn_s_setprio(1);
    mainloop<1>(c, acc, wr, wvoff, emb, 0);
    __builtin_amdgcn_s_setprio(0);
    out_row(c, acc[6], 6); out_row(c, acc[7], 7);
  } else {
    const int g = c.w - 4;
    const int yrow = (xcd & 3) * 32 + jx;
    __builtin_amdgcn_s_setprio(2);
    const unsigned epoch = __hip_atomic_load((GAS unsigned*)(ws + c.wb + WB_EPOCH), __ATOMIC_RELAXED, __HIP_MEMORY_SCOPE_AGENT);
    const int ksvc = jx * 4 + (xcd & 3);
    const bool svc_w = c.w == 7 && (ksvc < 32 || (ksvc >= 64 && ksvc < 96)), svc_e = c.w == 7 && ksvc >= 32 && ksvc < 64;
    const int wu = ksvc < 32 ? ksvc : ksvc - 32;
    u32x4 sv[9]; unsigned te[6];
    if (svc_w) {
      const u32x4* src = (const u32x4*)(((wu >> 5) ? w2 : w1) + (size_t)((32 * ((wu >> 3) & 3) + (c.lane & 31)) * 128 + 16 * (wu & 7) + 8 * (c.lane >> 5)) * 9);
#pragma unroll
      for (int k = 0; k < 9; ++k) sv[k] = src[k];
    }
    if (svc_e) {
      const int j = ksvc - 32, e0 = 192 * (j >> 3) + (c.lane >> 1);
#pragma unroll
      for (int k = 0; k < 6; ++k) { sv[k] = *(const u32x4*)(w_emb + (size_t)(e0 + 32 * k) * 128 + 16 * (j & 7) + 8 * (c.lane & 1)); te[k] = t_emb[c.b * 768 + e0 + 32 * k]; }
    }
    SB();
    u32x4 xr[8];
    { const unsigned o = (unsigned)(((c.b * 128 + 32 * g + 8 * (c.lane >> 4)) * 128 + yrow) * 128) * 2 + (c.lane & 15) * 16;
#pragma unroll
      for (int jj = 0; jj < 8; ++jj) xr[jj] = bld(c.rx, o, jj * 32768); }
    SB();
    if (c.t == 256) *(u32x2*)(smem + L_CNT) = u32x2{0u, 0u};
    bar(); SB();
    c.tag = __builtin_amdgcn_readfirstlane(epoch) + 1u;
    if (c.t == 256) *(unsigned*)(smem + L_CNT + 8) = c.tag;
    {
      float s = 0.f, qs = 0.f;
#pragma unroll
      for (int jj = 0; jj < 8; ++jj)
#pragma unroll
        for (int k = 0; k < 4; ++k) { s = dot2(xr[jj][k], 0x3f803f80u, s); qs = dot2(xr[jj][k], xr[jj][k], qs); }
      s = wave_sum(s); qs = wave_sum(qs);
      if (c.lane == 0) *(f32x2*)(smem + L_RED + g * 8) = f32x2{s, qs};
      unsigned old = 0;
      asm volatile("s_waitcnt lgkmcnt(0)" ::: "memory");
      if (c.lane == 0) old = __hip_atomic_fetch_add((LAS unsigned*)(smem + L_CNT), 1u, __ATOMIC_RELAXED, __HIP_MEMORY_SCOPE_WORKGROUP);
      if (__builtin_amdgcn_readfirstlane(old) == 3u) {
        cbar();
        if (c.lane < 4) { const f32x2 p = *(const f32x2*)(smem + L_RED + c.lane * 8);
          bst_sc1(u32x4{__float_as_uint(p[0]), c.tag, __float_as_uint(p[1]), c.tag}, c.rw, c.wb + WB_G1 + (unsigned)(c.jb * 4 + c.lane) * 16, 0); }
      }
    }
    {
      const int k16 = c.lane & 15;
      if (k16 == 7 || k16 == 8) {
        unsigned v[4];
#pragma unroll
        for (int m = 0; m < 4; ++m) {
          const unsigned a = __builtin_amdgcn_perm(xr[2 * m + 1][3], xr[2 * m][3], 0x07060302u), b2 = __builtin_amdgcn_perm(xr[2 * m + 1][0], xr[2 * m][0], 0x05040100u);
          v[m] = k16 == 7 ? a : b2;
        }
        const unsigned o = c.wb + WB_XC + (unsigned)((yrow * 2 + (k16 - 7)) * 64 + 16 * g + 4 * (c.lane >> 4)) * 8;
        bst_sc1(u32x4{v[0], c.tag, v[1], c.tag}, c.rw, o, 0); bst_sc1(u32x4{v[2], c.tag, v[3], c.tag}, c.rw, o, 16);
      }
    }
    asm volatile("" :: "v"(code_pf));
    if (svc_w) {
      const int cs = wu & 7, o = c.lane & 31, h = c.lane >> 5;
      const unsigned dst = c.wb + WB_WF + (unsigned)(wu >> 5) * 294912u + (unsigned)((wu >> 3) & 3) * 73728u + (unsigned)(o >> 4) * 1024u + (unsigned)(o & 15) * 16u;
      const unsigned dP = dst + (unsigned)(3 * (cs >> 1) + (cs & 1)) * 6144u + (unsigned)h * 512u;
      const unsigned dD = dst + (unsigned)(3 * (cs >> 1) + 2) * 6144u + (unsigned)(2 * (cs & 1) + h) * 256u;
#pragma unroll
      for (int k = 0; k < 9; ++k) {
        const int dy = k / 3, dx = k % 3;
        u32x4 o4;
#pragma unroll
        for (int m = 0; m < 4; ++m) {
          const int e0 = 18 * m + k, e1 = e0 + 9;
          const unsigned sel = ((e0 & 1) ? 0x0302u : 0x0100u) | ((e1 & 1) ? 0x07060000u : 0x05040000u);
          o4[m] = __builtin_amdgcn_perm(sv[e1 >> 3][(e1 >> 1) & 3], sv[e0 >> 3][(e0 >> 1) & 3], sel);
        }
        bst_sc1(o4, c.rw, dx < 2 ? dP : dD, dy * 2048 + (dx < 2 ? dx * 256 : 0));
      }
      asm volatile("s_waitcnt vmcnt(0)" ::: "memory");
      if (c.lane == 0) flag_store(ws + c.wb + WB_FLAG + wu * 64, c.tag);
    }
    if (svc_e) {
      f32x2 a[4];
#pragma unroll
      for (int m = 0; m < 4; ++m) a[m] = f32x2{0.f, 0.f};
#pragma unroll
      for (int k = 0; k < 6; ++k) {
        const float tv = bflo(te[k]);
        const float sl = tv * __builtin_amdgcn_rcpf(1.f + __builtin_amdgcn_exp2f(-tv * LOG2E));
#pragma unroll
        for (int m = 0; m < 4; ++m) a[m] += f32x2{bflo(sv[k][m]), bfhi(sv[k][m])} * sl;
      }
      float r[8];
#pragma unroll
      for (int m = 0; m < 8; ++m) r[m] = rows_sum(dppadd<0x128>(dppadd<0x124>(dppadd<0x4E>(a[m >> 1][m & 1]))));
      const int j = ksvc - 32;
      if (c.lane < 2) {
        const unsigned o = c.wb + WB_EG + (unsigned)((j >> 3) * 128 + 16 * (j & 7) + 8 * c.lane) * 8;
#pragma unroll
        for (int m = 0; m < 4; ++m) bst_sc1(u32x4{__float_as_uint(r[2 * m]), c.tag, __float_as_uint(r[2 * m + 1]), c.tag}, c.rw, o, m * 16);
      }
    }
    __builtin_amdgcn_s_setprio(0);
    if ((c.ty == 0) | (c.ty == 15) | (c.tx == 0) | (c.tx == 7)) {
      Halo hz; halo_init(c, hz);
#pragma unroll
      for (int r = 0; r < 4; ++r) if (hz.inr[r] && !hz.valid[r]) *(u32x4*)(smem + hz.lo[r]) = u32x4{0, 0, 0, 0};
    }
    Tasks t0; tasks_init(c, t0, c.t >> 3, c.t & 7);
    Tasks ht; tasks_init(c, ht, (c.t - 256) >> 2, 2 * (c.t & 3));
    bar(); SB();
    {
      u32x4 v0, v1, vc[2]; bool n0 = true, n1 = true, nc[2];
      const unsigned o = c.wb + WB_G1 + (unsigned)(g * 128 + c.lane) * 16;
      const bool xc = c.tx == 3 || c.tx == 4;
      unsigned co[2], cl[2]; bool cin[2];
#pragma unroll
      for (int r = 0; r < 2; ++r) {
        const int id = c.t - 256 + 256 * r, row = id >> 5, i = id & 31, y = c.y0 - 1 + row, col = c.tx == 3 ? 17 : 0;
        cin[r] = xc && id < 320;
        nc[r] = cin[r] && (unsigned)y < 128u;
        co[r] = c.wb + WB_XC + (unsigned)((y * 2 + (c.tx == 3 ? 1 : 0)) * 64 + 2 * i) * 8;
        cl[r] = L_T + row * RP + col * 256 + (((i >> 1) ^ swz(col & 15)) << 4) + (i & 1) * 8;
        vc[r] = u32x4{0, 0, 0, 0};
      }
      for (int spin = 0; spin < SPIN_MAX; ++spin) {
        cbar();
        if (n0) v0 = bld_sc1(c.rw, o, 0);
        if (n1) v1 = bld_sc1(c.rw, o, 1024);
#pragma unroll
        for (int r = 0; r < 2; ++r) if (nc[r]) vc[r] = bld_sc1(c.rw, co[r], 0);
        n0 = !tagged(v0, c.tag); n1 = !tagged(v1, c.tag);
#pragma unroll
        for (int r = 0; r < 2; ++r) nc[r] = nc[r] && !tagged(vc[r], c.tag);
        if (!__any(n0 || n1 || nc[0] || nc[1])) break;
        __builtin_amdgcn_s_sleep(2);
      }
      stats_to_lds(c, g, v0, v1);
#pragma unroll
      for (int r = 0; r < 2; ++r) if (cin[r]) *(u32x2*)(smem + cl[r]) = u32x2{vc[r][0], vc[r][2]};
    }
    bar();
    if (c.t == 256 && c.jb == 0) flag_store(ws + c.wb + WB_EPOCH, c.tag);
    norm_group0(c, t0, 0);
    bar();
#pragma unroll 1
    for (int k = 1; k < 4; ++k) {
      u32x4 ve[4];
      if (k == 3 && c.w == 4) {
#pragma unroll
        for (int kk = 0; kk < 4; ++kk) ve[kk] = bld_sc1(c.rw, c.wb + WB_EG + (unsigned)c.lane * 16, kk * 1024);
      }
      norm_group_h(c, ht, k);
      if (k == 3 && c.w == 4) {
        for (int spin = 0; spin < SPIN_MAX; ++spin) {
          bool ok = true;
#pragma unroll
          for (int kk = 0; kk < 4; ++kk) ok = ok & (ve[kk][1] == c.tag) & (ve[kk][3] == c.tag);
          if (!__any(!ok)) break;
          cbar();
#pragma unroll
          for (int kk = 0; kk < 4; ++kk) ve[kk] = bld_sc1(c.rw, c.wb + WB_EG + (unsigned)c.lane * 16, kk * 1024);
        }
        *(f32x2*)(smem + L_EMB + c.lane * 8) = f32x2{(__uint_as_float(ve[0][0]) + __uint_as_float(ve[1][0])) + (__uint_as_float(ve[2][0]) + __uint_as_float(ve[3][0])),
                                                    (__uint_as_float(ve[0][2]) + __uint_as_float(ve[1][2])) + (__uint_as_float(ve[2][2]) + __uint_as_float(ve[3][2]))};
      }
      bar();
    }
    {
      {
        mark_borders(c);
        asm volatile("s_waitcnt vmcnt(0)" ::: "memory");
        unsigned old = 0;
        if (c.lane == 0) old = __hip_atomic_fetch_add((LAS unsigned*)(smem + L_CNT + 4), 1u, __ATOMIC_RELAXED, __HIP_MEMORY_SCOPE_WORKGROUP);
        if (__builtin_amdgcn_readfirstlane(old) == 3u && c.lane == 0) flag_store(ws + c.wb + WB_CF + c.jb * 64, c.tag);
      }
      Halo hl; halo_init(c, hl);
      u32x4 hv[4]; bool need[4];
#pragma unroll
      for (int r = 0; r < 4; ++r) { need[r] = hl.valid[r]; hv[r] = u32x4{0, 0, 0, 0}; }
      u32x4 v0, v1; bool n0 = true, n1 = true;
      const unsigned o = c.wb + WB_G2 + (unsigned)(g * 128 + c.lane) * 16;
      bar_nowait();
      if (c.w == 5) {
        const int dy = (c.lane & 15) / 3 - 1, dx = (c.lane & 15) % 3 - 1;
        const int ny = c.ty + dy, nx = c.tx + dx;
        const bool chk = c.lane < 9 && (unsigned)ny < 16u && (unsigned)nx < 8u;
        for (int spin = 0; spin < SPIN_MAX; ++spin) {
          cbar();
          unsigned f = c.tag;
          if (chk) f = __builtin_amdgcn_raw_buffer_load_b32(c.rw, c.wb + WB_CF + (unsigned)(ny * 8 + nx) * 64, 0, SC1);
          if (!__any(f != c.tag)) break;
          __builtin_amdgcn_s_sleep(2);
        }
      }
      bar();
      for (int spin = 0; spin < SPIN_MAX; ++spin) {
        cbar();
#pragma unroll
        for (int r = 0; r < 4; ++r) if (need[r]) hv[r] = bld_sc1(c.rw, hl.go[r], 0);
        if (n0) v0 = bld_sc1(c.rw, o, 0);
        if (n1) v1 = bld_sc1(c.rw, o, 1024);
        bool any = false;
#pragma unroll
        for (int r = 0; r < 4; ++r) { need[r] = need[r] && (hv[r][0] == ~0u || hv[r][1] == ~0u || hv[r][2] == ~0u || hv[r][3] == ~0u); any |= need[r]; }
        n0 = !tagged(v0, c.tag); n1 = !tagged(v1, c.tag);
        if (!__any(any || n0 || n1)) break;
        __builtin_amdgcn_s_sleep(1);
      }
#pragma unroll
      for (int r = 0; r < 4; ++r) if (hl.valid[r]) *(u32x4*)(smem + hl.lo[r]) = hv[r];
      stats_to_lds(c, g, v0, v1);
    }
    bar();
    norm_group0(c, t0, 1);
    bar();
#pragma unroll 1
    for (int k = 1; k < 4; ++k) { norm_group_h(c, ht, k); bar(); }
  }
}

extern "C" void kernel_launch(void* const* d_in, const int* in_sizes, int n_in, void* d_out, int out_size, void* d_ws, size_t ws_size, hipStream_t stream) {
  (void)hipFuncSetAttribute((const void*)k_resblock, hipFuncAttributeMaxDynamicSharedMemorySize, L_END);
  k_resblock<<<256, 512, L_END, stream>>>((const u16*)d_in[0], (const u16*)d_in[1], (const u16*)d_in[2], (const u16*)d_in[3], (const u16*)d_in[4],
                                          (const u16*)d_in[5], (const u16*)d_in[6], (const u16*)d_in[7], (const u16*)d_in[8], (u16*)d_out, (char*)d_ws);
}
```
